```python
import torch

def _softmax(x, axis=-1):
    return torch.softmax(x, dim=axis)
def _gelu(x):
    return torch.nn.functional.gelu(x, approximate="tanh")
def _silu(x):
    return torch.nn.functional.silu(x)
def _sigmoid(x):
    return torch.sigmoid(x)
def _layernorm(x, axis=-1, eps=1e-5):
    m = x.mean(dim=axis, keepdim=True)
    v = x.var(dim=axis, keepdim=True, unbiased=False)
    return (x - m) / torch.sqrt(v + eps)
def _scatter_add(tgt, idx, src, axis=0):
    return tgt.clone().index_add_(axis, idx, src)
def _segment_sum(src, seg, n):
    out = torch.zeros((n,) + src.shape[1:], dtype=src.dtype)
    return out.index_add_(0, seg, src)
def _pad(x, pw):
    flat = tuple(v for pair in reversed(pw) for v in pair)
    return torch.nn.functional.pad(x, flat)

kernel_name = 'recurrent_scan__mha_block_cached__glu_gated_glu__prompt'

def setup_inputs():

    _w = (lambda a: (lambda t: t.to(torch.bfloat16) if t.is_floating_point() else t)(torch.as_tensor(a)))
    def _A_setup():
        B, Sq, D = 1, 2048, 448
        H, Sk, Dh = 16, 2048, 256
        return {
            "x": _w((lambda *s: torch.randn(*s, dtype=torch.bfloat16) * 0.05)(B, Sq, D)),
            "k_cache": _w((lambda *s: torch.randn(*s, dtype=torch.bfloat16) * 1.6)(B, H, Sk, Dh)),
            "v_cache": _w((lambda *s: torch.randn(*s, dtype=torch.bfloat16) * 0.2)(B, H, Sk, Dh)),
            "wq": _w((lambda *s: torch.randn(*s, dtype=torch.bfloat16) * 1.6)(D, H * Dh) * (D ** -0.5)),
            "wo": _w((lambda *s: torch.randn(*s, dtype=torch.bfloat16) * 0.2)(H * Dh, D) * ((H * Dh) ** -0.5)),
        }
    _A_in = _A_setup()

    def _B_setup():
        B, S, D, F = 1, 2048, 448, 1792
        return {
            "x": _w((lambda *s: torch.randn(*s, dtype=torch.bfloat16) * 0.05)(B, S, D)),
            "w_gate": _w((lambda *s: torch.randn(*s, dtype=torch.bfloat16) * 0.2)(D, F)),
            "w_up": _w((lambda *s: torch.randn(*s, dtype=torch.bfloat16) * 0.1)(D, F)),
            "w_down": _w((lambda *s: torch.randn(*s, dtype=torch.bfloat16) * 0.1)(F, D)),
        }
    _B_in = _B_setup()

    return {
        "A_x": _A_in["x"],
        "A_k_cache": _A_in["k_cache"],
        "A_v_cache": _A_in["v_cache"],
        "A_wq": _A_in["wq"],
        "A_wo": _A_in["wo"],
        "B_x": _B_in["x"],
        "B_w_gate": _B_in["w_gate"],
        "B_w_up": _B_in["w_up"],
        "B_w_down": _B_in["w_down"],
    }

def reference(A_x, A_k_cache, A_v_cache, A_wq, A_wo, B_x, B_w_gate, B_w_up, B_w_down):
    def _A_ref(x, k_cache, v_cache, wq, wo):
        B, Sq, D = x.shape
        H, Dh = 16, 256
        q = (lambda x, *d: x.permute(*d))((x @ wq).reshape(B, Sq, H, Dh), 0, 2, 1, 3)
        scores = torch.einsum("bhqd,bhkd->bhqk", q, k_cache) * (Dh ** -0.5)
        p = _softmax(scores, axis=-1)
        o = torch.einsum("bhqk,bhkd->bhqd", p, v_cache)
        return (lambda x, *d: x.permute(*d))(o, 0, 2, 1, 3).reshape(B, Sq, H * Dh) @ wo

    def _B_ref(x, w_gate, w_up, w_down):
        g = torch.einsum("bsd,df->bsf", x, w_gate)
        u = torch.einsum("bsd,df->bsf", x, w_up)
        h = _sigmoid(g) * u
        return torch.einsum("bsf,fd->bsd", h, w_down)

    x = A_x
    xn = _layernorm(x, axis=-1)
    s = _A_ref(xn, A_k_cache, A_v_cache, A_wq, A_wo)
    g = _silu(_B_ref(xn, B_w_gate, B_w_up, B_w_down))
    return x + s * g
```

```cpp
#include <hip/hip_runtime.h>
#include <hip/hip_bf16.h>

__global__ __launch_bounds__(256,1) void fused_block_kernel(const void* x, const void* k, const void* v, const void* wq, const void* wo, const void* wg, const void* wu, const void* wd, void* out, void* ws) {
  __shared__ __attribute__((aligned(16))) char lds[163840];
  unsigned wid = __builtin_amdgcn_readfirstlane(threadIdx.x>>6);
  asm volatile(
    "s_load_dwordx16 s[8:23], %0, 0x0\n"
    "s_load_dwordx4 s[24:27], %0, 0x40\n"
    "v_mbcnt_lo_u32_b32 v255, -1, 0\n"
    "v_mbcnt_hi_u32_b32 v255, -1, v255\n"
    "s_getpc_b64 s[72:73]\n"
    "K1PC0:\n"
    "s_add_u32 s72, s72, K1PRO-K1PC0\n"
    "s_addc_u32 s73, s73, 0\n"
    "s_and_b32 s73, s73, 0xffff\n"
    "s_mov_b32 s74, K1END-K1PRO\n"
    "s_mov_b32 s75, 0x20000\n"
    "s_lshr_b32 s76, %1, 3\n"
    "s_lshl_b32 s76, s76, 2\n"
    "s_add_u32 s76, s76, %2\n"
    "s_lshl_b32 s76, s76, 10\n"
    "v_and_b32 v126, 7, v255\n"
    "v_lshlrev_b32 v126, 7, v126\n"
    "buffer_load_dword v126, v126, s[72:75], s76 offen offset:0\n"
    "K1PRO:\n"
    "s_and_b32 s28, %1, 7\n"
    "s_lshl_b32 s28, s28, 1\n"
    "s_lshr_b32 s31, %1, 7\n"
    "s_add_u32 s28, s28, s31\n"
    "s_bfe_u32 s29, %1, 0x40003\n"
    "s_mov_b32 s30, %2\n"
    "v_lshrrev_b32 v112, 2, v255\n"
    "v_mul_u32_u24 v115, 896, v112\n"
    "v_bfe_u32 v112, v255, 4, 1\n"
    "v_mul_u32_u24 v112, 3, v112\n"
    "v_and_b32 v113, 3, v255\n"
    "v_xor_b32 v112, v112, v113\n"
    "v_lshl_add_u32 v115, v112, 4, v115\n"
    "s_lshl_b32 s56, s29, 7\n"
    "s_lshl_b32 s57, s30, 5\n"
    "s_add_u32 s56, s56, s57\n"
    "s_mul_i32 s96, s56, 896\n"
    "s_add_u32 s60, s96, 14336\n"
    "s_waitcnt lgkmcnt(0)\n"
    "s_mov_b32 s72, s8\n"
    "s_and_b32 s73, s9, 65535\n"
    "s_mov_b32 s74, 1835008\n"
    "s_mov_b32 s75, 131072\n"
    "buffer_load_dwordx4 v[0:3], v115, s[72:75], s96 offen offset:0\n"
    "buffer_load_dwordx4 v[4:7], v115, s[72:75], s96 offen offset:64\n"
    "buffer_load_dwordx4 v[8:11], v115, s[72:75], s96 offen offset:128\n"
    "buffer_load_dwordx4 v[12:15], v115, s[72:75], s96 offen offset:192\n"
    "buffer_load_dwordx4 v[16:19], v115, s[72:75], s96 offen offset:256\n"
    "buffer_load_dwordx4 v[20:23], v115, s[72:75], s96 offen offset:320\n"
    "buffer_load_dwordx4 v[24:27], v115, s[72:75], s96 offen offset:384\n"
    "buffer_load_dwordx4 v[28:31], v115, s[72:75], s96 offen offset:448\n"
    "buffer_load_dwordx4 v[32:35], v115, s[72:75], s96 offen offset:512\n"
    "buffer_load_dwordx4 v[36:39], v115, s[72:75], s96 offen offset:576\n"
    "buffer_load_dwordx4 v[40:43], v115, s[72:75], s96 offen offset:640\n"
    "buffer_load_dwordx4 v[44:47], v115, s[72:75], s96 offen offset:704\n"
    "buffer_load_dwordx4 v[48:51], v115, s[72:75], s96 offen offset:768\n"
    "buffer_load_dwordx4 v[52:55], v115, s[72:75], s96 offen offset:832\n"
    "s_add_u32 s56, s26, 50331648\n"
    "s_addc_u32 s57, s27, 0\n"
    "s_load_dwordx2 s[70:71], s[56:57], 0x0\n"
    "s_lshl_b32 s31, s28, 9\n"
    "s_add_u32 s88, s14, s31\n"
    "s_addc_u32 s89, s15, 0\n"
    "s_and_b32 s89, s89, 65535\n"
    "s_mov_b32 s91, 131072\n"
    "s_sub_u32 s90, 3670016, s31\n"
    "s_and_b32 s14, s30, 1\n"
    "s_lshr_b32 s15, s30, 1\n"
    "s_mul_i32 s69, s30, 7168\n"
    "s_mul_i32 s31, s30, 56\n"
    "s_lshl_b32 s58, s14, 5\n"
    "v_bfe_u32 v112, v255, 2, 3\n"
    "v_add_u32 v112, s31, v112\n"
    "v_mul_u32_u24 v249, 8192, v112\n"
    "v_lshrrev_b32 v112, 5, v255\n"
    "v_lshl_add_u32 v249, v112, 6, v249\n"
    "v_and_b32 v113, 3, v255\n"
    "v_lshl_add_u32 v249, v113, 4, v249\n"
    "v_xor_b32 v249, s58, v249\n"
    "v_bfe_u32 v112, v255, 2, 3\n"
    "v_add_u32 v112, s31, v112\n"
    "v_mul_u32_u24 v250, 3584, v112\n"
    "v_lshrrev_b32 v112, 5, v255\n"
    "v_lshl_add_u32 v250, v112, 6, v250\n"
    "v_and_b32 v113, 3, v255\n"
    "v_lshl_add_u32 v250, v113, 4, v250\n"
    "v_xor_b32 v250, s58, v250\n"
    "s_lshl_b32 s31, s28, 7\n"
    "s_add_u32 s76, s18, s31\n"
    "s_addc_u32 s77, s19, 0\n"
    "s_and_b32 s77, s77, 65535\n"
    "s_mov_b32 s79, 131072\n"
    "s_sub_u32 s78, 1605632, s31\n"
    "s_add_u32 s80, s20, s31\n"
    "s_addc_u32 s81, s21, 0\n"
    "s_and_b32 s81, s81, 65535\n"
    "s_mov_b32 s83, 131072\n"
    "s_sub_u32 s82, 1605632, s31\n"
    "s_lshl_b32 s31, s29, 2\n"
    "s_add_u32 s31, s31, s30\n"
    "s_mul_i32 s31, s31, 7\n"
    "v_lshrrev_b32 v112, 2, v255\n"
    "v_add_u32 v112, s31, v112\n"
    "v_and_b32 v113, 3, v255\n"
    "v_lshlrev_b32 v148, 13, v112\n"
    "v_lshl_add_u32 v148, v113, 7, v148\n"
    "v_cmp_gt_u32 vcc, 28, v255\n"
    "v_mov_b32 v114, 0x7fffff00\n"
    "v_mul_u32_u24 v112, 3584, v112\n"
    "s_nop 0\n"
    "v_cndmask_b32 v148, v114, v148, vcc\n"
    "v_cndmask_b32 v112, v114, v112, vcc\n"
    "v_lshl_add_u32 v149, v113, 11, v112\n"
    "v_cmp_lt_u32 vcc, 1, v113\n"
    "s_nop 1\n"
    "v_cndmask_b32 v149, v149, v114, vcc\n"
    "s_mov_b32 s20, s56\n"
    "s_mov_b32 s21, s57\n"
    "s_lshl_b32 s31, s28, 20\n"
    "s_add_u32 s10, s10, s31\n"
    "s_addc_u32 s11, s11, 0\n"
    "s_add_u32 s12, s12, s31\n"
    "s_addc_u32 s13, s13, 0\n"
    "s_add_u32 s36, s10, 30080\n"
    "s_addc_u32 s37, s11, 0\n"
    "s_and_b32 s37, s37, 65535\n"
    "s_mov_b32 s38, 1018496\n"
    "s_mov_b32 s39, 131072\n"
    "s_sub_u32 s40, s12, 2688\n"
    "s_subb_u32 s41, s13, 0\n"
    "s_and_b32 s41, s41, 65535\n"
    "s_mov_b32 s42, 1051264\n"
    "s_mov_b32 s43, 131072\n"
    "s_mov_b32 s44, 2688\n"
    "s_mov_b32 s45, 1792\n"
    "s_mov_b32 s46, 896\n"
    "s_mov_b32 s47, 0\n"
    "s_mov_b32 s48, 6784\n"
    "s_mov_b32 s49, 5888\n"
    "s_mov_b32 s50, 4992\n"
    "s_mov_b32 s51, 4096\n"
    "s_lshl_b32 s52, s30, 13\n"
    "s_add_u32 s53, s52, 65536\n"
    "v_bfe_u32 v112, v255, 2, 3\n"
    "v_mul_u32_u24 v247, 512, v112\n"
    "v_lshrrev_b32 v112, 5, v255\n"
    "v_lshl_add_u32 v247, v112, 6, v247\n"
    "v_bfe_u32 v112, v255, 4, 1\n"
    "v_mul_u32_u24 v112, 3, v112\n"
    "v_and_b32 v113, 3, v255\n"
    "v_xor_b32 v112, v112, v113\n"
    "v_lshl_add_u32 v247, v112, 4, v247\n"
    "v_add_u32 v247, s52, v247\n"
    "s_cmp_lt_u32 s29, 8\n"
    "s_cselect_b32 s64, s10, s12\n"
    "s_cselect_b32 s65, s11, s13\n"
    "s_cselect_b32 s68, 3, 2\n"
    "s_lshl_b32 s68, s68, 15\n"
    "s_and_b32 s65, s65, 65535\n"
    "s_mov_b32 s66, 1048576\n"
    "s_mov_b32 s67, 131072\n"
    "s_bfe_u32 s31, s29, 65538\n"
    "s_lshl_b32 s31, s31, 12\n"
    "s_add_u32 s68, s68, s31\n"
    "s_and_b32 s31, s29, 3\n"
    "s_lshl_b32 s31, s31, 7\n"
    "s_add_u32 s68, s68, s31\n"
    "s_lshl_b32 s55, s30, 8\n"
    "s_add_u32 s55, s55, 131072\n"
    "s_lshl_b32 s31, s14, 14\n"
    "v_bfe_u32 v112, v255, 3, 1\n"
    "v_lshlrev_b32 v244, 12, v112\n"
    "v_and_b32 v112, 7, v255\n"
    "v_lshl_add_u32 v244, v112, 6, v244\n"
    "v_bfe_u32 v112, v255, 2, 1\n"
    "v_mul_u32_u24 v112, 3, v112\n"
    "v_lshrrev_b32 v113, 4, v255\n"
    "v_xor_b32 v112, v112, v113\n"
    "v_lshl_add_u32 v244, v112, 4, v244\n"
    "v_add_u32 v244, s31, v244\n"
    "v_lshrrev_b32 v112, 5, v255\n"
    "v_lshlrev_b32 v245, 12, v112\n"
    "v_bfe_u32 v112, v255, 2, 3\n"
    "v_lshl_add_u32 v245, v112, 6, v245\n"
    "v_bfe_u32 v112, v255, 4, 1\n"
    "v_mul_u32_u24 v112, 3, v112\n"
    "v_bfe_u32 v113, v255, 1, 1\n"
    "v_xor_b32 v112, v112, v113\n"
    "v_lshl_add_u32 v245, v112, 4, v245\n"
    "v_and_b32 v112, 1, v255\n"
    "v_lshl_add_u32 v245, v112, 3, v245\n"
    "v_xor_b32 v246, 32, v245\n"
    "s_add_u32 s31, s31, 65536\n"
    "v_add_u32 v245, s31, v245\n"
    "v_add_u32 v246, s31, v246\n"
    "s_lshl_b32 s31, s14, 9\n"
    "v_subrev_u32 v248, s31, v244\n"
    "v_add_u32 v244, s31, v244\n"
    "s_mov_b32 s98, 16256\n"
    "s_mov_b32 s99, 1065353216\n"
    "s_mov_b32 s54, 1065369472\n"
    "s_waitcnt lgkmcnt(0)\n"
    "s_add_u32 s70, s70, 1\n"
    "s_addc_u32 s71, s71, 0\n"
    "v_lshlrev_b32 v122, 4, v255\n"
    "s_mul_i32 s56, s30, 28672\n"
    "s_xor_b32 s57, s30, 1\n"
    "s_mul_i32 s58, s57, 28672\n"
    "v_add_u32 v122, s56, v122\n"
    "v_and_b32 v118, 15, v255\n"
    "v_lshlrev_b32 v125, 6, v118\n"
    "v_bfe_u32 v118, v255, 2, 1\n"
    "v_mul_u32_u24 v118, 3, v118\n"
    "v_lshrrev_b32 v119, 4, v255\n"
    "v_xor_b32 v118, v118, v119\n"
    "v_lshl_add_u32 v125, v118, 4, v125\n"
    "v_add_u32 v123, s58, v125\n"
    "v_add_u32 v125, s56, v125\n"
    "s_lshl_b32 s56, s30, 7\n"
    "s_add_u32 s56, s56, 114688\n"
    "s_lshl_b32 s57, s57, 7\n"
    "s_add_u32 s57, s57, 114688\n"
    "v_and_b32 v118, 0xfc, v255\n"
    "v_add_u32 v121, s56, v118\n"
    "v_and_b32 v118, 15, v255\n"
    "v_lshlrev_b32 v118, 2, v118\n"
    "v_add_u32 v124, s56, v118\n"
    "v_add_u32 v120, s57, v118\n"
    "v_lshrrev_b32 v112, 4, v255\n"
    "v_lshlrev_b32 v114, 10, v112\n"
    "v_bfe_u32 v113, v255, 2, 2\n"
    "v_lshl_add_u32 v114, v113, 6, v114\n"
    "v_and_b32 v113, 3, v255\n"
    "v_lshl_add_u32 v114, v113, 3, v114\n"
    "s_lshl_b32 s31, s14, 9\n"
    "s_add_u32 s31, s31, 77824\n"
    "v_add_u32 v114, s31, v114\n"
    "v_and_b32 v112, 1, v112\n"
    "v_lshl_add_u32 v253, v112, 5, v114\n"
    "v_xor_b32 v254, 32, v253\n"
    "v_add_u32 v251, 57344, v253\n"
    "v_add_u32 v252, 57344, v254\n"
    "v_lshlrev_b32 v206, 4, v255\n"
    "s_lshl_b32 s31, s30, 12\n"
    "v_add_u32 v204, s31, v206\n"
    "s_xor_b32 s31, s31, 4096\n"
    "v_add_u32 v205, s31, v206\n"
    "s_mov_b32 s84, s26\n"
    "s_and_b32 s85, s27, 65535\n"
    "s_mov_b32 s86, 7340032\n"
    "s_mov_b32 s87, 131072\n"
    "s_lshl_b32 s31, s14, 1\n"
    "s_add_u32 s31, s31, 0\n"
    "s_and_b32 s31, s31, 3\n"
    "s_lshl_b32 s59, s15, 2\n"
    "s_add_u32 s31, s31, s59\n"
    "s_lshl_b32 s59, s29, 3\n"
    "s_add_u32 s31, s31, s59\n"
    "s_mul_i32 s92, s31, 57344\n"
    "s_lshl_b32 s31, s14, 1\n"
    "s_add_u32 s31, s31, 1\n"
    "s_and_b32 s31, s31, 3\n"
    "s_lshl_b32 s59, s15, 2\n"
    "s_add_u32 s31, s31, s59\n"
    "s_lshl_b32 s59, s29, 3\n"
    "s_add_u32 s31, s31, s59\n"
    "s_mul_i32 s93, s31, 57344\n"
    "s_lshl_b32 s31, s14, 1\n"
    "s_add_u32 s31, s31, 2\n"
    "s_and_b32 s31, s31, 3\n"
    "s_lshl_b32 s59, s15, 2\n"
    "s_add_u32 s31, s31, s59\n"
    "s_lshl_b32 s59, s29, 3\n"
    "s_add_u32 s31, s31, s59\n"
    "s_mul_i32 s94, s31, 57344\n"
    "s_lshl_b32 s31, s14, 1\n"
    "s_add_u32 s31, s31, 3\n"
    "s_and_b32 s31, s31, 3\n"
    "s_lshl_b32 s59, s15, 2\n"
    "s_add_u32 s31, s31, s59\n"
    "s_lshl_b32 s59, s29, 3\n"
    "s_add_u32 s31, s31, s59\n"
    "s_mul_i32 s95, s31, 57344\n"
    "s_lshl_b32 s31, s28, 1\n"
    "s_add_u32 s31, s31, s14\n"
    "s_lshl_b32 s31, s31, 10\n"
    "v_add_u32 v206, s31, v206\n"
    "v_xor_b32 v207, 32, v250\n"
    "v_xor_b32 v255, 32, v249\n"
    "s_waitcnt vmcnt(12)\n"
    "v_dot2_f32_bf16 v112, v0, s54, 0\n"
    "v_dot2_f32_bf16 v113, v0, v0, 0\n"
    "v_dot2_f32_bf16 v112, v1, s54, v112\n"
    "v_dot2_f32_bf16 v113, v1, v1, v113\n"
    "v_dot2_f32_bf16 v112, v2, s54, v112\n"
    "v_dot2_f32_bf16 v113, v2, v2, v113\n"
    "v_dot2_f32_bf16 v112, v3, s54, v112\n"
    "v_dot2_f32_bf16 v113, v3, v3, v113\n"
    "buffer_load_dwordx4 v[56:59], v115, s[72:75], s60 offen offset:0\n"
    "v_dot2_f32_bf16 v112, v4, s54, v112\n"
    "v_dot2_f32_bf16 v113, v4, v4, v113\n"
    "v_dot2_f32_bf16 v112, v5, s54, v112\n"
    "v_dot2_f32_bf16 v113, v5, v5, v113\n"
    "v_dot2_f32_bf16 v112, v6, s54, v112\n"
    "v_dot2_f32_bf16 v113, v6, v6, v113\n"
    "v_dot2_f32_bf16 v112, v7, s54, v112\n"
    "v_dot2_f32_bf16 v113, v7, v7, v113\n"
    "buffer_load_dwordx4 v[60:63], v115, s[72:75], s60 offen offset:64\n"
    "s_waitcnt vmcnt(12)\n"
    "v_dot2_f32_bf16 v112, v8, s54, v112\n"
    "v_dot2_f32_bf16 v113, v8, v8, v113\n"
    "v_dot2_f32_bf16 v112, v9, s54, v112\n"
    "v_dot2_f32_bf16 v113, v9, v9, v113\n"
    "v_dot2_f32_bf16 v112, v10, s54, v112\n"
    "v_dot2_f32_bf16 v113, v10, v10, v113\n"
    "v_dot2_f32_bf16 v112, v11, s54, v112\n"
    "v_dot2_f32_bf16 v113, v11, v11, v113\n"
    "buffer_load_dwordx4 v[64:67], v115, s[72:75], s60 offen offset:128\n"
    "v_dot2_f32_bf16 v112, v12, s54, v112\n"
    "v_dot2_f32_bf16 v113, v12, v12, v113\n"
    "v_dot2_f32_bf16 v112, v13, s54, v112\n"
    "v_dot2_f32_bf16 v113, v13, v13, v113\n"
    "v_dot2_f32_bf16 v112, v14, s54, v112\n"
    "v_dot2_f32_bf16 v113, v14, v14, v113\n"
    "v_dot2_f32_bf16 v112, v15, s54, v112\n"
    "v_dot2_f32_bf16 v113, v15, v15, v113\n"
    "buffer_load_dwordx4 v[68:71], v115, s[72:75], s60 offen offset:192\n"
    "s_waitcnt vmcnt(12)\n"
    "v_dot2_f32_bf16 v112, v16, s54, v112\n"
    "v_dot2_f32_bf16 v113, v16, v16, v113\n"
    "v_dot2_f32_bf16 v112, v17, s54, v112\n"
    "v_dot2_f32_bf16 v113, v17, v17, v113\n"
    "v_dot2_f32_bf16 v112, v18, s54, v112\n"
    "v_dot2_f32_bf16 v113, v18, v18, v113\n"
    "v_dot2_f32_bf16 v112, v19, s54, v112\n"
    "v_dot2_f32_bf16 v113, v19, v19, v113\n"
    "buffer_load_dwordx4 v[72:75], v115, s[72:75], s60 offen offset:256\n"
    "v_dot2_f32_bf16 v112, v20, s54, v112\n"
    "v_dot2_f32_bf16 v113, v20, v20, v113\n"
    "v_dot2_f32_bf16 v112, v21, s54, v112\n"
    "v_dot2_f32_bf16 v113, v21, v21, v113\n"
    "v_dot2_f32_bf16 v112, v22, s54, v112\n"
    "v_dot2_f32_bf16 v113, v22, v22, v113\n"
    "v_dot2_f32_bf16 v112, v23, s54, v112\n"
    "v_dot2_f32_bf16 v113, v23, v23, v113\n"
    "buffer_load_dwordx4 v[76:79], v115, s[72:75], s60 offen offset:320\n"
    "s_waitcnt vmcnt(12)\n"
    "v_dot2_f32_bf16 v112, v24, s54, v112\n"
    "v_dot2_f32_bf16 v113, v24, v24, v113\n"
    "v_dot2_f32_bf16 v112, v25, s54, v112\n"
    "v_dot2_f32_bf16 v113, v25, v25, v113\n"
    "v_dot2_f32_bf16 v112, v26, s54, v112\n"
    "v_dot2_f32_bf16 v113, v26, v26, v113\n"
    "v_dot2_f32_bf16 v112, v27, s54, v112\n"
    "v_dot2_f32_bf16 v113, v27, v27, v113\n"
    "buffer_load_dwordx4 v[80:83], v115, s[72:75], s60 offen offset:384\n"
    "v_dot2_f32_bf16 v112, v28, s54, v112\n"
    "v_dot2_f32_bf16 v113, v28, v28, v113\n"
    "v_dot2_f32_bf16 v112, v29, s54, v112\n"
    "v_dot2_f32_bf16 v113, v29, v29, v113\n"
    "v_dot2_f32_bf16 v112, v30, s54, v112\n"
    "v_dot2_f32_bf16 v113, v30, v30, v113\n"
    "v_dot2_f32_bf16 v112, v31, s54, v112\n"
    "v_dot2_f32_bf16 v113, v31, v31, v113\n"
    "buffer_load_dwordx4 v[84:87], v115, s[72:75], s60 offen offset:448\n"
    "s_waitcnt vmcnt(12)\n"
    "v_dot2_f32_bf16 v112, v32, s54, v112\n"
    "v_dot2_f32_bf16 v113, v32, v32, v113\n"
    "v_dot2_f32_bf16 v112, v33, s54, v112\n"
    "v_dot2_f32_bf16 v113, v33, v33, v113\n"
    "v_dot2_f32_bf16 v112, v34, s54, v112\n"
    "v_dot2_f32_bf16 v113, v34, v34, v113\n"
    "v_dot2_f32_bf16 v112, v35, s54, v112\n"
    "v_dot2_f32_bf16 v113, v35, v35, v113\n"
    "buffer_load_dwordx4 v[88:91], v115, s[72:75], s60 offen offset:512\n"
    "v_dot2_f32_bf16 v112, v36, s54, v112\n"
    "v_dot2_f32_bf16 v113, v36, v36, v113\n"
    "v_dot2_f32_bf16 v112, v37, s54, v112\n"
    "v_dot2_f32_bf16 v113, v37, v37, v113\n"
    "v_dot2_f32_bf16 v112, v38, s54, v112\n"
    "v_dot2_f32_bf16 v113, v38, v38, v113\n"
    "v_dot2_f32_bf16 v112, v39, s54, v112\n"
    "v_dot2_f32_bf16 v113, v39, v39, v113\n"
    "buffer_load_dwordx4 v[92:95], v115, s[72:75], s60 offen offset:576\n"
    "s_waitcnt vmcnt(12)\n"
    "v_dot2_f32_bf16 v112, v40, s54, v112\n"
    "v_dot2_f32_bf16 v113, v40, v40, v113\n"
    "v_dot2_f32_bf16 v112, v41, s54, v112\n"
    "v_dot2_f32_bf16 v113, v41, v41, v113\n"
    "v_dot2_f32_bf16 v112, v42, s54, v112\n"
    "v_dot2_f32_bf16 v113, v42, v42, v113\n"
    "v_dot2_f32_bf16 v112, v43, s54, v112\n"
    "v_dot2_f32_bf16 v113, v43, v43, v113\n"
    "buffer_load_dwordx4 v[96:99], v115, s[72:75], s60 offen offset:640\n"
    "v_dot2_f32_bf16 v112, v44, s54, v112\n"
    "v_dot2_f32_bf16 v113, v44, v44, v113\n"
    "v_dot2_f32_bf16 v112, v45, s54, v112\n"
    "v_dot2_f32_bf16 v113, v45, v45, v113\n"
    "v_dot2_f32_bf16 v112, v46, s54, v112\n"
    "v_dot2_f32_bf16 v113, v46, v46, v113\n"
    "v_dot2_f32_bf16 v112, v47, s54, v112\n"
    "v_dot2_f32_bf16 v113, v47, v47, v113\n"
    "buffer_load_dwordx4 v[100:103], v115, s[72:75], s60 offen offset:704\n"
    "s_waitcnt vmcnt(12)\n"
    "v_dot2_f32_bf16 v112, v48, s54, v112\n"
    "v_dot2_f32_bf16 v113, v48, v48, v113\n"
    "v_dot2_f32_bf16 v112, v49, s54, v112\n"
    "v_dot2_f32_bf16 v113, v49, v49, v113\n"
    "v_dot2_f32_bf16 v112, v50, s54, v112\n"
    "v_dot2_f32_bf16 v113, v50, v50, v113\n"
    "v_dot2_f32_bf16 v112, v51, s54, v112\n"
    "v_dot2_f32_bf16 v113, v51, v51, v113\n"
    "buffer_load_dwordx4 v[104:107], v115, s[72:75], s60 offen offset:768\n"
    "v_dot2_f32_bf16 v112, v52, s54, v112\n"
    "v_dot2_f32_bf16 v113, v52, v52, v113\n"
    "v_dot2_f32_bf16 v112, v53, s54, v112\n"
    "v_dot2_f32_bf16 v113, v53, v53, v113\n"
    "v_dot2_f32_bf16 v112, v54, s54, v112\n"
    "v_dot2_f32_bf16 v113, v54, v54, v113\n"
    "v_dot2_f32_bf16 v112, v55, s54, v112\n"
    "v_dot2_f32_bf16 v113, v55, v55, v113\n"
    "buffer_load_dwordx4 v[108:111], v115, s[72:75], s60 offen offset:832\n"
    "s_nop 2\n"
    "v_add_f32_dpp v112, v112, v112 quad_perm:[1,0,3,2] row_mask:0xf bank_mask:0xf\n"
    "v_add_f32_dpp v113, v113, v113 quad_perm:[1,0,3,2] row_mask:0xf bank_mask:0xf\n"
    "s_nop 0\n"
    "v_add_f32_dpp v112, v112, v112 quad_perm:[2,3,0,1] row_mask:0xf bank_mask:0xf\n"
    "v_add_f32_dpp v113, v113, v113 quad_perm:[2,3,0,1] row_mask:0xf bank_mask:0xf\n"
    "s_nop 0\n"
    "v_mul_f32 v112, 0x3b124925, v112\n"
    "v_mul_f32 v113, 0x3b124925, v113\n"
    "v_fma_f32 v113, -v112, v112, v113\n"
    "v_cvt_pk_bf16_f32 v114, v112, v112\n"
    "v_and_b32 v114, 0xffff0000, v114\n"
    "v_max_f32 v113, 0, v113\n"
    "v_sub_f32 v114, 0, v114\n"
    "v_cvt_pk_bf16_f32 v113, v113, v113\n"
    "v_and_b32 v113, 0xffff0000, v113\n"
    "v_add_f32 v113, 0x3727c5ac, v113\n"
    "v_cvt_pk_bf16_f32 v113, v113, v113\n"
    "v_and_b32 v113, 0xffff0000, v113\n"
    "v_sqrt_f32 v113, v113\n"
    "v_dot2_f32_bf16 v128, v0, s98, v114\n"
    "v_dot2_f32_bf16 v129, v0, s99, v114\n"
    "v_dot2_f32_bf16 v130, v1, s98, v114\n"
    "v_dot2_f32_bf16 v131, v1, s99, v114\n"
    "v_dot2_f32_bf16 v132, v2, s98, v114\n"
    "v_dot2_f32_bf16 v133, v2, s99, v114\n"
    "v_dot2_f32_bf16 v134, v3, s98, v114\n"
    "v_dot2_f32_bf16 v135, v3, s99, v114\n"
    "v_cvt_pk_bf16_f32 v113, v113, v113\n"
    "v_and_b32 v113, 0xffff0000, v113\n"
    "ds_write_b32 v121, v113 offset:0\n"
    "s_add_u32 m0, s69, 135168\n"
    "s_mov_b32 s96, 0\n"
    "buffer_load_dwordx4 v250, s[76:79], s96 offen offset:0 lds\n"
    "v_dot2_f32_bf16 v136, v4, s98, v114\n"
    "v_dot2_f32_bf16 v137, v4, s99, v114\n"
    "v_dot2_f32_bf16 v138, v5, s98, v114\n"
    "v_dot2_f32_bf16 v139, v5, s99, v114\n"
    "v_dot2_f32_bf16 v140, v6, s98, v114\n"
    "v_dot2_f32_bf16 v141, v6, s99, v114\n"
    "v_dot2_f32_bf16 v142, v7, s98, v114\n"
    "v_dot2_f32_bf16 v143, v7, s99, v114\n"
    "v_cvt_pk_bf16_f32 v0, v128, v129\n"
    "v_cvt_pk_bf16_f32 v1, v130, v131\n"
    "v_cvt_pk_bf16_f32 v2, v132, v133\n"
    "v_cvt_pk_bf16_f32 v3, v134, v135\n"
    "ds_write_b128 v122, v[0:3] offset:0\n"
    "v_dot2_f32_bf16 v128, v8, s98, v114\n"
    "v_dot2_f32_bf16 v129, v8, s99, v114\n"
    "v_dot2_f32_bf16 v130, v9, s98, v114\n"
    "v_dot2_f32_bf16 v131, v9, s99, v114\n"
    "v_dot2_f32_bf16 v132, v10, s98, v114\n"
    "v_dot2_f32_bf16 v133, v10, s99, v114\n"
    "v_dot2_f32_bf16 v134, v11, s98, v114\n"
    "v_dot2_f32_bf16 v135, v11, s99, v114\n"
    "v_cvt_pk_bf16_f32 v4, v136, v137\n"
    "v_cvt_pk_bf16_f32 v5, v138, v139\n"
    "v_cvt_pk_bf16_f32 v6, v140, v141\n"
    "v_cvt_pk_bf16_f32 v7, v142, v143\n"
    "ds_write_b128 v122, v[4:7] offset:1024\n"
    "s_add_u32 m0, s69, 136192\n"
    "s_mov_b32 s97, 28672\n"
    "buffer_load_dwordx4 v207, s[76:79], s97 offen offset:0 lds\n"
    "v_dot2_f32_bf16 v136, v12, s98, v114\n"
    "v_dot2_f32_bf16 v137, v12, s99, v114\n"
    "v_dot2_f32_bf16 v138, v13, s98, v114\n"
    "v_dot2_f32_bf16 v139, v13, s99, v114\n"
    "v_dot2_f32_bf16 v140, v14, s98, v114\n"
    "v_dot2_f32_bf16 v141, v14, s99, v114\n"
    "v_dot2_f32_bf16 v142, v15, s98, v114\n"
    "v_dot2_f32_bf16 v143, v15, s99, v114\n"
    "v_cvt_pk_bf16_f32 v8, v128, v129\n"
    "v_cvt_pk_bf16_f32 v9, v130, v131\n"
    "v_cvt_pk_bf16_f32 v10, v132, v133\n"
    "v_cvt_pk_bf16_f32 v11, v134, v135\n"
    "ds_write_b128 v122, v[8:11] offset:2048\n"
    "v_dot2_f32_bf16 v128, v16, s98, v114\n"
    "v_dot2_f32_bf16 v129, v16, s99, v114\n"
    "v_dot2_f32_bf16 v130, v17, s98, v114\n"
    "v_dot2_f32_bf16 v131, v17, s99, v114\n"
    "v_dot2_f32_bf16 v132, v18, s98, v114\n"
    "v_dot2_f32_bf16 v133, v18, s99, v114\n"
    "v_dot2_f32_bf16 v134, v19, s98, v114\n"
    "v_dot2_f32_bf16 v135, v19, s99, v114\n"
    "v_cvt_pk_bf16_f32 v12, v136, v137\n"
    "v_cvt_pk_bf16_f32 v13, v138, v139\n"
    "v_cvt_pk_bf16_f32 v14, v140, v141\n"
    "v_cvt_pk_bf16_f32 v15, v142, v143\n"
    "ds_write_b128 v122, v[12:15] offset:3072\n"
    "s_add_u32 m0, s69, 137216\n"
    "s_mov_b32 s96, 57344\n"
    "buffer_load_dwordx4 v250, s[76:79], s96 offen offset:0 lds\n"
    "v_dot2_f32_bf16 v136, v20, s98, v114\n"
    "v_dot2_f32_bf16 v137, v20, s99, v114\n"
    "v_dot2_f32_bf16 v138, v21, s98, v114\n"
    "v_dot2_f32_bf16 v139, v21, s99, v114\n"
    "v_dot2_f32_bf16 v140, v22, s98, v114\n"
    "v_dot2_f32_bf16 v141, v22, s99, v114\n"
    "v_dot2_f32_bf16 v142, v23, s98, v114\n"
    "v_dot2_f32_bf16 v143, v23, s99, v114\n"
    "v_cvt_pk_bf16_f32 v16, v128, v129\n"
    "v_cvt_pk_bf16_f32 v17, v130, v131\n"
    "v_cvt_pk_bf16_f32 v18, v132, v133\n"
    "v_cvt_pk_bf16_f32 v19, v134, v135\n"
    "ds_write_b128 v122, v[16:19] offset:4096\n"
    "v_dot2_f32_bf16 v128, v24, s98, v114\n"
    "v_dot2_f32_bf16 v129, v24, s99, v114\n"
    "v_dot2_f32_bf16 v130, v25, s98, v114\n"
    "v_dot2_f32_bf16 v131, v25, s99, v114\n"
    "v_dot2_f32_bf16 v132, v26, s98, v114\n"
    "v_dot2_f32_bf16 v133, v26, s99, v114\n"
    "v_dot2_f32_bf16 v134, v27, s98, v114\n"
    "v_dot2_f32_bf16 v135, v27, s99, v114\n"
    "v_cvt_pk_bf16_f32 v20, v136, v137\n"
    "v_cvt_pk_bf16_f32 v21, v138, v139\n"
    "v_cvt_pk_bf16_f32 v22, v140, v141\n"
    "v_cvt_pk_bf16_f32 v23, v142, v143\n"
    "ds_write_b128 v122, v[20:23] offset:5120\n"
    "s_add_u32 m0, s69, 138240\n"
    "s_mov_b32 s97, 86016\n"
    "buffer_load_dwordx4 v207, s[76:79], s97 offen offset:0 lds\n"
    "v_dot2_f32_bf16 v136, v28, s98, v114\n"
    "v_dot2_f32_bf16 v137, v28, s99, v114\n"
    "v_dot2_f32_bf16 v138, v29, s98, v114\n"
    "v_dot2_f32_bf16 v139, v29, s99, v114\n"
    "v_dot2_f32_bf16 v140, v30, s98, v114\n"
    "v_dot2_f32_bf16 v141, v30, s99, v114\n"
    "v_dot2_f32_bf16 v142, v31, s98, v114\n"
    "v_dot2_f32_bf16 v143, v31, s99, v114\n"
    "v_cvt_pk_bf16_f32 v24, v128, v129\n"
    "v_cvt_pk_bf16_f32 v25, v130, v131\n"
    "v_cvt_pk_bf16_f32 v26, v132, v133\n"
    "v_cvt_pk_bf16_f32 v27, v134, v135\n"
    "ds_write_b128 v122, v[24:27] offset:6144\n"
    "v_dot2_f32_bf16 v128, v32, s98, v114\n"
    "v_dot2_f32_bf16 v129, v32, s99, v114\n"
    "v_dot2_f32_bf16 v130, v33, s98, v114\n"
    "v_dot2_f32_bf16 v131, v33, s99, v114\n"
    "v_dot2_f32_bf16 v132, v34, s98, v114\n"
    "v_dot2_f32_bf16 v133, v34, s99, v114\n"
    "v_dot2_f32_bf16 v134, v35, s98, v114\n"
    "v_dot2_f32_bf16 v135, v35, s99, v114\n"
    "v_cvt_pk_bf16_f32 v28, v136, v137\n"
    "v_cvt_pk_bf16_f32 v29, v138, v139\n"
    "v_cvt_pk_bf16_f32 v30, v140, v141\n"
    "v_cvt_pk_bf16_f32 v31, v142, v143\n"
    "ds_write_b128 v122, v[28:31] offset:7168\n"
    "s_add_u32 m0, s69, 139264\n"
    "s_mov_b32 s96, 114688\n"
    "buffer_load_dwordx4 v250, s[76:79], s96 offen offset:0 lds\n"
    "v_dot2_f32_bf16 v136, v36, s98, v114\n"
    "v_dot2_f32_bf16 v137, v36, s99, v114\n"
    "v_dot2_f32_bf16 v138, v37, s98, v114\n"
    "v_dot2_f32_bf16 v139, v37, s99, v114\n"
    "v_dot2_f32_bf16 v140, v38, s98, v114\n"
    "v_dot2_f32_bf16 v141, v38, s99, v114\n"
    "v_dot2_f32_bf16 v142, v39, s98, v114\n"
    "v_dot2_f32_bf16 v143, v39, s99, v114\n"
    "v_cvt_pk_bf16_f32 v32, v128, v129\n"
    "v_cvt_pk_bf16_f32 v33, v130, v131\n"
    "v_cvt_pk_bf16_f32 v34, v132, v133\n"
    "v_cvt_pk_bf16_f32 v35, v134, v135\n"
    "ds_write_b128 v122, v[32:35] offset:8192\n"
    "v_dot2_f32_bf16 v128, v40, s98, v114\n"
    "v_dot2_f32_bf16 v129, v40, s99, v114\n"
    "v_dot2_f32_bf16 v130, v41, s98, v114\n"
    "v_dot2_f32_bf16 v131, v41, s99, v114\n"
    "v_dot2_f32_bf16 v132, v42, s98, v114\n"
    "v_dot2_f32_bf16 v133, v42, s99, v114\n"
    "v_dot2_f32_bf16 v134, v43, s98, v114\n"
    "v_dot2_f32_bf16 v135, v43, s99, v114\n"
    "v_cvt_pk_bf16_f32 v36, v136, v137\n"
    "v_cvt_pk_bf16_f32 v37, v138, v139\n"
    "v_cvt_pk_bf16_f32 v38, v140, v141\n"
    "v_cvt_pk_bf16_f32 v39, v142, v143\n"
    "ds_write_b128 v122, v[36:39] offset:9216\n"
    "s_add_u32 m0, s69, 140288\n"
    "s_mov_b32 s97, 143360\n"
    "buffer_load_dwordx4 v207, s[76:79], s97 offen offset:0 lds\n"
    "v_dot2_f32_bf16 v136, v44, s98, v114\n"
    "v_dot2_f32_bf16 v137, v44, s99, v114\n"
    "v_dot2_f32_bf16 v138, v45, s98, v114\n"
    "v_dot2_f32_bf16 v139, v45, s99, v114\n"
    "v_dot2_f32_bf16 v140, v46, s98, v114\n"
    "v_dot2_f32_bf16 v141, v46, s99, v114\n"
    "v_dot2_f32_bf16 v142, v47, s98, v114\n"
    "v_dot2_f32_bf16 v143, v47, s99, v114\n"
    "v_cvt_pk_bf16_f32 v40, v128, v129\n"
    "v_cvt_pk_bf16_f32 v41, v130, v131\n"
    "v_cvt_pk_bf16_f32 v42, v132, v133\n"
    "v_cvt_pk_bf16_f32 v43, v134, v135\n"
    "ds_write_b128 v122, v[40:43] offset:10240\n"
    "v_dot2_f32_bf16 v128, v48, s98, v114\n"
    "v_dot2_f32_bf16 v129, v48, s99, v114\n"
    "v_dot2_f32_bf16 v130, v49, s98, v114\n"
    "v_dot2_f32_bf16 v131, v49, s99, v114\n"
    "v_dot2_f32_bf16 v132, v50, s98, v114\n"
    "v_dot2_f32_bf16 v133, v50, s99, v114\n"
    "v_dot2_f32_bf16 v134, v51, s98, v114\n"
    "v_dot2_f32_bf16 v135, v51, s99, v114\n"
    "v_cvt_pk_bf16_f32 v44, v136, v137\n"
    "v_cvt_pk_bf16_f32 v45, v138, v139\n"
    "v_cvt_pk_bf16_f32 v46, v140, v141\n"
    "v_cvt_pk_bf16_f32 v47, v142, v143\n"
    "ds_write_b128 v122, v[44:47] offset:11264\n"
    "s_add_u32 m0, s69, 141312\n"
    "s_mov_b32 s96, 172032\n"
    "buffer_load_dwordx4 v250, s[76:79], s96 offen offset:0 lds\n"
    "v_dot2_f32_bf16 v136, v52, s98, v114\n"
    "v_dot2_f32_bf16 v137, v52, s99, v114\n"
    "v_dot2_f32_bf16 v138, v53, s98, v114\n"
    "v_dot2_f32_bf16 v139, v53, s99, v114\n"
    "v_dot2_f32_bf16 v140, v54, s98, v114\n"
    "v_dot2_f32_bf16 v141, v54, s99, v114\n"
    "v_dot2_f32_bf16 v142, v55, s98, v114\n"
    "v_dot2_f32_bf16 v143, v55, s99, v114\n"
    "v_cvt_pk_bf16_f32 v48, v128, v129\n"
    "v_cvt_pk_bf16_f32 v49, v130, v131\n"
    "v_cvt_pk_bf16_f32 v50, v132, v133\n"
    "v_cvt_pk_bf16_f32 v51, v134, v135\n"
    "ds_write_b128 v122, v[48:51] offset:12288\n"
    "s_nop 2\n"
    "v_cvt_pk_bf16_f32 v52, v136, v137\n"
    "v_cvt_pk_bf16_f32 v53, v138, v139\n"
    "v_cvt_pk_bf16_f32 v54, v140, v141\n"
    "v_cvt_pk_bf16_f32 v55, v142, v143\n"
    "ds_write_b128 v122, v[52:55] offset:13312\n"
    "s_waitcnt vmcnt(19)\n"
    "v_dot2_f32_bf16 v112, v56, s54, 0\n"
    "v_dot2_f32_bf16 v113, v56, v56, 0\n"
    "v_dot2_f32_bf16 v112, v57, s54, v112\n"
    "v_dot2_f32_bf16 v113, v57, v57, v113\n"
    "v_dot2_f32_bf16 v112, v58, s54, v112\n"
    "v_dot2_f32_bf16 v113, v58, v58, v113\n"
    "v_dot2_f32_bf16 v112, v59, s54, v112\n"
    "v_dot2_f32_bf16 v113, v59, v59, v113\n"
    "v_dot2_f32_bf16 v112, v60, s54, v112\n"
    "v_dot2_f32_bf16 v113, v60, v60, v113\n"
    "v_dot2_f32_bf16 v112, v61, s54, v112\n"
    "v_dot2_f32_bf16 v113, v61, v61, v113\n"
    "v_dot2_f32_bf16 v112, v62, s54, v112\n"
    "v_dot2_f32_bf16 v113, v62, v62, v113\n"
    "v_dot2_f32_bf16 v112, v63, s54, v112\n"
    "v_dot2_f32_bf16 v113, v63, v63, v113\n"
    "s_waitcnt vmcnt(17)\n"
    "v_dot2_f32_bf16 v112, v64, s54, v112\n"
    "v_dot2_f32_bf16 v113, v64, v64, v113\n"
    "v_dot2_f32_bf16 v112, v65, s54, v112\n"
    "v_dot2_f32_bf16 v113, v65, v65, v113\n"
    "v_dot2_f32_bf16 v112, v66, s54, v112\n"
    "v_dot2_f32_bf16 v113, v66, v66, v113\n"
    "v_dot2_f32_bf16 v112, v67, s54, v112\n"
    "v_dot2_f32_bf16 v113, v67, v67, v113\n"
    "v_dot2_f32_bf16 v112, v68, s54, v112\n"
    "v_dot2_f32_bf16 v113, v68, v68, v113\n"
    "v_dot2_f32_bf16 v112, v69, s54, v112\n"
    "v_dot2_f32_bf16 v113, v69, v69, v113\n"
    "v_dot2_f32_bf16 v112, v70, s54, v112\n"
    "v_dot2_f32_bf16 v113, v70, v70, v113\n"
    "v_dot2_f32_bf16 v112, v71, s54, v112\n"
    "v_dot2_f32_bf16 v113, v71, v71, v113\n"
    "s_waitcnt vmcnt(15)\n"
    "v_dot2_f32_bf16 v112, v72, s54, v112\n"
    "v_dot2_f32_bf16 v113, v72, v72, v113\n"
    "v_dot2_f32_bf16 v112, v73, s54, v112\n"
    "v_dot2_f32_bf16 v113, v73, v73, v113\n"
    "v_dot2_f32_bf16 v112, v74, s54, v112\n"
    "v_dot2_f32_bf16 v113, v74, v74, v113\n"
    "v_dot2_f32_bf16 v112, v75, s54, v112\n"
    "v_dot2_f32_bf16 v113, v75, v75, v113\n"
    "v_dot2_f32_bf16 v112, v76, s54, v112\n"
    "v_dot2_f32_bf16 v113, v76, v76, v113\n"
    "v_dot2_f32_bf16 v112, v77, s54, v112\n"
    "v_dot2_f32_bf16 v113, v77, v77, v113\n"
    "v_dot2_f32_bf16 v112, v78, s54, v112\n"
    "v_dot2_f32_bf16 v113, v78, v78, v113\n"
    "v_dot2_f32_bf16 v112, v79, s54, v112\n"
    "v_dot2_f32_bf16 v113, v79, v79, v113\n"
    "s_waitcnt vmcnt(13)\n"
    "v_dot2_f32_bf16 v112, v80, s54, v112\n"
    "v_dot2_f32_bf16 v113, v80, v80, v113\n"
    "v_dot2_f32_bf16 v112, v81, s54, v112\n"
    "v_dot2_f32_bf16 v113, v81, v81, v113\n"
    "v_dot2_f32_bf16 v112, v82, s54, v112\n"
    "v_dot2_f32_bf16 v113, v82, v82, v113\n"
    "v_dot2_f32_bf16 v112, v83, s54, v112\n"
    "v_dot2_f32_bf16 v113, v83, v83, v113\n"
    "v_dot2_f32_bf16 v112, v84, s54, v112\n"
    "v_dot2_f32_bf16 v113, v84, v84, v113\n"
    "v_dot2_f32_bf16 v112, v85, s54, v112\n"
    "v_dot2_f32_bf16 v113, v85, v85, v113\n"
    "v_dot2_f32_bf16 v112, v86, s54, v112\n"
    "v_dot2_f32_bf16 v113, v86, v86, v113\n"
    "v_dot2_f32_bf16 v112, v87, s54, v112\n"
    "v_dot2_f32_bf16 v113, v87, v87, v113\n"
    "s_waitcnt vmcnt(11)\n"
    "v_dot2_f32_bf16 v112, v88, s54, v112\n"
    "v_dot2_f32_bf16 v113, v88, v88, v113\n"
    "v_dot2_f32_bf16 v112, v89, s54, v112\n"
    "v_dot2_f32_bf16 v113, v89, v89, v113\n"
    "v_dot2_f32_bf16 v112, v90, s54, v112\n"
    "v_dot2_f32_bf16 v113, v90, v90, v113\n"
    "v_dot2_f32_bf16 v112, v91, s54, v112\n"
    "v_dot2_f32_bf16 v113, v91, v91, v113\n"
    "v_dot2_f32_bf16 v112, v92, s54, v112\n"
    "v_dot2_f32_bf16 v113, v92, v92, v113\n"
    "v_dot2_f32_bf16 v112, v93, s54, v112\n"
    "v_dot2_f32_bf16 v113, v93, v93, v113\n"
    "v_dot2_f32_bf16 v112, v94, s54, v112\n"
    "v_dot2_f32_bf16 v113, v94, v94, v113\n"
    "v_dot2_f32_bf16 v112, v95, s54, v112\n"
    "v_dot2_f32_bf16 v113, v95, v95, v113\n"
    "s_waitcnt vmcnt(9)\n"
    "v_dot2_f32_bf16 v112, v96, s54, v112\n"
    "v_dot2_f32_bf16 v113, v96, v96, v113\n"
    "v_dot2_f32_bf16 v112, v97, s54, v112\n"
    "v_dot2_f32_bf16 v113, v97, v97, v113\n"
    "v_dot2_f32_bf16 v112, v98, s54, v112\n"
    "v_dot2_f32_bf16 v113, v98, v98, v113\n"
    "v_dot2_f32_bf16 v112, v99, s54, v112\n"
    "v_dot2_f32_bf16 v113, v99, v99, v113\n"
    "v_dot2_f32_bf16 v112, v100, s54, v112\n"
    "v_dot2_f32_bf16 v113, v100, v100, v113\n"
    "v_dot2_f32_bf16 v112, v101, s54, v112\n"
    "v_dot2_f32_bf16 v113, v101, v101, v113\n"
    "v_dot2_f32_bf16 v112, v102, s54, v112\n"
    "v_dot2_f32_bf16 v113, v102, v102, v113\n"
    "v_dot2_f32_bf16 v112, v103, s54, v112\n"
    "v_dot2_f32_bf16 v113, v103, v103, v113\n"
    "s_waitcnt vmcnt(7)\n"
    "v_dot2_f32_bf16 v112, v104, s54, v112\n"
    "v_dot2_f32_bf16 v113, v104, v104, v113\n"
    "v_dot2_f32_bf16 v112, v105, s54, v112\n"
    "v_dot2_f32_bf16 v113, v105, v105, v113\n"
    "v_dot2_f32_bf16 v112, v106, s54, v112\n"
    "v_dot2_f32_bf16 v113, v106, v106, v113\n"
    "v_dot2_f32_bf16 v112, v107, s54, v112\n"
    "v_dot2_f32_bf16 v113, v107, v107, v113\n"
    "v_dot2_f32_bf16 v112, v108, s54, v112\n"
    "v_dot2_f32_bf16 v113, v108, v108, v113\n"
    "v_dot2_f32_bf16 v112, v109, s54, v112\n"
    "v_dot2_f32_bf16 v113, v109, v109, v113\n"
    "v_dot2_f32_bf16 v112, v110, s54, v112\n"
    "v_dot2_f32_bf16 v113, v110, v110, v113\n"
    "v_dot2_f32_bf16 v112, v111, s54, v112\n"
    "v_dot2_f32_bf16 v113, v111, v111, v113\n"
    "s_nop 2\n"
    "buffer_load_dword v127, v149, s[76:79], 0 offen offset:0\n"
    "buffer_load_dword v127, v148, s[88:91], 0 offen offset:0\n"
    "buffer_load_dword v127, v149, s[80:83], 0 offen offset:0\n"
    "v_add_f32_dpp v112, v112, v112 quad_perm:[1,0,3,2] row_mask:0xf bank_mask:0xf\n"
    "v_add_f32_dpp v113, v113, v113 quad_perm:[1,0,3,2] row_mask:0xf bank_mask:0xf\n"
    "s_nop 0\n"
    "v_add_f32_dpp v112, v112, v112 quad_perm:[2,3,0,1] row_mask:0xf bank_mask:0xf\n"
    "v_add_f32_dpp v113, v113, v113 quad_perm:[2,3,0,1] row_mask:0xf bank_mask:0xf\n"
    "s_nop 0\n"
    "v_mul_f32 v112, 0x3b124925, v112\n"
    "v_mul_f32 v113, 0x3b124925, v113\n"
    "v_fma_f32 v113, -v112, v112, v113\n"
    "v_cvt_pk_bf16_f32 v114, v112, v112\n"
    "v_and_b32 v114, 0xffff0000, v114\n"
    "v_max_f32 v113, 0, v113\n"
    "v_sub_f32 v114, 0, v114\n"
    "v_cvt_pk_bf16_f32 v113, v113, v113\n"
    "v_and_b32 v113, 0xffff0000, v113\n"
    "v_add_f32 v113, 0x3727c5ac, v113\n"
    "v_cvt_pk_bf16_f32 v113, v113, v113\n"
    "v_and_b32 v113, 0xffff0000, v113\n"
    "v_sqrt_f32 v113, v113\n"
    "v_dot2_f32_bf16 v128, v56, s98, v114\n"
    "v_dot2_f32_bf16 v129, v56, s99, v114\n"
    "v_dot2_f32_bf16 v130, v57, s98, v114\n"
    "v_dot2_f32_bf16 v131, v57, s99, v114\n"
    "v_dot2_f32_bf16 v132, v58, s98, v114\n"
    "v_dot2_f32_bf16 v133, v58, s99, v114\n"
    "v_dot2_f32_bf16 v134, v59, s98, v114\n"
    "v_dot2_f32_bf16 v135, v59, s99, v114\n"
    "v_cvt_pk_bf16_f32 v113, v113, v113\n"
    "v_and_b32 v113, 0xffff0000, v113\n"
    "ds_write_b32 v121, v113 offset:64\n"
    "v_dot2_f32_bf16 v136, v60, s98, v114\n"
    "v_dot2_f32_bf16 v137, v60, s99, v114\n"
    "v_dot2_f32_bf16 v138, v61, s98, v114\n"
    "v_dot2_f32_bf16 v139, v61, s99, v114\n"
    "v_dot2_f32_bf16 v140, v62, s98, v114\n"
    "v_dot2_f32_bf16 v141, v62, s99, v114\n"
    "v_dot2_f32_bf16 v142, v63, s98, v114\n"
    "v_dot2_f32_bf16 v143, v63, s99, v114\n"
    "v_cvt_pk_bf16_f32 v56, v128, v129\n"
    "v_cvt_pk_bf16_f32 v57, v130, v131\n"
    "v_cvt_pk_bf16_f32 v58, v132, v133\n"
    "v_cvt_pk_bf16_f32 v59, v134, v135\n"
    "ds_write_b128 v122, v[56:59] offset:14336\n"
    "v_dot2_f32_bf16 v128, v64, s98, v114\n"
    "v_dot2_f32_bf16 v129, v64, s99, v114\n"
    "v_dot2_f32_bf16 v130, v65, s98, v114\n"
    "v_dot2_f32_bf16 v131, v65, s99, v114\n"
    "v_dot2_f32_bf16 v132, v66, s98, v114\n"
    "v_dot2_f32_bf16 v133, v66, s99, v114\n"
    "v_dot2_f32_bf16 v134, v67, s98, v114\n"
    "v_dot2_f32_bf16 v135, v67, s99, v114\n"
    "v_cvt_pk_bf16_f32 v60, v136, v137\n"
    "v_cvt_pk_bf16_f32 v61, v138, v139\n"
    "v_cvt_pk_bf16_f32 v62, v140, v141\n"
    "v_cvt_pk_bf16_f32 v63, v142, v143\n"
    "ds_write_b128 v122, v[60:63] offset:15360\n"
    "v_dot2_f32_bf16 v136, v68, s98, v114\n"
    "v_dot2_f32_bf16 v137, v68, s99, v114\n"
    "v_dot2_f32_bf16 v138, v69, s98, v114\n"
    "v_dot2_f32_bf16 v139, v69, s99, v114\n"
    "v_dot2_f32_bf16 v140, v70, s98, v114\n"
    "v_dot2_f32_bf16 v141, v70, s99, v114\n"
    "v_dot2_f32_bf16 v142, v71, s98, v114\n"
    "v_dot2_f32_bf16 v143, v71, s99, v114\n"
    "v_cvt_pk_bf16_f32 v64, v128, v129\n"
    "v_cvt_pk_bf16_f32 v65, v130, v131\n"
    "v_cvt_pk_bf16_f32 v66, v132, v133\n"
    "v_cvt_pk_bf16_f32 v67, v134, v135\n"
    "ds_write_b128 v122, v[64:67] offset:16384\n"
    "v_dot2_f32_bf16 v128, v72, s98, v114\n"
    "v_dot2_f32_bf16 v129, v72, s99, v114\n"
    "v_dot2_f32_bf16 v130, v73, s98, v114\n"
    "v_dot2_f32_bf16 v131, v73, s99, v114\n"
    "v_dot2_f32_bf16 v132, v74, s98, v114\n"
    "v_dot2_f32_bf16 v133, v74, s99, v114\n"
    "v_dot2_f32_bf16 v134, v75, s98, v114\n"
    "v_dot2_f32_bf16 v135, v75, s99, v114\n"
    "v_cvt_pk_bf16_f32 v68, v136, v137\n"
    "v_cvt_pk_bf16_f32 v69, v138, v139\n"
    "v_cvt_pk_bf16_f32 v70, v140, v141\n"
    "v_cvt_pk_bf16_f32 v71, v142, v143\n"
    "ds_write_b128 v122, v[68:71] offset:17408\n"
    "v_dot2_f32_bf16 v136, v76, s98, v114\n"
    "v_dot2_f32_bf16 v137, v76, s99, v114\n"
    "v_dot2_f32_bf16 v138, v77, s98, v114\n"
    "v_dot2_f32_bf16 v139, v77, s99, v114\n"
    "v_dot2_f32_bf16 v140, v78, s98, v114\n"
    "v_dot2_f32_bf16 v141, v78, s99, v114\n"
    "v_dot2_f32_bf16 v142, v79, s98, v114\n"
    "v_dot2_f32_bf16 v143, v79, s99, v114\n"
    "v_cvt_pk_bf16_f32 v72, v128, v129\n"
    "v_cvt_pk_bf16_f32 v73, v130, v131\n"
    "v_cvt_pk_bf16_f32 v74, v132, v133\n"
    "v_cvt_pk_bf16_f32 v75, v134, v135\n"
    "ds_write_b128 v122, v[72:75] offset:18432\n"
    "v_dot2_f32_bf16 v128, v80, s98, v114\n"
    "v_dot2_f32_bf16 v129, v80, s99, v114\n"
    "v_dot2_f32_bf16 v130, v81, s98, v114\n"
    "v_dot2_f32_bf16 v131, v81, s99, v114\n"
    "v_dot2_f32_bf16 v132, v82, s98, v114\n"
    "v_dot2_f32_bf16 v133, v82, s99, v114\n"
    "v_dot2_f32_bf16 v134, v83, s98, v114\n"
    "v_dot2_f32_bf16 v135, v83, s99, v114\n"
    "v_cvt_pk_bf16_f32 v76, v136, v137\n"
    "v_cvt_pk_bf16_f32 v77, v138, v139\n"
    "v_cvt_pk_bf16_f32 v78, v140, v141\n"
    "v_cvt_pk_bf16_f32 v79, v142, v143\n"
    "ds_write_b128 v122, v[76:79] offset:19456\n"
    "v_dot2_f32_bf16 v136, v84, s98, v114\n"
    "v_dot2_f32_bf16 v137, v84, s99, v114\n"
    "v_dot2_f32_bf16 v138, v85, s98, v114\n"
    "v_dot2_f32_bf16 v139, v85, s99, v114\n"
    "v_dot2_f32_bf16 v140, v86, s98, v114\n"
    "v_dot2_f32_bf16 v141, v86, s99, v114\n"
    "v_dot2_f32_bf16 v142, v87, s98, v114\n"
    "v_dot2_f32_bf16 v143, v87, s99, v114\n"
    "v_cvt_pk_bf16_f32 v80, v128, v129\n"
    "v_cvt_pk_bf16_f32 v81, v130, v131\n"
    "v_cvt_pk_bf16_f32 v82, v132, v133\n"
    "v_cvt_pk_bf16_f32 v83, v134, v135\n"
    "ds_write_b128 v122, v[80:83] offset:20480\n"
    "v_dot2_f32_bf16 v128, v88, s98, v114\n"
    "v_dot2_f32_bf16 v129, v88, s99, v114\n"
    "v_dot2_f32_bf16 v130, v89, s98, v114\n"
    "v_dot2_f32_bf16 v131, v89, s99, v114\n"
    "v_dot2_f32_bf16 v132, v90, s98, v114\n"
    "v_dot2_f32_bf16 v133, v90, s99, v114\n"
    "v_dot2_f32_bf16 v134, v91, s98, v114\n"
    "v_dot2_f32_bf16 v135, v91, s99, v114\n"
    "v_cvt_pk_bf16_f32 v84, v136, v137\n"
    "v_cvt_pk_bf16_f32 v85, v138, v139\n"
    "v_cvt_pk_bf16_f32 v86, v140, v141\n"
    "v_cvt_pk_bf16_f32 v87, v142, v143\n"
    "ds_write_b128 v122, v[84:87] offset:21504\n"
    "v_dot2_f32_bf16 v136, v92, s98, v114\n"
    "v_dot2_f32_bf16 v137, v92, s99, v114\n"
    "v_dot2_f32_bf16 v138, v93, s98, v114\n"
    "v_dot2_f32_bf16 v139, v93, s99, v114\n"
    "v_dot2_f32_bf16 v140, v94, s98, v114\n"
    "v_dot2_f32_bf16 v141, v94, s99, v114\n"
    "v_dot2_f32_bf16 v142, v95, s98, v114\n"
    "v_dot2_f32_bf16 v143, v95, s99, v114\n"
    "v_cvt_pk_bf16_f32 v88, v128, v129\n"
    "v_cvt_pk_bf16_f32 v89, v130, v131\n"
    "v_cvt_pk_bf16_f32 v90, v132, v133\n"
    "v_cvt_pk_bf16_f32 v91, v134, v135\n"
    "ds_write_b128 v122, v[88:91] offset:22528\n"
    "v_dot2_f32_bf16 v128, v96, s98, v114\n"
    "v_dot2_f32_bf16 v129, v96, s99, v114\n"
    "v_dot2_f32_bf16 v130, v97, s98, v114\n"
    "v_dot2_f32_bf16 v131, v97, s99, v114\n"
    "v_dot2_f32_bf16 v132, v98, s98, v114\n"
    "v_dot2_f32_bf16 v133, v98, s99, v114\n"
    "v_dot2_f32_bf16 v134, v99, s98, v114\n"
    "v_dot2_f32_bf16 v135, v99, s99, v114\n"
    "v_cvt_pk_bf16_f32 v92, v136, v137\n"
    "v_cvt_pk_bf16_f32 v93, v138, v139\n"
    "v_cvt_pk_bf16_f32 v94, v140, v141\n"
    "v_cvt_pk_bf16_f32 v95, v142, v143\n"
    "ds_write_b128 v122, v[92:95] offset:23552\n"
    "v_dot2_f32_bf16 v136, v100, s98, v114\n"
    "v_dot2_f32_bf16 v137, v100, s99, v114\n"
    "v_dot2_f32_bf16 v138, v101, s98, v114\n"
    "v_dot2_f32_bf16 v139, v101, s99, v114\n"
    "v_dot2_f32_bf16 v140, v102, s98, v114\n"
    "v_dot2_f32_bf16 v141, v102, s99, v114\n"
    "v_dot2_f32_bf16 v142, v103, s98, v114\n"
    "v_dot2_f32_bf16 v143, v103, s99, v114\n"
    "v_cvt_pk_bf16_f32 v96, v128, v129\n"
    "v_cvt_pk_bf16_f32 v97, v130, v131\n"
    "v_cvt_pk_bf16_f32 v98, v132, v133\n"
    "v_cvt_pk_bf16_f32 v99, v134, v135\n"
    "ds_write_b128 v122, v[96:99] offset:24576\n"
    "v_dot2_f32_bf16 v128, v104, s98, v114\n"
    "v_dot2_f32_bf16 v129, v104, s99, v114\n"
    "v_dot2_f32_bf16 v130, v105, s98, v114\n"
    "v_dot2_f32_bf16 v131, v105, s99, v114\n"
    "v_dot2_f32_bf16 v132, v106, s98, v114\n"
    "v_dot2_f32_bf16 v133, v106, s99, v114\n"
    "v_dot2_f32_bf16 v134, v107, s98, v114\n"
    "v_dot2_f32_bf16 v135, v107, s99, v114\n"
    "v_cvt_pk_bf16_f32 v100, v136, v137\n"
    "v_cvt_pk_bf16_f32 v101, v138, v139\n"
    "v_cvt_pk_bf16_f32 v102, v140, v141\n"
    "v_cvt_pk_bf16_f32 v103, v142, v143\n"
    "ds_write_b128 v122, v[100:103] offset:25600\n"
    "v_dot2_f32_bf16 v136, v108, s98, v114\n"
    "v_dot2_f32_bf16 v137, v108, s99, v114\n"
    "v_dot2_f32_bf16 v138, v109, s98, v114\n"
    "v_dot2_f32_bf16 v139, v109, s99, v114\n"
    "v_dot2_f32_bf16 v140, v110, s98, v114\n"
    "v_dot2_f32_bf16 v141, v110, s99, v114\n"
    "v_dot2_f32_bf16 v142, v111, s98, v114\n"
    "v_dot2_f32_bf16 v143, v111, s99, v114\n"
    "v_cvt_pk_bf16_f32 v104, v128, v129\n"
    "v_cvt_pk_bf16_f32 v105, v130, v131\n"
    "v_cvt_pk_bf16_f32 v106, v132, v133\n"
    "v_cvt_pk_bf16_f32 v107, v134, v135\n"
    "ds_write_b128 v122, v[104:107] offset:26624\n"
    "s_nop 2\n"
    "v_cvt_pk_bf16_f32 v108, v136, v137\n"
    "v_cvt_pk_bf16_f32 v109, v138, v139\n"
    "v_cvt_pk_bf16_f32 v110, v140, v141\n"
    "v_cvt_pk_bf16_f32 v111, v142, v143\n"
    "ds_write_b128 v122, v[108:111] offset:27648\n"
    "s_waitcnt lgkmcnt(0) vmcnt(3)\n"
    "s_barrier\n"
    "ds_read_b32 v200, v124 offset:0\n"
    "ds_read_b32 v201, v124 offset:64\n"
    "ds_read_b32 v202, v120 offset:0\n"
    "ds_read_b32 v203, v120 offset:64\n"
    "ds_read_b128 a[0:3], v125 offset:0\n"
    "ds_read_b128 a[56:59], v125 offset:14336\n"
    "ds_read_b128 a[112:115], v123 offset:0\n"
    "ds_read_b128 a[168:171], v123 offset:14336\n"
    "ds_read_b128 a[4:7], v125 offset:1024\n"
    "ds_read_b128 a[60:63], v125 offset:15360\n"
    "ds_read_b128 a[116:119], v123 offset:1024\n"
    "ds_read_b128 a[172:175], v123 offset:15360\n"
    "ds_read_b128 a[8:11], v125 offset:2048\n"
    "ds_read_b128 a[64:67], v125 offset:16384\n"
    "ds_read_b128 a[120:123], v123 offset:2048\n"
    "ds_read_b128 a[176:179], v123 offset:16384\n"
    "ds_read_b128 a[12:15], v125 offset:3072\n"
    "ds_read_b128 a[68:71], v125 offset:17408\n"
    "ds_read_b128 a[124:127], v123 offset:3072\n"
    "ds_read_b128 a[180:183], v123 offset:17408\n"
    "ds_read_b128 a[16:19], v125 offset:4096\n"
    "ds_read_b128 a[72:75], v125 offset:18432\n"
    "ds_read_b128 a[128:131], v123 offset:4096\n"
    "ds_read_b128 a[184:187], v123 offset:18432\n"
    "ds_read_b128 a[20:23], v125 offset:5120\n"
    "ds_read_b128 a[76:79], v125 offset:19456\n"
    "ds_read_b128 a[132:135], v123 offset:5120\n"
    "ds_read_b128 a[188:191], v123 offset:19456\n"
    "ds_read_b128 a[24:27], v125 offset:6144\n"
    "ds_read_b128 a[80:83], v125 offset:20480\n"
    "ds_read_b128 a[136:139], v123 offset:6144\n"
    "ds_read_b128 a[192:195], v123 offset:20480\n"
    "ds_read_b128 a[28:31], v125 offset:7168\n"
    "ds_read_b128 a[84:87], v125 offset:21504\n"
    "ds_read_b128 a[140:143], v123 offset:7168\n"
    "ds_read_b128 a[196:199], v123 offset:21504\n"
    "ds_read_b128 a[32:35], v125 offset:8192\n"
    "ds_read_b128 a[88:91], v125 offset:22528\n"
    "ds_read_b128 a[144:147], v123 offset:8192\n"
    "ds_read_b128 a[200:203], v123 offset:22528\n"
    "ds_read_b128 a[36:39], v125 offset:9216\n"
    "ds_read_b128 a[92:95], v125 offset:23552\n"
    "ds_read_b128 a[148:151], v123 offset:9216\n"
    "ds_read_b128 a[204:207], v123 offset:23552\n"
    "ds_read_b128 a[40:43], v125 offset:10240\n"
    "ds_read_b128 a[96:99], v125 offset:24576\n"
    "ds_read_b128 a[152:155], v123 offset:10240\n"
    "ds_read_b128 a[208:211], v123 offset:24576\n"
    "ds_read_b128 a[44:47], v125 offset:11264\n"
    "ds_read_b128 a[100:103], v125 offset:25600\n"
    "ds_read_b128 a[156:159], v123 offset:11264\n"
    "ds_read_b128 a[212:215], v123 offset:25600\n"
    "ds_read_b128 a[48:51], v125 offset:12288\n"
    "ds_read_b128 a[104:107], v125 offset:26624\n"
    "ds_read_b128 a[160:163], v123 offset:12288\n"
    "ds_read_b128 a[216:219], v123 offset:26624\n"
    "ds_read_b128 a[52:55], v125 offset:13312\n"
    "ds_read_b128 a[108:111], v125 offset:27648\n"
    "ds_read_b128 a[164:167], v123 offset:13312\n"
    "ds_read_b128 a[220:223], v123 offset:27648\n"
    "ds_read_b64_tr_b16 v[208:209], v251 offset:0\n"
    "ds_read_b64_tr_b16 v[210:211], v251 offset:256\n"
    "ds_read_b64_tr_b16 v[212:213], v251 offset:4096\n"
    "ds_read_b64_tr_b16 v[214:215], v251 offset:4352\n"
    "ds_read_b64_tr_b16 v[216:217], v252 offset:0\n"
    "ds_read_b64_tr_b16 v[218:219], v252 offset:256\n"
    "ds_read_b64_tr_b16 v[220:221], v252 offset:4096\n"
    "ds_read_b64_tr_b16 v[222:223], v252 offset:4352\n"
    "s_sub_u32 s72, s10, 2688\n"
    "s_subb_u32 s73, s11, 0\n"
    "s_and_b32 s73, s73, 65535\n"
    "s_mov_b32 s74, 1051264\n"
    "s_mov_b32 s75, 131072\n"
    "s_waitcnt lgkmcnt(8)\n"
    "s_barrier\n"
    "v_rcp_f32 v192, v200\n"
    "v_rcp_f32 v193, v201\n"
    "v_rcp_f32 v194, v202\n"
    "v_rcp_f32 v195, v203\n"
    "v_mul_f32 v196, 0xbfb8aa3b, v192\n"
    "v_mul_f32 v197, 0xbfb8aa3b, v193\n"
    "v_mul_f32 v198, 0xbfb8aa3b, v194\n"
    "v_mul_f32 v199, 0xbfb8aa3b, v195\n"
    "v_mul_f32 v192, 0x3db8aa3b, v192\n"
    "v_mul_f32 v193, 0x3db8aa3b, v193\n"
    "v_mul_f32 v194, 0x3db8aa3b, v194\n"
    "v_mul_f32 v195, 0x3db8aa3b, v195\n"
    "s_waitcnt lgkmcnt(4)\n"
    "v_mfma_f32_16x16x32_bf16 v[160:163], v[208:211], a[0:3], 0\n"
    "s_add_u32 m0, s69, 77824\n"
    "v_mfma_f32_16x16x32_bf16 v[160:163], v[212:215], a[4:7], v[160:163]\n"
    "s_mov_b32 s96, 802816\n"
    "ds_read_b64_tr_b16 v[224:225], v251 offset:8192\n"
    "ds_read_b64_tr_b16 v[226:227], v251 offset:8448\n"
    "v_mfma_f32_16x16x32_bf16 v[164:167], v[208:211], a[56:59], 0\n"
    "s_nop 0\n"
    "ds_read_b64_tr_b16 v[228:229], v251 offset:12288\n"
    "ds_read_b64_tr_b16 v[230:231], v251 offset:12544\n"
    "v_mfma_f32_16x16x32_bf16 v[164:167], v[212:215], a[60:63], v[164:167]\n"
    "buffer_load_dwordx4 v250, s[76:79], s96 offen offset:0 lds\n"
    "v_mfma_f32_16x16x32_bf16 v[168:171], v[208:211], a[112:115], 0\n"
    "s_add_u32 m0, s69, 78848\n"
    "v_mfma_f32_16x16x32_bf16 v[168:171], v[212:215], a[116:119], v[168:171]\n"
    "s_mov_b32 s97, 831488\n"
    "v_mfma_f32_16x16x32_bf16 v[172:175], v[208:211], a[168:171], 0\n"
    "buffer_load_dwordx4 v207, s[76:79], s97 offen offset:0 lds\n"
    "v_mfma_f32_16x16x32_bf16 v[172:175], v[212:215], a[172:175], v[172:175]\n"
    "s_waitcnt lgkmcnt(4)\n"
    "v_mfma_f32_16x16x32_bf16 v[176:179], v[216:219], a[0:3], 0\n"
    "s_add_u32 m0, s69, 79872\n"
    "v_mfma_f32_16x16x32_bf16 v[176:179], v[220:223], a[4:7], v[176:179]\n"
    "s_mov_b32 s96, 860160\n"
    "ds_read_b64_tr_b16 v[208:209], v252 offset:8192\n"
    "ds_read_b64_tr_b16 v[210:211], v252 offset:8448\n"
    "v_mfma_f32_16x16x32_bf16 v[180:183], v[216:219], a[56:59], 0\n"
    "s_nop 0\n"
    "ds_read_b64_tr_b16 v[212:213], v252 offset:12288\n"
    "ds_read_b64_tr_b16 v[214:215], v252 offset:12544\n"
    "v_mfma_f32_16x16x32_bf16 v[180:183], v[220:223], a[60:63], v[180:183]\n"
    "buffer_load_dwordx4 v250, s[76:79], s96 offen offset:0 lds\n"
    "v_mfma_f32_16x16x32_bf16 v[184:187], v[216:219], a[112:115], 0\n"
    "s_add_u32 m0, s69, 80896\n"
    "v_mfma_f32_16x16x32_bf16 v[184:187], v[220:223], a[116:119], v[184:187]\n"
    "s_mov_b32 s97, 888832\n"
    "v_mfma_f32_16x16x32_bf16 v[188:191], v[216:219], a[168:171], 0\n"
    "buffer_load_dwordx4 v207, s[76:79], s97 offen offset:0 lds\n"
    "v_mfma_f32_16x16x32_bf16 v[188:191], v[220:223], a[172:175], v[188:191]\n"
    "s_waitcnt lgkmcnt(4)\n"
    "v_mfma_f32_16x16x32_bf16 v[160:163], v[224:227], a[8:11], v[160:163]\n"
    "s_add_u32 m0, s69, 81920\n"
    "v_mfma_f32_16x16x32_bf16 v[160:163], v[228:231], a[12:15], v[160:163]\n"
    "s_mov_b32 s96, 917504\n"
    "ds_read_b64_tr_b16 v[216:217], v251 offset:16384\n"
    "ds_read_b64_tr_b16 v[218:219], v251 offset:16640\n"
    "v_mfma_f32_16x16x32_bf16 v[164:167], v[224:227], a[64:67], v[164:167]\n"
    "s_nop 0\n"
    "ds_read_b64_tr_b16 v[220:221], v251 offset:20480\n"
    "ds_read_b64_tr_b16 v[222:223], v251 offset:20736\n"
    "v_mfma_f32_16x16x32_bf16 v[164:167], v[228:231], a[68:71], v[164:167]\n"
    "buffer_load_dwordx4 v250, s[76:79], s96 offen offset:0 lds\n"
    "v_mfma_f32_16x16x32_bf16 v[168:171], v[224:227], a[120:123], v[168:171]\n"
    "s_add_u32 m0, s69, 82944\n"
    "v_mfma_f32_16x16x32_bf16 v[168:171], v[228:231], a[124:127], v[168:171]\n"
    "s_mov_b32 s97, 946176\n"
    "v_mfma_f32_16x16x32_bf16 v[172:175], v[224:227], a[176:179], v[172:175]\n"
    "buffer_load_dwordx4 v207, s[76:79], s97 offen offset:0 lds\n"
    "v_mfma_f32_16x16x32_bf16 v[172:175], v[228:231], a[180:183], v[172:175]\n"
    "s_waitcnt lgkmcnt(4)\n"
    "v_mfma_f32_16x16x32_bf16 v[176:179], v[208:211], a[8:11], v[176:179]\n"
    "s_add_u32 m0, s69, 83968\n"
    "v_mfma_f32_16x16x32_bf16 v[176:179], v[212:215], a[12:15], v[176:179]\n"
    "s_mov_b32 s96, 974848\n"
    "ds_read_b64_tr_b16 v[224:225], v252 offset:16384\n"
    "ds_read_b64_tr_b16 v[226:227], v252 offset:16640\n"
    "v_mfma_f32_16x16x32_bf16 v[180:183], v[208:211], a[64:67], v[180:183]\n"
    "s_nop 0\n"
    "ds_read_b64_tr_b16 v[228:229], v252 offset:20480\n"
    "ds_read_b64_tr_b16 v[230:231], v252 offset:20736\n"
    "v_mfma_f32_16x16x32_bf16 v[180:183], v[212:215], a[68:71], v[180:183]\n"
    "buffer_load_dwordx4 v250, s[76:79], s96 offen offset:0 lds\n"
    "v_mfma_f32_16x16x32_bf16 v[184:187], v[208:211], a[120:123], v[184:187]\n"
    "s_add_u32 m0, s69, 106496\n"
    "v_mfma_f32_16x16x32_bf16 v[184:187], v[212:215], a[124:127], v[184:187]\n"
    "s_mov_b32 s96, 0\n"
    "v_mfma_f32_16x16x32_bf16 v[188:191], v[208:211], a[176:179], v[188:191]\n"
    "buffer_load_dwordx4 v249, s[88:91], s96 offen offset:0 lds\n"
    "v_mfma_f32_16x16x32_bf16 v[188:191], v[212:215], a[180:183], v[188:191]\n"
    "s_waitcnt lgkmcnt(4)\n"
    "v_mfma_f32_16x16x32_bf16 v[160:163], v[216:219], a[16:19], v[160:163]\n"
    "s_add_u32 m0, s69, 107520\n"
    "v_mfma_f32_16x16x32_bf16 v[160:163], v[220:223], a[20:23], v[160:163]\n"
    "s_mov_b32 s97, 65536\n"
    "ds_read_b64_tr_b16 v[208:209], v251 offset:24576\n"
    "ds_read_b64_tr_b16 v[210:211], v251 offset:24832\n"
    "v_mfma_f32_16x16x32_bf16 v[164:167], v[216:219], a[72:75], v[164:167]\n"
    "s_nop 0\n"
    "ds_read_b64_tr_b16 v[212:213], v252 offset:24576\n"
    "ds_read_b64_tr_b16 v[214:215], v252 offset:24832\n"
    "v_mfma_f32_16x16x32_bf16 v[164:167], v[220:223], a[76:79], v[164:167]\n"
    "buffer_load_dwordx4 v255, s[88:91], s97 offen offset:0 lds\n"
    "v_mfma_f32_16x16x32_bf16 v[168:171], v[216:219], a[128:131], v[168:171]\n"
    "s_add_u32 m0, s69, 108544\n"
    "v_mfma_f32_16x16x32_bf16 v[168:171], v[220:223], a[132:135], v[168:171]\n"
    "s_mov_b32 s96, 131072\n"
    "v_mfma_f32_16x16x32_bf16 v[172:175], v[216:219], a[184:187], v[172:175]\n"
    "buffer_load_dwordx4 v249, s[88:91], s96 offen offset:0 lds\n"
    "v_mfma_f32_16x16x32_bf16 v[172:175], v[220:223], a[188:191], v[172:175]\n"
    "s_waitcnt lgkmcnt(4)\n"
    "v_mfma_f32_16x16x32_bf16 v[176:179], v[224:227], a[16:19], v[176:179]\n"
    "s_waitcnt lgkmcnt(0) vmcnt(3)\n"
    "s_barrier\n"
    "v_mfma_f32_16x16x32_bf16 v[176:179], v[228:231], a[20:23], v[176:179]\n"
    "s_add_u32 m0, s69, 109568\n"
    "ds_read_b64_tr_b16 v[216:217], v253 offset:0\n"
    "ds_read_b64_tr_b16 v[218:219], v253 offset:256\n"
    "v_mfma_f32_16x16x32_bf16 v[180:183], v[224:227], a[72:75], v[180:183]\n"
    "s_mov_b32 s97, 196608\n"
    "ds_read_b64_tr_b16 v[220:221], v253 offset:4096\n"
    "ds_read_b64_tr_b16 v[222:223], v253 offset:4352\n"
    "v_mfma_f32_16x16x32_bf16 v[180:183], v[228:231], a[76:79], v[180:183]\n"
    "buffer_load_dwordx4 v255, s[88:91], s97 offen offset:0 lds\n"
    "v_mfma_f32_16x16x32_bf16 v[184:187], v[224:227], a[128:131], v[184:187]\n"
    "s_add_u32 m0, s69, 110592\n"
    "v_mfma_f32_16x16x32_bf16 v[184:187], v[228:231], a[132:135], v[184:187]\n"
    "s_mov_b32 s96, 262144\n"
    "v_mfma_f32_16x16x32_bf16 v[188:191], v[224:227], a[184:187], v[188:191]\n"
    "buffer_load_dwordx4 v249, s[88:91], s96 offen offset:0 lds\n"
    "v_mfma_f32_16x16x32_bf16 v[188:191], v[228:231], a[188:191], v[188:191]\n"
    "s_nop 0\n"
    "v_mfma_f32_16x16x32_bf16 v[160:163], v[208:211], a[24:27], v[160:163]\n"
    "s_add_u32 m0, s69, 111616\n"
    "v_mfma_f32_16x16x32_bf16 v[176:179], v[212:215], a[24:27], v[176:179]\n"
    "s_mov_b32 s97, 327680\n"
    "ds_read_b64_tr_b16 v[224:225], v254 offset:0\n"
    "ds_read_b64_tr_b16 v[226:227], v254 offset:256\n"
    "v_mfma_f32_16x16x32_bf16 v[164:167], v[208:211], a[80:83], v[164:167]\n"
    "s_nop 0\n"
    "ds_read_b64_tr_b16 v[228:229], v254 offset:4096\n"
    "ds_read_b64_tr_b16 v[230:231], v254 offset:4352\n"
    "v_mfma_f32_16x16x32_bf16 v[180:183], v[212:215], a[80:83], v[180:183]\n"
    "buffer_load_dwordx4 v255, s[88:91], s97 offen offset:0 lds\n"
    "v_mfma_f32_16x16x32_bf16 v[168:171], v[208:211], a[136:139], v[168:171]\n"
    "s_add_u32 m0, s69, 112640\n"
    "v_mfma_f32_16x16x32_bf16 v[184:187], v[212:215], a[136:139], v[184:187]\n"
    "s_mov_b32 s96, 393216\n"
    "v_mfma_f32_16x16x32_bf16 v[172:175], v[208:211], a[192:195], v[172:175]\n"
    "buffer_load_dwordx4 v249, s[88:91], s96 offen offset:0 lds\n"
    "v_mfma_f32_16x16x32_bf16 v[188:191], v[212:215], a[192:195], v[188:191]\n"
    "s_waitcnt lgkmcnt(4)\n"
    "v_mfma_f32_16x16x32_bf16 v[160:163], v[216:219], a[28:31], v[160:163]\n"
    "s_add_u32 m0, s69, 135168\n"
    "v_mfma_f32_16x16x32_bf16 v[160:163], v[220:223], a[32:35], v[160:163]\n"
    "s_mov_b32 s96, 1835008\n"
    "ds_read_b64_tr_b16 v[208:209], v253 offset:8192\n"
    "ds_read_b64_tr_b16 v[210:211], v253 offset:8448\n"
    "v_mfma_f32_16x16x32_bf16 v[164:167], v[216:219], a[84:87], v[164:167]\n"
    "s_nop 0\n"
    "ds_read_b64_tr_b16 v[212:213], v253 offset:12288\n"
    "ds_read_b64_tr_b16 v[214:215], v253 offset:12544\n"
    "v_mfma_f32_16x16x32_bf16 v[164:167], v[220:223], a[88:91], v[164:167]\n"
    "buffer_load_dwordx4 v249, s[88:91], s96 offen offset:0 lds\n"
    "v_mfma_f32_16x16x32_bf16 v[168:171], v[216:219], a[140:143], v[168:171]\n"
    "s_add_u32 m0, s69, 136192\n"
    "v_mfma_f32_16x16x32_bf16 v[168:171], v[220:223], a[144:147], v[168:171]\n"
    "s_mov_b32 s97, 1900544\n"
    "v_mfma_f32_16x16x32_bf16 v[172:175], v[216:219], a[196:199], v[172:175]\n"
    "v_mfma_f32_16x16x32_bf16 v[172:175], v[220:223], a[200:203], v[172:175]\n"
    "s_waitcnt lgkmcnt(4)\n"
    "v_mfma_f32_16x16x32_bf16 v[176:179], v[224:227], a[28:31], v[176:179]\n"
    "buffer_load_dwordx4 v255, s[88:91], s97 offen offset:0 lds\n"
    "v_mfma_f32_16x16x32_bf16 v[176:179], v[228:231], a[32:35], v[176:179]\n"
    "s_add_u32 m0, s69, 137216\n"
    "ds_read_b64_tr_b16 v[216:217], v254 offset:8192\n"
    "ds_read_b64_tr_b16 v[218:219], v254 offset:8448\n"
    "v_mfma_f32_16x16x32_bf16 v[180:183], v[224:227], a[84:87], v[180:183]\n"
    "s_mov_b32 s96, 1966080\n"
    "ds_read_b64_tr_b16 v[220:221], v254 offset:12288\n"
    "ds_read_b64_tr_b16 v[222:223], v254 offset:12544\n"
    "v_mfma_f32_16x16x32_bf16 v[180:183], v[228:231], a[88:91], v[180:183]\n"
    "v_mfma_f32_16x16x32_bf16 v[184:187], v[224:227], a[140:143], v[184:187]\n"
    "v_mfma_f32_16x16x32_bf16 v[184:187], v[228:231], a[144:147], v[184:187]\n"
    "v_mfma_f32_16x16x32_bf16 v[188:191], v[224:227], a[196:199], v[188:191]\n"
    "buffer_load_dwordx4 v249, s[88:91], s96 offen offset:0 lds\n"
    "v_mfma_f32_16x16x32_bf16 v[188:191], v[228:231], a[200:203], v[188:191]\n"
    "s_waitcnt lgkmcnt(4)\n"
    "v_mfma_f32_16x16x32_bf16 v[160:163], v[208:211], a[36:39], v[160:163]\n"
    "s_add_u32 m0, s69, 138240\n"
    "v_mfma_f32_16x16x32_bf16 v[160:163], v[212:215], a[40:43], v[160:163]\n"
    "s_mov_b32 s97, 2031616\n"
    "ds_read_b64_tr_b16 v[224:225], v253 offset:16384\n"
    "ds_read_b64_tr_b16 v[226:227], v253 offset:16640\n"
    "v_mfma_f32_16x16x32_bf16 v[164:167], v[208:211], a[92:95], v[164:167]\n"
    "s_nop 0\n"
    "ds_read_b64_tr_b16 v[228:229], v253 offset:20480\n"
    "ds_read_b64_tr_b16 v[230:231], v253 offset:20736\n"
    "v_mfma_f32_16x16x32_bf16 v[164:167], v[212:215], a[96:99], v[164:167]\n"
    "v_mfma_f32_16x16x32_bf16 v[168:171], v[208:211], a[148:151], v[168:171]\n"
    "buffer_load_dwordx4 v255, s[88:91], s97 offen offset:0 lds\n"
    "v_mfma_f32_16x16x32_bf16 v[168:171], v[212:215], a[152:155], v[168:171]\n"
    "s_add_u32 m0, s69, 139264\n"
    "v_mfma_f32_16x16x32_bf16 v[172:175], v[208:211], a[204:207], v[172:175]\n"
    "s_mov_b32 s96, 2097152\n"
    "v_mfma_f32_16x16x32_bf16 v[172:175], v[212:215], a[208:211], v[172:175]\n"
    "s_waitcnt lgkmcnt(4)\n"
    "v_mfma_f32_16x16x32_bf16 v[176:179], v[216:219], a[36:39], v[176:179]\n"
    "v_mfma_f32_16x16x32_bf16 v[176:179], v[220:223], a[40:43], v[176:179]\n"
    "s_nop 0\n"
    "ds_read_b64_tr_b16 v[208:209], v254 offset:16384\n"
    "ds_read_b64_tr_b16 v[210:211], v254 offset:16640\n"
    "v_mfma_f32_16x16x32_bf16 v[180:183], v[216:219], a[92:95], v[180:183]\n"
    "s_nop 0\n"
    "ds_read_b64_tr_b16 v[212:213], v254 offset:20480\n"
    "ds_read_b64_tr_b16 v[214:215], v254 offset:20736\n"
    "v_mfma_f32_16x16x32_bf16 v[180:183], v[220:223], a[96:99], v[180:183]\n"
    "buffer_load_dwordx4 v249, s[88:91], s96 offen offset:0 lds\n"
    "v_mfma_f32_16x16x32_bf16 v[184:187], v[216:219], a[148:151], v[184:187]\n"
    "s_add_u32 m0, s69, 140288\n"
    "v_mfma_f32_16x16x32_bf16 v[184:187], v[220:223], a[152:155], v[184:187]\n"
    "s_mov_b32 s97, 2162688\n"
    "v_mfma_f32_16x16x32_bf16 v[188:191], v[216:219], a[204:207], v[188:191]\n"
    "v_mfma_f32_16x16x32_bf16 v[188:191], v[220:223], a[208:211], v[188:191]\n"
    "s_waitcnt lgkmcnt(4)\n"
    "v_mfma_f32_16x16x32_bf16 v[160:163], v[224:227], a[44:47], v[160:163]\n"
    "buffer_load_dwordx4 v255, s[88:91], s97 offen offset:0 lds\n"
    "v_mfma_f32_16x16x32_bf16 v[160:163], v[228:231], a[48:51], v[160:163]\n"
    "s_add_u32 m0, s69, 141312\n"
    "ds_read_b64_tr_b16 v[216:217], v253 offset:24576\n"
    "ds_read_b64_tr_b16 v[218:219], v253 offset:24832\n"
    "v_mfma_f32_16x16x32_bf16 v[164:167], v[224:227], a[100:103], v[164:167]\n"
    "s_mov_b32 s96, 2228224\n"
    "ds_read_b64_tr_b16 v[220:221], v254 offset:24576\n"
    "ds_read_b64_tr_b16 v[222:223], v254 offset:24832\n"
    "v_mfma_f32_16x16x32_bf16 v[164:167], v[228:231], a[104:107], v[164:167]\n"
    "v_mfma_f32_16x16x32_bf16 v[168:171], v[224:227], a[156:159], v[168:171]\n"
    "v_mfma_f32_16x16x32_bf16 v[168:171], v[228:231], a[160:163], v[168:171]\n"
    "v_mfma_f32_16x16x32_bf16 v[172:175], v[224:227], a[212:215], v[172:175]\n"
    "buffer_load_dwordx4 v249, s[88:91], s96 offen offset:0 lds\n"
    "v_mfma_f32_16x16x32_bf16 v[172:175], v[228:231], a[216:219], v[172:175]\n"
    "s_waitcnt lgkmcnt(4)\n"
    "v_mfma_f32_16x16x32_bf16 v[176:179], v[208:211], a[44:47], v[176:179]\n"
    "s_waitcnt lgkmcnt(0) vmcnt(7)\n"
    "s_barrier\n"
    "v_mfma_f32_16x16x32_bf16 v[176:179], v[212:215], a[48:51], v[176:179]\n"
    "s_add_u32 m0, s69, 77824\n"
    "ds_read_b64_tr_b16 v[224:225], v253 offset:28672\n"
    "ds_read_b64_tr_b16 v[226:227], v253 offset:28928\n"
    "v_mfma_f32_16x16x32_bf16 v[180:183], v[208:211], a[100:103], v[180:183]\n"
    "s_mov_b32 s96, 0\n"
    "ds_read_b64_tr_b16 v[228:229], v253 offset:32768\n"
    "ds_read_b64_tr_b16 v[230:231], v253 offset:33024\n"
    "v_mfma_f32_16x16x32_bf16 v[180:183], v[212:215], a[104:107], v[180:183]\n"
    "buffer_load_dwordx4 v250, s[80:83], s96 offen offset:0 lds\n"
    "v_mfma_f32_16x16x32_bf16 v[184:187], v[208:211], a[156:159], v[184:187]\n"
    "s_add_u32 m0, s69, 78848\n"
    "v_mfma_f32_16x16x32_bf16 v[184:187], v[212:215], a[160:163], v[184:187]\n"
    "s_mov_b32 s97, 28672\n"
    "v_mfma_f32_16x16x32_bf16 v[188:191], v[208:211], a[212:215], v[188:191]\n"
    "buffer_load_dwordx4 v207, s[80:83], s97 offen offset:0 lds\n"
    "v_mfma_f32_16x16x32_bf16 v[188:191], v[212:215], a[216:219], v[188:191]\n"
    "s_nop 0\n"
    "v_mfma_f32_16x16x32_bf16 v[160:163], v[216:219], a[52:55], v[160:163]\n"
    "s_add_u32 m0, s69, 79872\n"
    "v_mfma_f32_16x16x32_bf16 v[176:179], v[220:223], a[52:55], v[176:179]\n"
    "s_mov_b32 s96, 57344\n"
    "ds_read_b64_tr_b16 v[208:209], v254 offset:28672\n"
    "ds_read_b64_tr_b16 v[210:211], v254 offset:28928\n"
    "v_mfma_f32_16x16x32_bf16 v[164:167], v[216:219], a[108:111], v[164:167]\n"
    "s_nop 0\n"
    "ds_read_b64_tr_b16 v[212:213], v254 offset:32768\n"
    "ds_read_b64_tr_b16 v[214:215], v254 offset:33024\n"
    "v_mfma_f32_16x16x32_bf16 v[180:183], v[220:223], a[108:111], v[180:183]\n"
    "v_mfma_f32_16x16x32_bf16 v[168:171], v[216:219], a[164:167], v[168:171]\n"
    "buffer_load_dwordx4 v250, s[80:83], s96 offen offset:0 lds\n"
    "v_mfma_f32_16x16x32_bf16 v[184:187], v[220:223], a[164:167], v[184:187]\n"
    "s_add_u32 m0, s69, 80896\n"
    "v_mfma_f32_16x16x32_bf16 v[172:175], v[216:219], a[220:223], v[172:175]\n"
    "s_mov_b32 s97, 86016\n"
    "v_mfma_f32_16x16x32_bf16 v[188:191], v[220:223], a[220:223], v[188:191]\n"
    "s_waitcnt lgkmcnt(4)\n"
    "v_mfma_f32_16x16x32_bf16 v[128:131], v[224:227], a[0:3], 0\n"
    "v_mfma_f32_16x16x32_bf16 v[128:131], v[228:231], a[4:7], v[128:131]\n"
    "s_nop 0\n"
    "ds_read_b64_tr_b16 v[216:217], v253 offset:36864\n"
    "ds_read_b64_tr_b16 v[218:219], v253 offset:37120\n"
    "v_mfma_f32_16x16x32_bf16 v[132:135], v[224:227], a[56:59], 0\n"
    "s_nop 0\n"
    "ds_read_b64_tr_b16 v[220:221], v253 offset:40960\n"
    "ds_read_b64_tr_b16 v[222:223], v253 offset:41216\n"
    "v_mfma_f32_16x16x32_bf16 v[132:135], v[228:231], a[60:63], v[132:135]\n"
    "buffer_load_dwordx4 v207, s[80:83], s97 offen offset:0 lds\n"
    "v_mfma_f32_16x16x32_bf16 v[136:139], v[224:227], a[112:115], 0\n"
    "s_add_u32 m0, s69, 81920\n"
    "v_mfma_f32_16x16x32_bf16 v[136:139], v[228:231], a[116:119], v[136:139]\n"
    "s_mov_b32 s96, 114688\n"
    "v_mfma_f32_16x16x32_bf16 v[140:143], v[224:227], a[168:171], 0\n"
    "v_mfma_f32_16x16x32_bf16 v[140:143], v[228:231], a[172:175], v[140:143]\n"
    "s_waitcnt lgkmcnt(4)\n"
    "v_mfma_f32_16x16x32_bf16 v[144:147], v[208:211], a[0:3], 0\n"
    "v_mul_f32 v160, v160, v196\n"
    "v_mul_f32 v161, v161, v196\n"
    "v_mfma_f32_16x16x32_bf16 v[144:147], v[212:215], a[4:7], v[144:147]\n"
    "s_nop 0\n"
    "ds_read_b64_tr_b16 v[224:225], v254 offset:36864\n"
    "ds_read_b64_tr_b16 v[226:227], v254 offset:37120\n"
    "v_mfma_f32_16x16x32_bf16 v[148:151], v[208:211], a[56:59], 0\n"
    "s_nop 0\n"
    "ds_read_b64_tr_b16 v[228:229], v254 offset:40960\n"
    "ds_read_b64_tr_b16 v[230:231], v254 offset:41216\n"
    "v_mfma_f32_16x16x32_bf16 v[148:151], v[212:215], a[60:63], v[148:151]\n"
    "buffer_load_dwordx4 v250, s[80:83], s96 offen offset:0 lds\n"
    "v_mfma_f32_16x16x32_bf16 v[152:155], v[208:211], a[112:115], 0\n"
    "s_add_u32 m0, s69, 82944\n"
    "v_mul_f32 v162, v162, v196\n"
    "v_mul_f32 v163, v163, v196\n"
    "v_mfma_f32_16x16x32_bf16 v[152:155], v[212:215], a[116:119], v[152:155]\n"
    "s_mov_b32 s97, 143360\n"
    "v_exp_f32 v160, v160\n"
    "v_mfma_f32_16x16x32_bf16 v[156:159], v[208:211], a[168:171], 0\n"
    "v_exp_f32 v161, v161\n"
    "v_mfma_f32_16x16x32_bf16 v[156:159], v[212:215], a[172:175], v[156:159]\n"
    "s_waitcnt lgkmcnt(4)\n"
    "v_mul_f32 v176, v176, v196\n"
    "v_mul_f32 v177, v177, v196\n"
    "v_mfma_f32_16x16x32_bf16 v[128:131], v[216:219], a[8:11], v[128:131]\n"
    "buffer_load_dwordx4 v207, s[80:83], s97 offen offset:0 lds\n"
    "v_mfma_f32_16x16x32_bf16 v[128:131], v[220:223], a[12:15], v[128:131]\n"
    "s_add_u32 m0, s69, 83968\n"
    "ds_read_b64_tr_b16 v[208:209], v253 offset:45056\n"
    "ds_read_b64_tr_b16 v[210:211], v253 offset:45312\n"
    "v_mfma_f32_16x16x32_bf16 v[132:135], v[216:219], a[64:67], v[132:135]\n"
    "s_mov_b32 s96, 172032\n"
    "ds_read_b64_tr_b16 v[212:213], v253 offset:49152\n"
    "ds_read_b64_tr_b16 v[214:215], v253 offset:49408\n"
    "v_mfma_f32_16x16x32_bf16 v[132:135], v[220:223], a[68:71], v[132:135]\n"
    "v_exp_f32 v162, v162\n"
    "v_mfma_f32_16x16x32_bf16 v[136:139], v[216:219], a[120:123], v[136:139]\n"
    "v_exp_f32 v163, v163\n"
    "v_mfma_f32_16x16x32_bf16 v[136:139], v[220:223], a[124:127], v[136:139]\n"
    "v_mul_f32 v178, v178, v196\n"
    "v_mul_f32 v179, v179, v196\n"
    "v_mfma_f32_16x16x32_bf16 v[140:143], v[216:219], a[176:179], v[140:143]\n"
    "buffer_load_dwordx4 v250, s[80:83], s96 offen offset:0 lds\n"
    "v_mfma_f32_16x16x32_bf16 v[140:143], v[220:223], a[180:183], v[140:143]\n"
    "s_waitcnt lgkmcnt(4)\n"
    "v_exp_f32 v176, v176\n"
    "v_mfma_f32_16x16x32_bf16 v[144:147], v[224:227], a[8:11], v[144:147]\n"
    "s_add_u32 m0, s69, 106496\n"
    "v_exp_f32 v177, v177\n"
    "v_mfma_f32_16x16x32_bf16 v[144:147], v[228:231], a[12:15], v[144:147]\n"
    "s_mov_b32 s96, 802816\n"
    "ds_read_b64_tr_b16 v[216:217], v254 offset:45056\n"
    "ds_read_b64_tr_b16 v[218:219], v254 offset:45312\n"
    "v_mfma_f32_16x16x32_bf16 v[148:151], v[224:227], a[64:67], v[148:151]\n"
    "s_nop 0\n"
    "ds_read_b64_tr_b16 v[220:221], v254 offset:49152\n"
    "ds_read_b64_tr_b16 v[222:223], v254 offset:49408\n"
    "v_mfma_f32_16x16x32_bf16 v[148:151], v[228:231], a[68:71], v[148:151]\n"
    "v_fma_f32 v160, v160, v200, v200\n"
    "v_fma_f32 v161, v161, v200, v200\n"
    "v_mfma_f32_16x16x32_bf16 v[152:155], v[224:227], a[120:123], v[152:155]\n"
    "v_exp_f32 v178, v178\n"
    "v_mfma_f32_16x16x32_bf16 v[152:155], v[228:231], a[124:127], v[152:155]\n"
    "v_exp_f32 v179, v179\n"
    "v_mfma_f32_16x16x32_bf16 v[156:159], v[224:227], a[176:179], v[156:159]\n"
    "v_fma_f32 v162, v162, v200, v200\n"
    "v_fma_f32 v163, v163, v200, v200\n"
    "v_mfma_f32_16x16x32_bf16 v[156:159], v[228:231], a[180:183], v[156:159]\n"
    "s_waitcnt lgkmcnt(4)\n"
    "v_rcp_f32 v160, v160\n"
    "v_mfma_f32_16x16x32_bf16 v[128:131], v[208:211], a[16:19], v[128:131]\n"
    "v_rcp_f32 v161, v161\n"
    "v_mfma_f32_16x16x32_bf16 v[128:131], v[212:215], a[20:23], v[128:131]\n"
    "s_nop 0\n"
    "ds_read_b64_tr_b16 v[224:225], v253 offset:53248\n"
    "ds_read_b64_tr_b16 v[226:227], v253 offset:53504\n"
    "v_mfma_f32_16x16x32_bf16 v[132:135], v[208:211], a[72:75], v[132:135]\n"
    "s_nop 0\n"
    "ds_read_b64_tr_b16 v[228:229], v254 offset:53248\n"
    "ds_read_b64_tr_b16 v[230:231], v254 offset:53504\n"
    "v_mfma_f32_16x16x32_bf16 v[132:135], v[212:215], a[76:79], v[132:135]\n"
    "v_fma_f32 v176, v176, v200, v200\n"
    "v_fma_f32 v177, v177, v200, v200\n"
    "v_mfma_f32_16x16x32_bf16 v[136:139], v[208:211], a[128:131], v[136:139]\n"
    "v_rcp_f32 v162, v162\n"
    "v_mfma_f32_16x16x32_bf16 v[136:139], v[212:215], a[132:135], v[136:139]\n"
    "v_rcp_f32 v163, v163\n"
    "v_mfma_f32_16x16x32_bf16 v[140:143], v[208:211], a[184:187], v[140:143]\n"
    "v_fma_f32 v178, v178, v200, v200\n"
    "v_fma_f32 v179, v179, v200, v200\n"
    "v_mfma_f32_16x16x32_bf16 v[140:143], v[212:215], a[188:191], v[140:143]\n"
    "s_waitcnt lgkmcnt(4)\n"
    "v_rcp_f32 v176, v176\n"
    "v_mfma_f32_16x16x32_bf16 v[144:147], v[216:219], a[16:19], v[144:147]\n"
    "s_waitcnt lgkmcnt(0) vmcnt(7)\n"
    "s_barrier\n"
    "v_rcp_f32 v177, v177\n"
    "v_mfma_f32_16x16x32_bf16 v[144:147], v[220:223], a[20:23], v[144:147]\n"
    "s_nop 0\n"
    "ds_read_b64_tr_b16 v[208:209], v251 offset:0\n"
    "ds_read_b64_tr_b16 v[210:211], v251 offset:256\n"
    "v_mfma_f32_16x16x32_bf16 v[148:151], v[216:219], a[72:75], v[148:151]\n"
    "s_nop 0\n"
    "ds_read_b64_tr_b16 v[212:213], v251 offset:4096\n"
    "ds_read_b64_tr_b16 v[214:215], v251 offset:4352\n"
    "v_mfma_f32_16x16x32_bf16 v[148:151], v[220:223], a[76:79], v[148:151]\n"
    "buffer_load_dwordx4 v250, s[80:83], s96 offen offset:0 lds\n"
    "v_mfma_f32_16x16x32_bf16 v[152:155], v[216:219], a[128:131], v[152:155]\n"
    "s_add_u32 m0, s69, 107520\n"
    "v_rcp_f32 v178, v178\n"
    "v_mfma_f32_16x16x32_bf16 v[152:155], v[220:223], a[132:135], v[152:155]\n"
    "s_mov_b32 s97, 831488\n"
    "v_rcp_f32 v179, v179\n"
    "v_mfma_f32_16x16x32_bf16 v[156:159], v[216:219], a[184:187], v[156:159]\n"
    "v_mul_f32 v164, v164, v197\n"
    "v_mul_f32 v165, v165, v197\n"
    "v_mfma_f32_16x16x32_bf16 v[156:159], v[220:223], a[188:191], v[156:159]\n"
    "s_nop 0\n"
    "v_mul_f32 v166, v166, v197\n"
    "v_mul_f32 v167, v167, v197\n"
    "v_mfma_f32_16x16x32_bf16 v[128:131], v[224:227], a[24:27], v[128:131]\n"
    "buffer_load_dwordx4 v207, s[80:83], s97 offen offset:0 lds\n"
    "v_mfma_f32_16x16x32_bf16 v[144:147], v[228:231], a[24:27], v[144:147]\n"
    "s_add_u32 m0, s69, 108544\n"
    "ds_read_b64_tr_b16 v[216:217], v252 offset:0\n"
    "ds_read_b64_tr_b16 v[218:219], v252 offset:256\n"
    "v_mfma_f32_16x16x32_bf16 v[132:135], v[224:227], a[80:83], v[132:135]\n"
    "s_mov_b32 s96, 860160\n"
    "ds_read_b64_tr_b16 v[220:221], v252 offset:4096\n"
    "ds_read_b64_tr_b16 v[222:223], v252 offset:4352\n"
    "v_mfma_f32_16x16x32_bf16 v[148:151], v[228:231], a[80:83], v[148:151]\n"
    "v_exp_f32 v164, v164\n"
    "v_mfma_f32_16x16x32_bf16 v[136:139], v[224:227], a[136:139], v[136:139]\n"
    "v_exp_f32 v165, v165\n"
    "v_mfma_f32_16x16x32_bf16 v[152:155], v[228:231], a[136:139], v[152:155]\n"
    "v_mul_f32 v180, v180, v197\n"
    "v_mul_f32 v181, v181, v197\n"
    "v_mfma_f32_16x16x32_bf16 v[140:143], v[224:227], a[192:195], v[140:143]\n"
    "buffer_load_dwordx4 v250, s[80:83], s96 offen offset:0 lds\n"
    "v_mfma_f32_16x16x32_bf16 v[156:159], v[228:231], a[192:195], v[156:159]\n"
    "s_waitcnt lgkmcnt(4)\n"
    "v_exp_f32 v166, v166\n"
    "v_mfma_f32_16x16x32_bf16 v[128:131], v[208:211], a[28:31], v[128:131]\n"
    "s_add_u32 m0, s69, 109568\n"
    "v_exp_f32 v167, v167\n"
    "v_mfma_f32_16x16x32_bf16 v[128:131], v[212:215], a[32:35], v[128:131]\n"
    "s_mov_b32 s97, 888832\n"
    "ds_read_b64_tr_b16 v[224:225], v251 offset:8192\n"
    "ds_read_b64_tr_b16 v[226:227], v251 offset:8448\n"
    "v_mfma_f32_16x16x32_bf16 v[132:135], v[208:211], a[84:87], v[132:135]\n"
    "s_nop 0\n"
    "ds_read_b64_tr_b16 v[228:229], v251 offset:12288\n"
    "ds_read_b64_tr_b16 v[230:231], v251 offset:12544\n"
    "v_mfma_f32_16x16x32_bf16 v[132:135], v[212:215], a[88:91], v[132:135]\n"
    "v_mul_f32 v182, v182, v197\n"
    "v_mul_f32 v183, v183, v197\n"
    "v_mfma_f32_16x16x32_bf16 v[136:139], v[208:211], a[140:143], v[136:139]\n"
    "buffer_load_dwordx4 v207, s[80:83], s97 offen offset:0 lds\n"
    "v_mfma_f32_16x16x32_bf16 v[136:139], v[212:215], a[144:147], v[136:139]\n"
    "s_add_u32 m0, s69, 110592\n"
    "v_exp_f32 v180, v180\n"
    "v_mfma_f32_16x16x32_bf16 v[140:143], v[208:211], a[196:199], v[140:143]\n"
    "s_mov_b32 s96, 917504\n"
    "v_exp_f32 v181, v181\n"
    "v_mfma_f32_16x16x32_bf16 v[140:143], v[212:215], a[200:203], v[140:143]\n"
    "s_waitcnt lgkmcnt(4)\n"
    "v_fma_f32 v164, v164, v201, v201\n"
    "v_fma_f32 v165, v165, v201, v201\n"
    "v_mfma_f32_16x16x32_bf16 v[144:147], v[216:219], a[28:31], v[144:147]\n"
    "v_exp_f32 v182, v182\n"
    "v_mfma_f32_16x16x32_bf16 v[144:147], v[220:223], a[32:35], v[144:147]\n"
    "s_nop 0\n"
    "ds_read_b64_tr_b16 v[208:209], v252 offset:8192\n"
    "ds_read_b64_tr_b16 v[210:211], v252 offset:8448\n"
    "v_mfma_f32_16x16x32_bf16 v[148:151], v[216:219], a[84:87], v[148:151]\n"
    "s_nop 0\n"
    "ds_read_b64_tr_b16 v[212:213], v252 offset:12288\n"
    "ds_read_b64_tr_b16 v[214:215], v252 offset:12544\n"
    "v_mfma_f32_16x16x32_bf16 v[148:151], v[220:223], a[88:91], v[148:151]\n"
    "buffer_load_dwordx4 v250, s[80:83], s96 offen offset:0 lds\n"
    "v_mfma_f32_16x16x32_bf16 v[152:155], v[216:219], a[140:143], v[152:155]\n"
    "s_add_u32 m0, s69, 111616\n"
    "v_exp_f32 v183, v183\n"
    "v_mfma_f32_16x16x32_bf16 v[152:155], v[220:223], a[144:147], v[152:155]\n"
    "s_mov_b32 s97, 946176\n"
    "v_fma_f32 v166, v166, v201, v201\n"
    "v_fma_f32 v167, v167, v201, v201\n"
    "v_mfma_f32_16x16x32_bf16 v[156:159], v[216:219], a[196:199], v[156:159]\n"
    "v_rcp_f32 v164, v164\n"
    "v_mfma_f32_16x16x32_bf16 v[156:159], v[220:223], a[200:203], v[156:159]\n"
    "s_waitcnt lgkmcnt(4)\n"
    "v_rcp_f32 v165, v165\n"
    "v_mfma_f32_16x16x32_bf16 v[128:131], v[224:227], a[36:39], v[128:131]\n"
    "buffer_load_dwordx4 v207, s[80:83], s97 offen offset:0 lds\n"
    "v_mfma_f32_16x16x32_bf16 v[128:131], v[228:231], a[40:43], v[128:131]\n"
    "s_add_u32 m0, s69, 112640\n"
    "ds_read_b64_tr_b16 v[216:217], v251 offset:16384\n"
    "ds_read_b64_tr_b16 v[218:219], v251 offset:16640\n"
    "v_mfma_f32_16x16x32_bf16 v[132:135], v[224:227], a[92:95], v[132:135]\n"
    "s_mov_b32 s96, 974848\n"
    "ds_read_b64_tr_b16 v[220:221], v251 offset:20480\n"
    "ds_read_b64_tr_b16 v[222:223], v251 offset:20736\n"
    "v_mfma_f32_16x16x32_bf16 v[132:135], v[228:231], a[96:99], v[132:135]\n"
    "v_fma_f32 v180, v180, v201, v201\n"
    "v_fma_f32 v181, v181, v201, v201\n"
    "v_mfma_f32_16x16x32_bf16 v[136:139], v[224:227], a[148:151], v[136:139]\n"
    "v_rcp_f32 v166, v166\n"
    "v_mfma_f32_16x16x32_bf16 v[136:139], v[228:231], a[152:155], v[136:139]\n"
    "v_rcp_f32 v167, v167\n"
    "v_mfma_f32_16x16x32_bf16 v[140:143], v[224:227], a[204:207], v[140:143]\n"
    "buffer_load_dwordx4 v250, s[80:83], s96 offen offset:0 lds\n"
    "v_mfma_f32_16x16x32_bf16 v[140:143], v[228:231], a[208:211], v[140:143]\n"
    "s_waitcnt lgkmcnt(4)\n"
    "v_fma_f32 v182, v182, v201, v201\n"
    "v_fma_f32 v183, v183, v201, v201\n"
    "v_mfma_f32_16x16x32_bf16 v[144:147], v[208:211], a[36:39], v[144:147]\n"
    "s_add_u32 m0, s69, 135168\n"
    "v_rcp_f32 v180, v180\n"
    "v_mfma_f32_16x16x32_bf16 v[144:147], v[212:215], a[40:43], v[144:147]\n"
    "s_mov_b32 s96, 128\n"
    "ds_read_b64_tr_b16 v[224:225], v252 offset:16384\n"
    "ds_read_b64_tr_b16 v[226:227], v252 offset:16640\n"
    "v_mfma_f32_16x16x32_bf16 v[148:151], v[208:211], a[92:95], v[148:151]\n"
    "s_nop 0\n"
    "ds_read_b64_tr_b16 v[228:229], v252 offset:20480\n"
    "ds_read_b64_tr_b16 v[230:231], v252 offset:20736\n"
    "v_mfma_f32_16x16x32_bf16 v[148:151], v[212:215], a[96:99], v[148:151]\n"
    "v_rcp_f32 v181, v181\n"
    "v_mfma_f32_16x16x32_bf16 v[152:155], v[208:211], a[148:151], v[152:155]\n"
    "v_rcp_f32 v182, v182\n"
    "v_mfma_f32_16x16x32_bf16 v[152:155], v[212:215], a[152:155], v[152:155]\n"
    "v_rcp_f32 v183, v183\n"
    "v_mfma_f32_16x16x32_bf16 v[156:159], v[208:211], a[204:207], v[156:159]\n"
    "v_mul_f32 v168, v168, v198\n"
    "v_mul_f32 v169, v169, v198\n"
    "v_mfma_f32_16x16x32_bf16 v[156:159], v[212:215], a[208:211], v[156:159]\n"
    "s_waitcnt lgkmcnt(4)\n"
    "v_mul_f32 v170, v170, v198\n"
    "v_mul_f32 v171, v171, v198\n"
    "v_mfma_f32_16x16x32_bf16 v[128:131], v[216:219], a[44:47], v[128:131]\n"
    "v_exp_f32 v168, v168\n"
    "v_mfma_f32_16x16x32_bf16 v[128:131], v[220:223], a[48:51], v[128:131]\n"
    "s_nop 0\n"
    "ds_read_b64_tr_b16 v[208:209], v251 offset:24576\n"
    "ds_read_b64_tr_b16 v[210:211], v251 offset:24832\n"
    "v_mfma_f32_16x16x32_bf16 v[132:135], v[216:219], a[100:103], v[132:135]\n"
    "s_nop 0\n"
    "ds_read_b64_tr_b16 v[212:213], v252 offset:24576\n"
    "ds_read_b64_tr_b16 v[214:215], v252 offset:24832\n"
    "v_mfma_f32_16x16x32_bf16 v[132:135], v[220:223], a[104:107], v[132:135]\n"
    "v_exp_f32 v169, v169\n"
    "v_mfma_f32_16x16x32_bf16 v[136:139], v[216:219], a[156:159], v[136:139]\n"
    "v_mul_f32 v184, v184, v198\n"
    "v_mul_f32 v185, v185, v198\n"
    "v_mfma_f32_16x16x32_bf16 v[136:139], v[220:223], a[160:163], v[136:139]\n"
    "v_exp_f32 v170, v170\n"
    "v_mfma_f32_16x16x32_bf16 v[140:143], v[216:219], a[212:215], v[140:143]\n"
    "v_exp_f32 v171, v171\n"
    "v_mfma_f32_16x16x32_bf16 v[140:143], v[220:223], a[216:219], v[140:143]\n"
    "s_waitcnt lgkmcnt(4)\n"
    "v_mul_f32 v186, v186, v198\n"
    "v_mul_f32 v187, v187, v198\n"
    "v_mfma_f32_16x16x32_bf16 v[144:147], v[224:227], a[44:47], v[144:147]\n"
    "s_waitcnt lgkmcnt(0) vmcnt(7)\n"
    "s_barrier\n"
    "v_exp_f32 v184, v184\n"
    "v_mfma_f32_16x16x32_bf16 v[144:147], v[228:231], a[48:51], v[144:147]\n"
    "s_nop 0\n"
    "ds_read_b64_tr_b16 v[216:217], v253 offset:0\n"
    "ds_read_b64_tr_b16 v[218:219], v253 offset:256\n"
    "v_mfma_f32_16x16x32_bf16 v[148:151], v[224:227], a[100:103], v[148:151]\n"
    "s_nop 0\n"
    "ds_read_b64_tr_b16 v[220:221], v253 offset:4096\n"
    "ds_read_b64_tr_b16 v[222:223], v253 offset:4352\n"
    "v_mfma_f32_16x16x32_bf16 v[148:151], v[228:231], a[104:107], v[148:151]\n"
    "buffer_load_dwordx4 v249, s[88:91], s96 offen offset:0 lds\n"
    "v_mfma_f32_16x16x32_bf16 v[152:155], v[224:227], a[156:159], v[152:155]\n"
    "s_add_u32 m0, s69, 136192\n"
    "v_exp_f32 v185, v185\n"
    "v_mfma_f32_16x16x32_bf16 v[152:155], v[228:231], a[160:163], v[152:155]\n"
    "s_mov_b32 s97, 65664\n"
    "v_fma_f32 v168, v168, v202, v202\n"
    "v_fma_f32 v169, v169, v202, v202\n"
    "v_mfma_f32_16x16x32_bf16 v[156:159], v[224:227], a[212:215], v[156:159]\n"
    "v_exp_f32 v186, v186\n"
    "v_mfma_f32_16x16x32_bf16 v[156:159], v[228:231], a[216:219], v[156:159]\n"
    "s_nop 0\n"
    "v_exp_f32 v187, v187\n"
    "v_mfma_f32_16x16x32_bf16 v[128:131], v[208:211], a[52:55], v[128:131]\n"
    "buffer_load_dwordx4 v255, s[88:91], s97 offen offset:0 lds\n"
    "v_mfma_f32_16x16x32_bf16 v[144:147], v[212:215], a[52:55], v[144:147]\n"
    "s_add_u32 m0, s69, 137216\n"
    "ds_read_b64_tr_b16 v[224:225], v254 offset:0\n"
    "ds_read_b64_tr_b16 v[226:227], v254 offset:256\n"
    "v_mfma_f32_16x16x32_bf16 v[132:135], v[208:211], a[108:111], v[132:135]\n"
    "s_mov_b32 s96, 131200\n"
    "ds_read_b64_tr_b16 v[228:229], v254 offset:4096\n"
    "ds_read_b64_tr_b16 v[230:231], v254 offset:4352\n"
    "v_mfma_f32_16x16x32_bf16 v[148:151], v[212:215], a[108:111], v[148:151]\n"
    "v_fma_f32 v170, v170, v202, v202\n"
    "v_fma_f32 v171, v171, v202, v202\n"
    "v_mfma_f32_16x16x32_bf16 v[136:139], v[208:211], a[164:167], v[136:139]\n"
    "v_rcp_f32 v168, v168\n"
    "v_mfma_f32_16x16x32_bf16 v[152:155], v[212:215], a[164:167], v[152:155]\n"
    "v_rcp_f32 v169, v169\n"
    "v_mfma_f32_16x16x32_bf16 v[140:143], v[208:211], a[220:223], v[140:143]\n"
    "buffer_load_dwordx4 v249, s[88:91], s96 offen offset:0 lds\n"
    "v_mfma_f32_16x16x32_bf16 v[156:159], v[212:215], a[220:223], v[156:159]\n"
    "s_waitcnt lgkmcnt(4)\n"
    "v_fma_f32 v184, v184, v202, v202\n"
    "v_fma_f32 v185, v185, v202, v202\n"
    "v_mfma_f32_16x16x32_bf16 v[20:23], v[216:219], a[0:3], 0\n"
    "s_add_u32 m0, s69, 138240\n"
    "v_rcp_f32 v170, v170\n"
    "v_mfma_f32_16x16x32_bf16 v[20:23], v[220:223], a[4:7], v[20:23]\n"
    "s_mov_b32 s97, 196736\n"
    "ds_read_b64_tr_b16 v[208:209], v253 offset:8192\n"
    "ds_read_b64_tr_b16 v[210:211], v253 offset:8448\n"
    "v_mfma_f32_16x16x32_bf16 v[52:55], v[216:219], a[56:59], 0\n"
    "s_nop 0\n"
    "ds_read_b64_tr_b16 v[212:213], v253 offset:12288\n"
    "ds_read_b64_tr_b16 v[214:215], v253 offset:12544\n"
    "v_mfma_f32_16x16x32_bf16 v[52:55], v[220:223], a[60:63], v[52:55]\n"
    "v_rcp_f32 v171, v171\n"
    "v_mfma_f32_16x16x32_bf16 v[84:87], v[216:219], a[112:115], 0\n"
    "buffer_load_dwordx4 v255, s[88:91], s97 offen offset:0 lds\n"
    "v_mfma_f32_16x16x32_bf16 v[84:87], v[220:223], a[116:119], v[84:87]\n"
    "s_add_u32 m0, s69, 139264\n"
    "v_fma_f32 v186, v186, v202, v202\n"
    "v_fma_f32 v187, v187, v202, v202\n"
    "v_mfma_f32_16x16x32_bf16 v[116:119], v[216:219], a[168:171], 0\n"
    "s_mov_b32 s96, 262272\n"
    "v_rcp_f32 v184, v184\n"
    "v_mfma_f32_16x16x32_bf16 v[116:119], v[220:223], a[172:175], v[116:119]\n"
    "s_waitcnt lgkmcnt(4)\n"
    "v_rcp_f32 v185, v185\n"
    "v_mfma_f32_16x16x32_bf16 v[28:31], v[224:227], a[0:3], 0\n"
    "v_rcp_f32 v186, v186\n"
    "v_mfma_f32_16x16x32_bf16 v[28:31], v[228:231], a[4:7], v[28:31]\n"
    "s_nop 0\n"
    "ds_read_b64_tr_b16 v[216:217], v254 offset:8192\n"
    "ds_read_b64_tr_b16 v[218:219], v254 offset:8448\n"
    "v_mfma_f32_16x16x32_bf16 v[60:63], v[224:227], a[56:59], 0\n"
    "s_nop 0\n"
    "ds_read_b64_tr_b16 v[220:221], v254 offset:12288\n"
    "ds_read_b64_tr_b16 v[222:223], v254 offset:12544\n"
    "v_mfma_f32_16x16x32_bf16 v[60:63], v[228:231], a[60:63], v[60:63]\n"
    "buffer_load_dwordx4 v249, s[88:91], s96 offen offset:0 lds\n"
    "v_mfma_f32_16x16x32_bf16 v[92:95], v[224:227], a[112:115], 0\n"
    "s_add_u32 m0, s69, 140288\n"
    "v_rcp_f32 v187, v187\n"
    "v_mfma_f32_16x16x32_bf16 v[92:95], v[228:231], a[116:119], v[92:95]\n"
    "s_mov_b32 s97, 327808\n"
    "v_mul_f32 v172, v172, v199\n"
    "v_mul_f32 v173, v173, v199\n"
    "v_mfma_f32_16x16x32_bf16 v[124:127], v[224:227], a[168:171], 0\n"
    "v_mul_f32 v174, v174, v199\n"
    "v_mul_f32 v175, v175, v199\n"
    "v_mfma_f32_16x16x32_bf16 v[124:127], v[228:231], a[172:175], v[124:127]\n"
    "s_waitcnt lgkmcnt(4)\n"
    "v_exp_f32 v172, v172\n"
    "v_mfma_f32_16x16x32_bf16 v[20:23], v[208:211], a[8:11], v[20:23]\n"
    "buffer_load_dwordx4 v255, s[88:91], s97 offen offset:0 lds\n"
    "v_mfma_f32_16x16x32_bf16 v[20:23], v[212:215], a[12:15], v[20:23]\n"
    "s_add_u32 m0, s69, 141312\n"
    "ds_read_b64_tr_b16 v[224:225], v253 offset:16384\n"
    "ds_read_b64_tr_b16 v[226:227], v253 offset:16640\n"
    "v_mfma_f32_16x16x32_bf16 v[52:55], v[208:211], a[64:67], v[52:55]\n"
    "s_mov_b32 s96, 393344\n"
    "ds_read_b64_tr_b16 v[228:229], v253 offset:20480\n"
    "ds_read_b64_tr_b16 v[230:231], v253 offset:20736\n"
    "v_mfma_f32_16x16x32_bf16 v[52:55], v[212:215], a[68:71], v[52:55]\n"
    "v_exp_f32 v173, v173\n"
    "v_mfma_f32_16x16x32_bf16 v[84:87], v[208:211], a[120:123], v[84:87]\n"
    "v_mul_f32 v188, v188, v199\n"
    "v_mul_f32 v189, v189, v199\n"
    "v_mfma_f32_16x16x32_bf16 v[84:87], v[212:215], a[124:127], v[84:87]\n"
    "v_exp_f32 v174, v174\n"
    "v_mfma_f32_16x16x32_bf16 v[116:119], v[208:211], a[176:179], v[116:119]\n"
    "buffer_load_dwordx4 v249, s[88:91], s96 offen offset:0 lds\n"
    "v_mfma_f32_16x16x32_bf16 v[116:119], v[212:215], a[180:183], v[116:119]\n"
    "s_waitcnt lgkmcnt(4)\n"
    "v_exp_f32 v175, v175\n"
    "v_mfma_f32_16x16x32_bf16 v[28:31], v[216:219], a[8:11], v[28:31]\n"
    "s_add_u32 m0, s69, 77824\n"
    "v_mul_f32 v190, v190, v199\n"
    "v_mul_f32 v191, v191, v199\n"
    "v_mfma_f32_16x16x32_bf16 v[28:31], v[220:223], a[12:15], v[28:31]\n"
    "s_mov_b32 s96, 1835136\n"
    "ds_read_b64_tr_b16 v[208:209], v254 offset:16384\n"
    "ds_read_b64_tr_b16 v[210:211], v254 offset:16640\n"
    "v_mfma_f32_16x16x32_bf16 v[60:63], v[216:219], a[64:67], v[60:63]\n"
    "s_nop 0\n"
    "ds_read_b64_tr_b16 v[212:213], v254 offset:20480\n"
    "ds_read_b64_tr_b16 v[214:215], v254 offset:20736\n"
    "v_mfma_f32_16x16x32_bf16 v[60:63], v[220:223], a[68:71], v[60:63]\n"
    "v_exp_f32 v188, v188\n"
    "v_mfma_f32_16x16x32_bf16 v[92:95], v[216:219], a[120:123], v[92:95]\n"
    "v_exp_f32 v189, v189\n"
    "v_mfma_f32_16x16x32_bf16 v[92:95], v[220:223], a[124:127], v[92:95]\n"
    "v_fma_f32 v172, v172, v203, v203\n"
    "v_fma_f32 v173, v173, v203, v203\n"
    "v_mfma_f32_16x16x32_bf16 v[124:127], v[216:219], a[176:179], v[124:127]\n"
    "v_exp_f32 v190, v190\n"
    "v_mfma_f32_16x16x32_bf16 v[124:127], v[220:223], a[180:183], v[124:127]\n"
    "s_waitcnt lgkmcnt(4)\n"
    "v_exp_f32 v191, v191\n"
    "v_mfma_f32_16x16x32_bf16 v[20:23], v[224:227], a[16:19], v[20:23]\n"
    "v_fma_f32 v174, v174, v203, v203\n"
    "v_fma_f32 v175, v175, v203, v203\n"
    "v_mfma_f32_16x16x32_bf16 v[20:23], v[228:231], a[20:23], v[20:23]\n"
    "s_nop 0\n"
    "ds_read_b64_tr_b16 v[216:217], v253 offset:24576\n"
    "ds_read_b64_tr_b16 v[218:219], v253 offset:24832\n"
    "v_mfma_f32_16x16x32_bf16 v[52:55], v[224:227], a[72:75], v[52:55]\n"
    "s_nop 0\n"
    "ds_read_b64_tr_b16 v[220:221], v254 offset:24576\n"
    "ds_read_b64_tr_b16 v[222:223], v254 offset:24832\n"
    "v_mfma_f32_16x16x32_bf16 v[52:55], v[228:231], a[76:79], v[52:55]\n"
    "v_rcp_f32 v172, v172\n"
    "v_mfma_f32_16x16x32_bf16 v[84:87], v[224:227], a[128:131], v[84:87]\n"
    "v_rcp_f32 v173, v173\n"
    "v_mfma_f32_16x16x32_bf16 v[84:87], v[228:231], a[132:135], v[84:87]\n"
    "v_fma_f32 v188, v188, v203, v203\n"
    "v_fma_f32 v189, v189, v203, v203\n"
    "v_mfma_f32_16x16x32_bf16 v[116:119], v[224:227], a[184:187], v[116:119]\n"
    "v_rcp_f32 v174, v174\n"
    "v_mfma_f32_16x16x32_bf16 v[116:119], v[228:231], a[188:191], v[116:119]\n"
    "s_waitcnt lgkmcnt(4)\n"
    "v_rcp_f32 v175, v175\n"
    "v_mfma_f32_16x16x32_bf16 v[28:31], v[208:211], a[16:19], v[28:31]\n"
    "s_waitcnt lgkmcnt(0) vmcnt(7)\n"
    "s_barrier\n"
    "v_fma_f32 v190, v190, v203, v203\n"
    "v_fma_f32 v191, v191, v203, v203\n"
    "v_mfma_f32_16x16x32_bf16 v[28:31], v[212:215], a[20:23], v[28:31]\n"
    "s_nop 0\n"
    "ds_read_b64_tr_b16 v[224:225], v253 offset:28672\n"
    "ds_read_b64_tr_b16 v[226:227], v253 offset:28928\n"
    "v_mfma_f32_16x16x32_bf16 v[60:63], v[208:211], a[72:75], v[60:63]\n"
    "s_nop 0\n"
    "ds_read_b64_tr_b16 v[228:229], v253 offset:32768\n"
    "ds_read_b64_tr_b16 v[230:231], v253 offset:33024\n"
    "v_mfma_f32_16x16x32_bf16 v[60:63], v[212:215], a[76:79], v[60:63]\n"
    "buffer_load_dwordx4 v249, s[88:91], s96 offen offset:0 lds\n"
    "v_mfma_f32_16x16x32_bf16 v[92:95], v[208:211], a[128:131], v[92:95]\n"
    "s_add_u32 m0, s69, 78848\n"
    "v_rcp_f32 v188, v188\n"
    "v_mfma_f32_16x16x32_bf16 v[92:95], v[212:215], a[132:135], v[92:95]\n"
    "s_mov_b32 s97, 1900672\n"
    "v_rcp_f32 v189, v189\n"
    "v_mfma_f32_16x16x32_bf16 v[124:127], v[208:211], a[184:187], v[124:127]\n"
    "v_rcp_f32 v190, v190\n"
    "v_mfma_f32_16x16x32_bf16 v[124:127], v[212:215], a[188:191], v[124:127]\n"
    "s_nop 0\n"
    "v_rcp_f32 v191, v191\n"
    "v_mfma_f32_16x16x32_bf16 v[20:23], v[216:219], a[24:27], v[20:23]\n"
    "buffer_load_dwordx4 v255, s[88:91], s97 offen offset:0 lds\n"
    "v_mfma_f32_16x16x32_bf16 v[28:31], v[220:223], a[24:27], v[28:31]\n"
    "s_add_u32 m0, s69, 79872\n"
    "ds_read_b64_tr_b16 v[208:209], v254 offset:28672\n"
    "ds_read_b64_tr_b16 v[210:211], v254 offset:28928\n"
    "v_mfma_f32_16x16x32_bf16 v[52:55], v[216:219], a[80:83], v[52:55]\n"
    "s_mov_b32 s96, 1966208\n"
    "ds_read_b64_tr_b16 v[212:213], v254 offset:32768\n"
    "ds_read_b64_tr_b16 v[214:215], v254 offset:33024\n"
    "v_mfma_f32_16x16x32_bf16 v[60:63], v[220:223], a[80:83], v[60:63]\n"
    "v_mul_f32 v128, v128, v192\n"
    "v_mul_f32 v129, v129, v192\n"
    "v_mfma_f32_16x16x32_bf16 v[84:87], v[216:219], a[136:139], v[84:87]\n"
    "v_mul_f32 v130, v130, v192\n"
    "v_mul_f32 v131, v131, v192\n"
    "v_mfma_f32_16x16x32_bf16 v[92:95], v[220:223], a[136:139], v[92:95]\n"
    "v_mul_f32 v144, v144, v192\n"
    "v_mul_f32 v145, v145, v192\n"
    "v_mfma_f32_16x16x32_bf16 v[116:119], v[216:219], a[192:195], v[116:119]\n"
    "buffer_load_dwordx4 v249, s[88:91], s96 offen offset:0 lds\n"
    "v_mfma_f32_16x16x32_bf16 v[124:127], v[220:223], a[192:195], v[124:127]\n"
    "s_waitcnt lgkmcnt(4)\n"
    "v_mul_f32 v146, v146, v192\n"
    "v_mul_f32 v147, v147, v192\n"
    "v_mfma_f32_16x16x32_bf16 v[20:23], v[224:227], a[28:31], v[20:23]\n"
    "s_add_u32 m0, s69, 80896\n"
    "v_cvt_pk_bf16_f32 v0, v128, v129\n"
    "v_cvt_pk_bf16_f32 v1, v130, v131\n"
    "v_mfma_f32_16x16x32_bf16 v[20:23], v[228:231], a[32:35], v[20:23]\n"
    "s_mov_b32 s97, 2031744\n"
    "ds_read_b64_tr_b16 v[216:217], v253 offset:36864\n"
    "ds_read_b64_tr_b16 v[218:219], v253 offset:37120\n"
    "v_mfma_f32_16x16x32_bf16 v[52:55], v[224:227], a[84:87], v[52:55]\n"
    "s_nop 0\n"
    "ds_read_b64_tr_b16 v[220:221], v253 offset:40960\n"
    "ds_read_b64_tr_b16 v[222:223], v253 offset:41216\n"
    "v_mfma_f32_16x16x32_bf16 v[52:55], v[228:231], a[88:91], v[52:55]\n"
    "v_cvt_pk_bf16_f32 v2, v144, v145\n"
    "v_cvt_pk_bf16_f32 v3, v146, v147\n"
    "v_mfma_f32_16x16x32_bf16 v[84:87], v[224:227], a[140:143], v[84:87]\n"
    "buffer_load_dwordx4 v255, s[88:91], s97 offen offset:0 lds\n"
    "v_mfma_f32_16x16x32_bf16 v[84:87], v[228:231], a[144:147], v[84:87]\n"
    "s_add_u32 m0, s69, 81920\n"
    "v_mul_f32 v132, v132, v193\n"
    "v_mul_f32 v133, v133, v193\n"
    "v_mfma_f32_16x16x32_bf16 v[116:119], v[224:227], a[196:199], v[116:119]\n"
    "s_mov_b32 s96, 2097280\n"
    "v_mul_f32 v134, v134, v193\n"
    "v_mul_f32 v135, v135, v193\n"
    "v_mfma_f32_16x16x32_bf16 v[116:119], v[228:231], a[200:203], v[116:119]\n"
    "s_waitcnt lgkmcnt(4)\n"
    "v_mul_f32 v148, v148, v193\n"
    "v_mul_f32 v149, v149, v193\n"
    "v_mfma_f32_16x16x32_bf16 v[28:31], v[208:211], a[28:31], v[28:31]\n"
    "v_mul_f32 v150, v150, v193\n"
    "v_mul_f32 v151, v151, v193\n"
    "v_mfma_f32_16x16x32_bf16 v[28:31], v[212:215], a[32:35], v[28:31]\n"
    "s_nop 0\n"
    "ds_read_b64_tr_b16 v[224:225], v254 offset:36864\n"
    "ds_read_b64_tr_b16 v[226:227], v254 offset:37120\n"
    "v_mfma_f32_16x16x32_bf16 v[60:63], v[208:211], a[84:87], v[60:63]\n"
    "s_nop 0\n"
    "ds_read_b64_tr_b16 v[228:229], v254 offset:40960\n"
    "ds_read_b64_tr_b16 v[230:231], v254 offset:41216\n"
    "v_mfma_f32_16x16x32_bf16 v[60:63], v[212:215], a[88:91], v[60:63]\n"
    "buffer_load_dwordx4 v249, s[88:91], s96 offen offset:0 lds\n"
    "v_mfma_f32_16x16x32_bf16 v[92:95], v[208:211], a[140:143], v[92:95]\n"
    "s_add_u32 m0, s69, 82944\n"
    "v_cvt_pk_bf16_f32 v32, v132, v133\n"
    "v_cvt_pk_bf16_f32 v33, v134, v135\n"
    "v_mfma_f32_16x16x32_bf16 v[92:95], v[212:215], a[144:147], v[92:95]\n"
    "s_mov_b32 s97, 2162816\n"
    "v_cvt_pk_bf16_f32 v34, v148, v149\n"
    "v_cvt_pk_bf16_f32 v35, v150, v151\n"
    "v_mfma_f32_16x16x32_bf16 v[124:127], v[208:211], a[196:199], v[124:127]\n"
    "v_permlane32_swap_b32 v0, v2\n"
    "v_permlane32_swap_b32 v1, v3\n"
    "v_mfma_f32_16x16x32_bf16 v[124:127], v[212:215], a[200:203], v[124:127]\n"
    "s_waitcnt lgkmcnt(4)\n"
    "v_permlane32_swap_b32 v32, v34\n"
    "v_permlane32_swap_b32 v33, v35\n"
    "v_mfma_f32_16x16x32_bf16 v[20:23], v[216:219], a[36:39], v[20:23]\n"
    "buffer_load_dwordx4 v255, s[88:91], s97 offen offset:0 lds\n"
    "v_mfma_f32_16x16x32_bf16 v[20:23], v[220:223], a[40:43], v[20:23]\n"
    "s_add_u32 m0, s69, 83968\n"
    "ds_read_b64_tr_b16 v[208:209], v253 offset:45056\n"
    "ds_read_b64_tr_b16 v[210:211], v253 offset:45312\n"
    "v_mfma_f32_16x16x32_bf16 v[52:55], v[216:219], a[92:95], v[52:55]\n"
    "s_mov_b32 s96, 2228352\n"
    "ds_read_b64_tr_b16 v[212:213], v253 offset:49152\n"
    "ds_read_b64_tr_b16 v[214:215], v253 offset:49408\n"
    "v_mfma_f32_16x16x32_bf16 v[52:55], v[220:223], a[96:99], v[52:55]\n"
    "v_permlane16_swap_b32 v0, v2\n"
    "v_permlane16_swap_b32 v1, v3\n"
    "v_mfma_f32_16x16x32_bf16 v[84:87], v[216:219], a[148:151], v[84:87]\n"
    "v_permlane16_swap_b32 v32, v34\n"
    "v_permlane16_swap_b32 v33, v35\n"
    "v_mfma_f32_16x16x32_bf16 v[84:87], v[220:223], a[152:155], v[84:87]\n"
    "ds_write_b128 v204, v[0:3] offset:32768\n"
    "v_mfma_f32_16x16x32_bf16 v[116:119], v[216:219], a[204:207], v[116:119]\n"
    "buffer_load_dwordx4 v249, s[88:91], s96 offen offset:0 lds\n"
    "v_mfma_f32_16x16x32_bf16 v[116:119], v[220:223], a[208:211], v[116:119]\n"
    "s_waitcnt lgkmcnt(5)\n"
    "ds_write_b128 v204, v[32:35] offset:33792\n"
    "v_mfma_f32_16x16x32_bf16 v[28:31], v[224:227], a[36:39], v[28:31]\n"
    "s_cmp_lt_u32 s28, 12\n"
    "v_mul_f32 v136, v136, v194\n"
    "v_mul_f32 v137, v137, v194\n"
    "v_mfma_f32_16x16x32_bf16 v[28:31], v[228:231], a[40:43], v[28:31]\n"
    "s_cselect_b32 s78, s78, 0\n"
    "ds_read_b64_tr_b16 v[216:217], v254 offset:45056\n"
    "ds_read_b64_tr_b16 v[218:219], v254 offset:45312\n"
    "v_mfma_f32_16x16x32_bf16 v[60:63], v[224:227], a[92:95], v[60:63]\n"
    "s_add_u32 m0, s69, 106496\n"
    "ds_read_b64_tr_b16 v[220:221], v254 offset:49152\n"
    "ds_read_b64_tr_b16 v[222:223], v254 offset:49408\n"
    "v_mfma_f32_16x16x32_bf16 v[60:63], v[228:231], a[96:99], v[60:63]\n"
    "s_mov_b32 s96, 2048\n"
    "v_mul_f32 v138, v138, v194\n"
    "v_mul_f32 v139, v139, v194\n"
    "v_mfma_f32_16x16x32_bf16 v[92:95], v[224:227], a[148:151], v[92:95]\n"
    "v_mul_f32 v152, v152, v194\n"
    "v_mul_f32 v153, v153, v194\n"
    "v_mfma_f32_16x16x32_bf16 v[92:95], v[228:231], a[152:155], v[92:95]\n"
    "v_mul_f32 v154, v154, v194\n"
    "v_mul_f32 v155, v155, v194\n"
    "v_mfma_f32_16x16x32_bf16 v[124:127], v[224:227], a[204:207], v[124:127]\n"
    "v_cvt_pk_bf16_f32 v64, v136, v137\n"
    "v_cvt_pk_bf16_f32 v65, v138, v139\n"
    "v_mfma_f32_16x16x32_bf16 v[124:127], v[228:231], a[208:211], v[124:127]\n"
    "s_waitcnt lgkmcnt(6)\n"
    "v_cvt_pk_bf16_f32 v66, v152, v153\n"
    "v_cvt_pk_bf16_f32 v67, v154, v155\n"
    "v_mfma_f32_16x16x32_bf16 v[20:23], v[208:211], a[44:47], v[20:23]\n"
    "v_mul_f32 v140, v140, v195\n"
    "v_mul_f32 v141, v141, v195\n"
    "v_mfma_f32_16x16x32_bf16 v[20:23], v[212:215], a[48:51], v[20:23]\n"
    "s_nop 0\n"
    "ds_read_b64_tr_b16 v[224:225], v253 offset:53248\n"
    "ds_read_b64_tr_b16 v[226:227], v253 offset:53504\n"
    "v_mfma_f32_16x16x32_bf16 v[52:55], v[208:211], a[100:103], v[52:55]\n"
    "s_nop 0\n"
    "ds_read_b64_tr_b16 v[228:229], v254 offset:53248\n"
    "ds_read_b64_tr_b16 v[230:231], v254 offset:53504\n"
    "v_mfma_f32_16x16x32_bf16 v[52:55], v[212:215], a[104:107], v[52:55]\n"
    "v_mul_f32 v142, v142, v195\n"
    "v_mul_f32 v143, v143, v195\n"
    "v_mfma_f32_16x16x32_bf16 v[84:87], v[208:211], a[156:159], v[84:87]\n"
    "v_mul_f32 v156, v156, v195\n"
    "v_mul_f32 v157, v157, v195\n"
    "v_mfma_f32_16x16x32_bf16 v[84:87], v[212:215], a[160:163], v[84:87]\n"
    "v_mul_f32 v158, v158, v195\n"
    "v_mul_f32 v159, v159, v195\n"
    "v_mfma_f32_16x16x32_bf16 v[116:119], v[208:211], a[212:215], v[116:119]\n"
    "v_cvt_pk_bf16_f32 v96, v140, v141\n"
    "v_cvt_pk_bf16_f32 v97, v142, v143\n"
    "v_mfma_f32_16x16x32_bf16 v[116:119], v[212:215], a[216:219], v[116:119]\n"
    "s_waitcnt lgkmcnt(4)\n"
    "v_cvt_pk_bf16_f32 v98, v156, v157\n"
    "v_cvt_pk_bf16_f32 v99, v158, v159\n"
    "v_mfma_f32_16x16x32_bf16 v[28:31], v[216:219], a[44:47], v[28:31]\n"
    "s_waitcnt lgkmcnt(0) vmcnt(7)\n"
    "s_barrier\n"
    "v_permlane32_swap_b32 v64, v66\n"
    "v_permlane32_swap_b32 v65, v67\n"
    "v_mfma_f32_16x16x32_bf16 v[28:31], v[220:223], a[48:51], v[28:31]\n"
    "s_nop 0\n"
    "ds_read_b64_tr_b16 v[208:209], v251 offset:0\n"
    "ds_read_b64_tr_b16 v[210:211], v251 offset:256\n"
    "v_mfma_f32_16x16x32_bf16 v[60:63], v[216:219], a[100:103], v[60:63]\n"
    "s_nop 0\n"
    "ds_read_b64_tr_b16 v[212:213], v251 offset:4096\n"
    "ds_read_b64_tr_b16 v[214:215], v251 offset:4352\n"
    "v_mfma_f32_16x16x32_bf16 v[60:63], v[220:223], a[104:107], v[60:63]\n"
    "buffer_load_dwordx4 v250, s[76:79], s96 offen offset:0 lds\n"
    "v_mfma_f32_16x16x32_bf16 v[92:95], v[216:219], a[156:159], v[92:95]\n"
    "s_add_u32 m0, s69, 107520\n"
    "v_permlane32_swap_b32 v96, v98\n"
    "v_permlane32_swap_b32 v97, v99\n"
    "v_mfma_f32_16x16x32_bf16 v[92:95], v[220:223], a[160:163], v[92:95]\n"
    "s_mov_b32 s97, 30720\n"
    "v_permlane16_swap_b32 v64, v66\n"
    "v_permlane16_swap_b32 v65, v67\n"
    "v_mfma_f32_16x16x32_bf16 v[124:127], v[216:219], a[212:215], v[124:127]\n"
    "v_permlane16_swap_b32 v96, v98\n"
    "v_permlane16_swap_b32 v97, v99\n"
    "v_mfma_f32_16x16x32_bf16 v[124:127], v[220:223], a[216:219], v[124:127]\n"
    "s_nop 0\n"
    "ds_write_b128 v204, v[64:67] offset:34816\n"
    "v_mfma_f32_16x16x32_bf16 v[20:23], v[224:227], a[52:55], v[20:23]\n"
    "buffer_load_dwordx4 v207, s[76:79], s97 offen offset:0 lds\n"
    "v_mfma_f32_16x16x32_bf16 v[28:31], v[228:231], a[52:55], v[28:31]\n"
    "s_add_u32 m0, s69, 108544\n"
    "ds_read_b64_tr_b16 v[216:217], v252 offset:0\n"
    "ds_read_b64_tr_b16 v[218:219], v252 offset:256\n"
    "v_mfma_f32_16x16x32_bf16 v[52:55], v[224:227], a[108:111], v[52:55]\n"
    "s_mov_b32 s96, 59392\n"
    "ds_read_b64_tr_b16 v[220:221], v252 offset:4096\n"
    "ds_read_b64_tr_b16 v[222:223], v252 offset:4352\n"
    "v_mfma_f32_16x16x32_bf16 v[60:63], v[228:231], a[108:111], v[60:63]\n"
    "ds_write_b128 v204, v[96:99] offset:35840\n"
    "v_mfma_f32_16x16x32_bf16 v[84:87], v[224:227], a[164:167], v[84:87]\n"
    "buffer_load_dwordx4 v250, s[76:79], s96 offen offset:0 lds\n"
    "v_mfma_f32_16x16x32_bf16 v[92:95], v[228:231], a[164:167], v[92:95]\n"
    "s_add_u32 m0, s69, 109568\n"
    "v_mfma_f32_16x16x32_bf16 v[116:119], v[224:227], a[220:223], v[116:119]\n"
    "s_mov_b32 s97, 88064\n"
    "v_mfma_f32_16x16x32_bf16 v[124:127], v[228:231], a[220:223], v[124:127]\n"
    "s_waitcnt lgkmcnt(6)\n"
    "v_mfma_f32_16x16x32_bf16 v[128:131], v[208:211], a[0:3], 0\n"
    "v_mfma_f32_16x16x32_bf16 v[128:131], v[212:215], a[4:7], v[128:131]\n"
    "s_nop 0\n"
    "ds_read_b64_tr_b16 v[224:225], v251 offset:8192\n"
    "ds_read_b64_tr_b16 v[226:227], v251 offset:8448\n"
    "v_mfma_f32_16x16x32_bf16 v[132:135], v[208:211], a[56:59], 0\n"
    "s_nop 0\n"
    "ds_read_b64_tr_b16 v[228:229], v251 offset:12288\n"
    "ds_read_b64_tr_b16 v[230:231], v251 offset:12544\n"
    "v_mfma_f32_16x16x32_bf16 v[132:135], v[212:215], a[60:63], v[132:135]\n"
    "buffer_load_dwordx4 v207, s[76:79], s97 offen offset:0 lds\n"
    "v_mfma_f32_16x16x32_bf16 v[136:139], v[208:211], a[112:115], 0\n"
    "s_add_u32 m0, s69, 110592\n"
    "v_mfma_f32_16x16x32_bf16 v[136:139], v[212:215], a[116:119], v[136:139]\n"
    "s_mov_b32 s96, 116736\n"
    "v_mfma_f32_16x16x32_bf16 v[140:143], v[208:211], a[168:171], 0\n"
    "v_mfma_f32_16x16x32_bf16 v[140:143], v[212:215], a[172:175], v[140:143]\n"
    "s_waitcnt lgkmcnt(5)\n"
    "v_mfma_f32_16x16x32_bf16 v[144:147], v[216:219], a[0:3], 0\n"
    "v_mul_f32 v160, v160, v20\n"
    "v_mul_f32 v161, v161, v21\n"
    "v_mfma_f32_16x16x32_bf16 v[144:147], v[220:223], a[4:7], v[144:147]\n"
    "s_nop 0\n"
    "ds_read_b64_tr_b16 v[208:209], v252 offset:8192\n"
    "ds_read_b64_tr_b16 v[210:211], v252 offset:8448\n"
    "v_mfma_f32_16x16x32_bf16 v[148:151], v[216:219], a[56:59], 0\n"
    "s_nop 0\n"
    "ds_read_b64_tr_b16 v[212:213], v252 offset:12288\n"
    "ds_read_b64_tr_b16 v[214:215], v252 offset:12544\n"
    "v_mfma_f32_16x16x32_bf16 v[148:151], v[220:223], a[60:63], v[148:151]\n"
    "buffer_load_dwordx4 v250, s[76:79], s96 offen offset:0 lds\n"
    "v_mfma_f32_16x16x32_bf16 v[152:155], v[216:219], a[112:115], 0\n"
    "s_add_u32 m0, s69, 111616\n"
    "v_mul_f32 v162, v162, v22\n"
    "v_mul_f32 v163, v163, v23\n"
    "v_mfma_f32_16x16x32_bf16 v[152:155], v[220:223], a[116:119], v[152:155]\n"
    "s_mov_b32 s97, 145408\n"
    "v_cvt_pk_bf16_f32 v232, v160, v161\n"
    "v_cvt_pk_bf16_f32 v233, v162, v163\n"
    "v_mfma_f32_16x16x32_bf16 v[156:159], v[216:219], a[168:171], 0\n"
    "v_mul_f32 v176, v176, v28\n"
    "v_mul_f32 v177, v177, v29\n"
    "v_mfma_f32_16x16x32_bf16 v[156:159], v[220:223], a[172:175], v[156:159]\n"
    "s_waitcnt lgkmcnt(4)\n"
    "v_mul_f32 v178, v178, v30\n"
    "v_mul_f32 v179, v179, v31\n"
    "v_mfma_f32_16x16x32_bf16 v[128:131], v[224:227], a[8:11], v[128:131]\n"
    "buffer_load_dwordx4 v207, s[76:79], s97 offen offset:0 lds\n"
    "v_mfma_f32_16x16x32_bf16 v[128:131], v[228:231], a[12:15], v[128:131]\n"
    "s_add_u32 m0, s69, 112640\n"
    "ds_read_b64_tr_b16 v[216:217], v251 offset:16384\n"
    "ds_read_b64_tr_b16 v[218:219], v251 offset:16640\n"
    "v_mfma_f32_16x16x32_bf16 v[132:135], v[224:227], a[64:67], v[132:135]\n"
    "s_mov_b32 s96, 174080\n"
    "ds_read_b64_tr_b16 v[220:221], v251 offset:20480\n"
    "ds_read_b64_tr_b16 v[222:223], v251 offset:20736\n"
    "v_mfma_f32_16x16x32_bf16 v[132:135], v[228:231], a[68:71], v[132:135]\n"
    "v_cvt_pk_bf16_f32 v234, v176, v177\n"
    "v_cvt_pk_bf16_f32 v235, v178, v179\n"
    "v_mfma_f32_16x16x32_bf16 v[136:139], v[224:227], a[120:123], v[136:139]\n"
    "buffer_store_dwordx4 v[232:235], v206, s[84:87], s92 offen offset:0 sc1\n"
    "v_mfma_f32_16x16x32_bf16 v[136:139], v[228:231], a[124:127], v[136:139]\n"
    "v_mul_f32 v164, v164, v52\n"
    "v_mul_f32 v165, v165, v53\n"
    "v_mfma_f32_16x16x32_bf16 v[140:143], v[224:227], a[176:179], v[140:143]\n"
    "buffer_load_dwordx4 v250, s[76:79], s96 offen offset:0 lds\n"
    "v_mfma_f32_16x16x32_bf16 v[140:143], v[228:231], a[180:183], v[140:143]\n"
    "s_waitcnt lgkmcnt(4)\n"
    "v_mul_f32 v166, v166, v54\n"
    "v_mul_f32 v167, v167, v55\n"
    "v_mfma_f32_16x16x32_bf16 v[144:147], v[208:211], a[8:11], v[144:147]\n"
    "s_add_u32 m0, s69, 135168\n"
    "v_cvt_pk_bf16_f32 v236, v164, v165\n"
    "v_cvt_pk_bf16_f32 v237, v166, v167\n"
    "v_mfma_f32_16x16x32_bf16 v[144:147], v[212:215], a[12:15], v[144:147]\n"
    "s_mov_b32 s96, 804864\n"
    "ds_read_b64_tr_b16 v[224:225], v252 offset:16384\n"
    "ds_read_b64_tr_b16 v[226:227], v252 offset:16640\n"
    "v_mfma_f32_16x16x32_bf16 v[148:151], v[208:211], a[64:67], v[148:151]\n"
    "s_nop 0\n"
    "ds_read_b64_tr_b16 v[228:229], v252 offset:20480\n"
    "ds_read_b64_tr_b16 v[230:231], v252 offset:20736\n"
    "v_mfma_f32_16x16x32_bf16 v[148:151], v[212:215], a[68:71], v[148:151]\n"
    "v_mul_f32 v180, v180, v60\n"
    "v_mul_f32 v181, v181, v61\n"
    "v_mfma_f32_16x16x32_bf16 v[152:155], v[208:211], a[120:123], v[152:155]\n"
    "v_mul_f32 v182, v182, v62\n"
    "v_mul_f32 v183, v183, v63\n"
    "v_mfma_f32_16x16x32_bf16 v[152:155], v[212:215], a[124:127], v[152:155]\n"
    "v_cvt_pk_bf16_f32 v238, v180, v181\n"
    "v_cvt_pk_bf16_f32 v239, v182, v183\n"
    "v_mfma_f32_16x16x32_bf16 v[156:159], v[208:211], a[176:179], v[156:159]\n"
    "buffer_store_dwordx4 v[236:239], v206, s[84:87], s93 offen offset:0 sc1\n"
    "v_mfma_f32_16x16x32_bf16 v[156:159], v[212:215], a[180:183], v[156:159]\n"
    "s_waitcnt lgkmcnt(4)\n"
    "v_mul_f32 v168, v168, v84\n"
    "v_mul_f32 v169, v169, v85\n"
    "v_mfma_f32_16x16x32_bf16 v[128:131], v[216:219], a[16:19], v[128:131]\n"
    "v_mul_f32 v170, v170, v86\n"
    "v_mul_f32 v171, v171, v87\n"
    "v_mfma_f32_16x16x32_bf16 v[128:131], v[220:223], a[20:23], v[128:131]\n"
    "s_nop 0\n"
    "ds_read_b64_tr_b16 v[208:209], v251 offset:24576\n"
    "ds_read_b64_tr_b16 v[210:211], v251 offset:24832\n"
    "v_mfma_f32_16x16x32_bf16 v[132:135], v[216:219], a[72:75], v[132:135]\n"
    "s_nop 0\n"
    "ds_read_b64_tr_b16 v[212:213], v252 offset:24576\n"
    "ds_read_b64_tr_b16 v[214:215], v252 offset:24832\n"
    "v_mfma_f32_16x16x32_bf16 v[132:135], v[220:223], a[76:79], v[132:135]\n"
    "v_cvt_pk_bf16_f32 v232, v168, v169\n"
    "v_cvt_pk_bf16_f32 v233, v170, v171\n"
    "v_mfma_f32_16x16x32_bf16 v[136:139], v[216:219], a[128:131], v[136:139]\n"
    "v_mul_f32 v184, v184, v92\n"
    "v_mul_f32 v185, v185, v93\n"
    "v_mfma_f32_16x16x32_bf16 v[136:139], v[220:223], a[132:135], v[136:139]\n"
    "v_mul_f32 v186, v186, v94\n"
    "v_mul_f32 v187, v187, v95\n"
    "v_mfma_f32_16x16x32_bf16 v[140:143], v[216:219], a[184:187], v[140:143]\n"
    "v_cvt_pk_bf16_f32 v234, v184, v185\n"
    "v_cvt_pk_bf16_f32 v235, v186, v187\n"
    "v_mfma_f32_16x16x32_bf16 v[140:143], v[220:223], a[188:191], v[140:143]\n"
    "s_waitcnt lgkmcnt(4)\n"
    "buffer_store_dwordx4 v[232:235], v206, s[84:87], s94 offen offset:0 sc1\n"
    "v_mfma_f32_16x16x32_bf16 v[144:147], v[224:227], a[16:19], v[144:147]\n"
    "s_waitcnt lgkmcnt(0) vmcnt(10)\n"
    "s_barrier\n"
    "v_mul_f32 v172, v172, v116\n"
    "v_mul_f32 v173, v173, v117\n"
    "v_mfma_f32_16x16x32_bf16 v[144:147], v[228:231], a[20:23], v[144:147]\n"
    "s_nop 0\n"
    "ds_read_b64_tr_b16 v[216:217], v253 offset:0\n"
    "ds_read_b64_tr_b16 v[218:219], v253 offset:256\n"
    "v_mfma_f32_16x16x32_bf16 v[148:151], v[224:227], a[72:75], v[148:151]\n"
    "s_nop 0\n"
    "ds_read_b64_tr_b16 v[220:221], v253 offset:4096\n"
    "ds_read_b64_tr_b16 v[222:223], v253 offset:4352\n"
    "v_mfma_f32_16x16x32_bf16 v[148:151], v[228:231], a[76:79], v[148:151]\n"
    "buffer_load_dwordx4 v250, s[76:79], s96 offen offset:0 lds\n"
    "v_mfma_f32_16x16x32_bf16 v[152:155], v[224:227], a[128:131], v[152:155]\n"
    "s_add_u32 m0, s69, 136192\n"
    "v_mul_f32 v174, v174, v118\n"
    "v_mul_f32 v175, v175, v119\n"
    "v_mfma_f32_16x16x32_bf16 v[152:155], v[228:231], a[132:135], v[152:155]\n"
    "s_mov_b32 s97, 833536\n"
    "v_cvt_pk_bf16_f32 v236, v172, v173\n"
    "v_cvt_pk_bf16_f32 v237, v174, v175\n"
    "v_mfma_f32_16x16x32_bf16 v[156:159], v[224:227], a[184:187], v[156:159]\n"
    "ds_read_b128 v[4:7], v205 offset:34816\n"
    "v_mul_f32 v188, v188, v124\n"
    "v_mfma_f32_16x16x32_bf16 v[156:159], v[228:231], a[188:191], v[156:159]\n"
    "s_nop 0\n"
    "v_mul_f32 v189, v189, v125\n"
    "v_mul_f32 v190, v190, v126\n"
    "v_mfma_f32_16x16x32_bf16 v[128:131], v[208:211], a[24:27], v[128:131]\n"
    "ds_read_b128 v[36:39], v205 offset:35840\n"
    "v_mul_f32 v191, v191, v127\n"
    "v_mfma_f32_16x16x32_bf16 v[144:147], v[212:215], a[24:27], v[144:147]\n"
    "s_nop 0\n"
    "ds_read_b64_tr_b16 v[224:225], v254 offset:0\n"
    "ds_read_b64_tr_b16 v[226:227], v254 offset:256\n"
    "v_mfma_f32_16x16x32_bf16 v[132:135], v[208:211], a[80:83], v[132:135]\n"
    "s_nop 0\n"
    "ds_read_b64_tr_b16 v[228:229], v254 offset:4096\n"
    "ds_read_b64_tr_b16 v[230:231], v254 offset:4352\n"
    "v_mfma_f32_16x16x32_bf16 v[148:151], v[212:215], a[80:83], v[148:151]\n"
    "ds_read_b128 v[68:71], v205 offset:32768\n"
    "v_cvt_pk_bf16_f32 v238, v188, v189\n"
    "v_mfma_f32_16x16x32_bf16 v[136:139], v[208:211], a[136:139], v[136:139]\n"
    "ds_read_b128 v[100:103], v205 offset:33792\n"
    "v_cvt_pk_bf16_f32 v239, v190, v191\n"
    "v_mfma_f32_16x16x32_bf16 v[152:155], v[212:215], a[136:139], v[152:155]\n"
    "buffer_load_dwordx4 v207, s[76:79], s97 offen offset:0 lds\n"
    "v_mfma_f32_16x16x32_bf16 v[140:143], v[208:211], a[192:195], v[140:143]\n"
    "s_add_u32 m0, s69, 137216\n"
    "buffer_store_dwordx4 v[236:239], v206, s[84:87], s95 offen offset:0 sc1\n"
    "v_mfma_f32_16x16x32_bf16 v[156:159], v[212:215], a[192:195], v[156:159]\n"
    "s_waitcnt lgkmcnt(8)\n"
    "v_mfma_f32_16x16x32_bf16 v[128:131], v[216:219], a[28:31], v[128:131]\n"
    "s_mov_b32 s96, 862208\n"
    "v_mfma_f32_16x16x32_bf16 v[128:131], v[220:223], a[32:35], v[128:131]\n"
    "s_nop 0\n"
    "ds_read_b64_tr_b16 v[208:209], v253 offset:8192\n"
    "ds_read_b64_tr_b16 v[210:211], v253 offset:8448\n"
    "v_mfma_f32_16x16x32_bf16 v[132:135], v[216:219], a[84:87], v[132:135]\n"
    "s_nop 0\n"
    "ds_read_b64_tr_b16 v[212:213], v253 offset:12288\n"
    "ds_read_b64_tr_b16 v[214:215], v253 offset:12544\n"
    "v_mfma_f32_16x16x32_bf16 v[132:135], v[220:223], a[88:91], v[132:135]\n"
    "buffer_load_dwordx4 v250, s[76:79], s96 offen offset:0 lds\n"
    "v_mfma_f32_16x16x32_bf16 v[136:139], v[216:219], a[140:143], v[136:139]\n"
    "s_add_u32 m0, s69, 138240\n"
    "v_mfma_f32_16x16x32_bf16 v[136:139], v[220:223], a[144:147], v[136:139]\n"
    "s_mov_b32 s97, 890880\n"
    "v_mfma_f32_16x16x32_bf16 v[140:143], v[216:219], a[196:199], v[140:143]\n"
    "buffer_load_dwordx4 v207, s[76:79], s97 offen offset:0 lds\n"
    "v_mfma_f32_16x16x32_bf16 v[140:143], v[220:223], a[200:203], v[140:143]\n"
    "s_waitcnt lgkmcnt(6)\n"
    "v_mfma_f32_16x16x32_bf16 v[144:147], v[224:227], a[28:31], v[144:147]\n"
    "s_add_u32 m0, s69, 139264\n"
    "v_mfma_f32_16x16x32_bf16 v[144:147], v[228:231], a[32:35], v[144:147]\n"
    "s_mov_b32 s96, 919552\n"
    "ds_read_b64_tr_b16 v[216:217], v254 offset:8192\n"
    "ds_read_b64_tr_b16 v[218:219], v254 offset:8448\n"
    "v_mfma_f32_16x16x32_bf16 v[148:151], v[224:227], a[84:87], v[148:151]\n"
    "s_nop 0\n"
    "ds_read_b64_tr_b16 v[220:221], v254 offset:12288\n"
    "ds_read_b64_tr_b16 v[222:223], v254 offset:12544\n"
    "v_mfma_f32_16x16x32_bf16 v[148:151], v[228:231], a[88:91], v[148:151]\n"
    "buffer_load_dwordx4 v250, s[76:79], s96 offen offset:0 lds\n"
    "v_mfma_f32_16x16x32_bf16 v[152:155], v[224:227], a[140:143], v[152:155]\n"
    "s_add_u32 m0, s69, 140288\n"
    "v_mfma_f32_16x16x32_bf16 v[152:155], v[228:231], a[144:147], v[152:155]\n"
    "s_mov_b32 s97, 948224\n"
    "v_mfma_f32_16x16x32_bf16 v[156:159], v[224:227], a[196:199], v[156:159]\n"
    "buffer_load_dwordx4 v207, s[76:79], s97 offen offset:0 lds\n"
    "v_mfma_f32_16x16x32_bf16 v[156:159], v[228:231], a[200:203], v[156:159]\n"
    "s_waitcnt lgkmcnt(4)\n"
    "v_mfma_f32_16x16x32_bf16 v[128:131], v[208:211], a[36:39], v[128:131]\n"
    "s_add_u32 m0, s69, 141312\n"
    "v_mfma_f32_16x16x32_bf16 v[128:131], v[212:215], a[40:43], v[128:131]\n"
    "s_mov_b32 s96, 976896\n"
    "ds_read_b64_tr_b16 v[224:225], v253 offset:16384\n"
    "ds_read_b64_tr_b16 v[226:227], v253 offset:16640\n"
    "v_mfma_f32_16x16x32_bf16 v[132:135], v[208:211], a[92:95], v[132:135]\n"
    "s_nop 0\n"
    "ds_read_b64_tr_b16 v[228:229], v253 offset:20480\n"
    "ds_read_b64_tr_b16 v[230:231], v253 offset:20736\n"
    "v_mfma_f32_16x16x32_bf16 v[132:135], v[212:215], a[96:99], v[132:135]\n"
    "v_mfma_f32_16x16x32_bf16 v[136:139], v[208:211], a[148:151], v[136:139]\n"
    "buffer_load_dwordx4 v250, s[76:79], s96 offen offset:0 lds\n"
    "v_mfma_f32_16x16x32_bf16 v[136:139], v[212:215], a[152:155], v[136:139]\n"
    "s_add_u32 m0, s69, 77824\n"
    "v_mfma_f32_16x16x32_bf16 v[140:143], v[208:211], a[204:207], v[140:143]\n"
    "s_mov_b32 s96, 256\n"
    "v_mfma_f32_16x16x32_bf16 v[140:143], v[212:215], a[208:211], v[140:143]\n"
    "s_waitcnt lgkmcnt(4)\n"
    "v_mfma_f32_16x16x32_bf16 v[144:147], v[216:219], a[36:39], v[144:147]\n"
    "v_mfma_f32_16x16x32_bf16 v[144:147], v[220:223], a[40:43], v[144:147]\n"
    "s_nop 0\n"
    "ds_read_b64_tr_b16 v[208:209], v254 offset:16384\n"
    "ds_read_b64_tr_b16 v[210:211], v254 offset:16640\n"
    "v_mfma_f32_16x16x32_bf16 v[148:151], v[216:219], a[92:95], v[148:151]\n"
    "s_nop 0\n"
    "ds_read_b64_tr_b16 v[212:213], v254 offset:20480\n"
    "ds_read_b64_tr_b16 v[214:215], v254 offset:20736\n"
    "v_mfma_f32_16x16x32_bf16 v[148:151], v[220:223], a[96:99], v[148:151]\n"
    "v_mfma_f32_16x16x32_bf16 v[152:155], v[216:219], a[148:151], v[152:155]\n"
    "v_mfma_f32_16x16x32_bf16 v[152:155], v[220:223], a[152:155], v[152:155]\n"
    "v_mfma_f32_16x16x32_bf16 v[156:159], v[216:219], a[204:207], v[156:159]\n"
    "v_mfma_f32_16x16x32_bf16 v[156:159], v[220:223], a[208:211], v[156:159]\n"
    "s_waitcnt lgkmcnt(4)\n"
    "v_mfma_f32_16x16x32_bf16 v[128:131], v[224:227], a[44:47], v[128:131]\n"
    "v_mfma_f32_16x16x32_bf16 v[128:131], v[228:231], a[48:51], v[128:131]\n"
    "s_nop 0\n"
    "ds_read_b64_tr_b16 v[216:217], v253 offset:24576\n"
    "ds_read_b64_tr_b16 v[218:219], v253 offset:24832\n"
    "v_mfma_f32_16x16x32_bf16 v[132:135], v[224:227], a[100:103], v[132:135]\n"
    "s_nop 0\n"
    "ds_read_b64_tr_b16 v[220:221], v254 offset:24576\n"
    "ds_read_b64_tr_b16 v[222:223], v254 offset:24832\n"
    "v_mfma_f32_16x16x32_bf16 v[132:135], v[228:231], a[104:107], v[132:135]\n"
    "v_mfma_f32_16x16x32_bf16 v[136:139], v[224:227], a[156:159], v[136:139]\n"
    "v_mfma_f32_16x16x32_bf16 v[136:139], v[228:231], a[160:163], v[136:139]\n"
    "v_mfma_f32_16x16x32_bf16 v[140:143], v[224:227], a[212:215], v[140:143]\n"
    "v_mfma_f32_16x16x32_bf16 v[140:143], v[228:231], a[216:219], v[140:143]\n"
    "s_waitcnt lgkmcnt(4)\n"
    "v_mfma_f32_16x16x32_bf16 v[144:147], v[208:211], a[44:47], v[144:147]\n"
    "s_waitcnt lgkmcnt(0) vmcnt(10)\n"
    "s_barrier\n"
    "v_mfma_f32_16x16x32_bf16 v[144:147], v[212:215], a[48:51], v[144:147]\n"
    "s_nop 0\n"
    "ds_read_b64_tr_b16 v[224:225], v253 offset:28672\n"
    "ds_read_b64_tr_b16 v[226:227], v253 offset:28928\n"
    "v_mfma_f32_16x16x32_bf16 v[148:151], v[208:211], a[100:103], v[148:151]\n"
    "s_nop 0\n"
    "ds_read_b64_tr_b16 v[228:229], v253 offset:32768\n"
    "ds_read_b64_tr_b16 v[230:231], v253 offset:33024\n"
    "v_mfma_f32_16x16x32_bf16 v[148:151], v[212:215], a[104:107], v[148:151]\n"
    "buffer_load_dwordx4 v249, s[88:91], s96 offen offset:0 lds\n"
    "v_mfma_f32_16x16x32_bf16 v[152:155], v[208:211], a[156:159], v[152:155]\n"
    "s_add_u32 m0, s69, 78848\n"
    "v_mfma_f32_16x16x32_bf16 v[152:155], v[212:215], a[160:163], v[152:155]\n"
    "s_mov_b32 s97, 65792\n"
    "v_mfma_f32_16x16x32_bf16 v[156:159], v[208:211], a[212:215], v[156:159]\n"
    "buffer_load_dwordx4 v255, s[88:91], s97 offen offset:0 lds\n"
    "v_mfma_f32_16x16x32_bf16 v[156:159], v[212:215], a[216:219], v[156:159]\n"
    "s_nop 0\n"
    "v_mfma_f32_16x16x32_bf16 v[128:131], v[216:219], a[52:55], v[128:131]\n"
    "s_add_u32 m0, s69, 79872\n"
    "v_mfma_f32_16x16x32_bf16 v[144:147], v[220:223], a[52:55], v[144:147]\n"
    "s_mov_b32 s96, 131328\n"
    "ds_read_b64_tr_b16 v[208:209], v254 offset:28672\n"
    "ds_read_b64_tr_b16 v[210:211], v254 offset:28928\n"
    "v_mfma_f32_16x16x32_bf16 v[132:135], v[216:219], a[108:111], v[132:135]\n"
    "s_nop 0\n"
    "ds_read_b64_tr_b16 v[212:213], v254 offset:32768\n"
    "ds_read_b64_tr_b16 v[214:215], v254 offset:33024\n"
    "v_mfma_f32_16x16x32_bf16 v[148:151], v[220:223], a[108:111], v[148:151]\n"
    "v_mfma_f32_16x16x32_bf16 v[136:139], v[216:219], a[164:167], v[136:139]\n"
    "buffer_load_dwordx4 v249, s[88:91], s96 offen offset:0 lds\n"
    "v_mfma_f32_16x16x32_bf16 v[152:155], v[220:223], a[164:167], v[152:155]\n"
    "s_add_u32 m0, s69, 80896\n"
    "v_mfma_f32_16x16x32_bf16 v[140:143], v[216:219], a[220:223], v[140:143]\n"
    "s_mov_b32 s97, 196864\n"
    "v_mfma_f32_16x16x32_bf16 v[156:159], v[220:223], a[220:223], v[156:159]\n"
    "s_waitcnt lgkmcnt(4)\n"
    "v_mfma_f32_16x16x32_bf16 v[160:163], v[224:227], a[0:3], 0\n"
    "v_mfma_f32_16x16x32_bf16 v[160:163], v[228:231], a[4:7], v[160:163]\n"
    "s_nop 0\n"
    "ds_read_b64_tr_b16 v[216:217], v253 offset:36864\n"
    "ds_read_b64_tr_b16 v[218:219], v253 offset:37120\n"
    "v_mfma_f32_16x16x32_bf16 v[164:167], v[224:227], a[56:59], 0\n"
    "s_nop 0\n"
    "ds_read_b64_tr_b16 v[220:221], v253 offset:40960\n"
    "ds_read_b64_tr_b16 v[222:223], v253 offset:41216\n"
    "v_mfma_f32_16x16x32_bf16 v[164:167], v[228:231], a[60:63], v[164:167]\n"
    "buffer_load_dwordx4 v255, s[88:91], s97 offen offset:0 lds\n"
    "v_mfma_f32_16x16x32_bf16 v[168:171], v[224:227], a[112:115], 0\n"
    "s_add_u32 m0, s69, 81920\n"
    "v_mfma_f32_16x16x32_bf16 v[168:171], v[228:231], a[116:119], v[168:171]\n"
    "s_mov_b32 s96, 262400\n"
    "v_mfma_f32_16x16x32_bf16 v[172:175], v[224:227], a[168:171], 0\n"
    "v_mfma_f32_16x16x32_bf16 v[172:175], v[228:231], a[172:175], v[172:175]\n"
    "s_waitcnt lgkmcnt(4)\n"
    "v_mfma_f32_16x16x32_bf16 v[176:179], v[208:211], a[0:3], 0\n"
    "v_mul_f32 v128, v128, v192\n"
    "v_mul_f32 v129, v129, v192\n"
    "v_mfma_f32_16x16x32_bf16 v[176:179], v[212:215], a[4:7], v[176:179]\n"
    "s_nop 0\n"
    "ds_read_b64_tr_b16 v[224:225], v254 offset:36864\n"
    "ds_read_b64_tr_b16 v[226:227], v254 offset:37120\n"
    "v_mfma_f32_16x16x32_bf16 v[180:183], v[208:211], a[56:59], 0\n"
    "s_nop 0\n"
    "ds_read_b64_tr_b16 v[228:229], v254 offset:40960\n"
    "ds_read_b64_tr_b16 v[230:231], v254 offset:41216\n"
    "v_mfma_f32_16x16x32_bf16 v[180:183], v[212:215], a[60:63], v[180:183]\n"
    "buffer_load_dwordx4 v249, s[88:91], s96 offen offset:0 lds\n"
    "v_mfma_f32_16x16x32_bf16 v[184:187], v[208:211], a[112:115], 0\n"
    "s_add_u32 m0, s69, 82944\n"
    "v_mul_f32 v130, v130, v192\n"
    "v_mul_f32 v131, v131, v192\n"
    "v_mfma_f32_16x16x32_bf16 v[184:187], v[212:215], a[116:119], v[184:187]\n"
    "s_mov_b32 s97, 327936\n"
    "v_mul_f32 v144, v144, v192\n"
    "v_mul_f32 v145, v145, v192\n"
    "v_mfma_f32_16x16x32_bf16 v[188:191], v[208:211], a[168:171], 0\n"
    "v_mul_f32 v146, v146, v192\n"
    "v_mul_f32 v147, v147, v192\n"
    "v_mfma_f32_16x16x32_bf16 v[188:191], v[212:215], a[172:175], v[188:191]\n"
    "s_waitcnt lgkmcnt(4)\n"
    "v_cvt_pk_bf16_f32 v8, v128, v129\n"
    "v_cvt_pk_bf16_f32 v9, v130, v131\n"
    "v_mfma_f32_16x16x32_bf16 v[160:163], v[216:219], a[8:11], v[160:163]\n"
    "buffer_load_dwordx4 v255, s[88:91], s97 offen offset:0 lds\n"
    "v_mfma_f32_16x16x32_bf16 v[160:163], v[220:223], a[12:15], v[160:163]\n"
    "s_add_u32 m0, s69, 83968\n"
    "ds_read_b64_tr_b16 v[208:209], v253 offset:45056\n"
    "ds_read_b64_tr_b16 v[210:211], v253 offset:45312\n"
    "v_mfma_f32_16x16x32_bf16 v[164:167], v[216:219], a[64:67], v[164:167]\n"
    "s_mov_b32 s96, 393472\n"
    "ds_read_b64_tr_b16 v[212:213], v253 offset:49152\n"
    "ds_read_b64_tr_b16 v[214:215], v253 offset:49408\n"
    "v_mfma_f32_16x16x32_bf16 v[164:167], v[220:223], a[68:71], v[164:167]\n"
    "v_cvt_pk_bf16_f32 v10, v144, v145\n"
    "v_cvt_pk_bf16_f32 v11, v146, v147\n"
    "v_mfma_f32_16x16x32_bf16 v[168:171], v[216:219], a[120:123], v[168:171]\n"
    "v_mul_f32 v132, v132, v193\n"
    "v_mul_f32 v133, v133, v193\n"
    "v_mfma_f32_16x16x32_bf16 v[168:171], v[220:223], a[124:127], v[168:171]\n"
    "v_mul_f32 v134, v134, v193\n"
    "v_mul_f32 v135, v135, v193\n"
    "v_mfma_f32_16x16x32_bf16 v[172:175], v[216:219], a[176:179], v[172:175]\n"
    "buffer_load_dwordx4 v249, s[88:91], s96 offen offset:0 lds\n"
    "v_mfma_f32_16x16x32_bf16 v[172:175], v[220:223], a[180:183], v[172:175]\n"
    "s_waitcnt lgkmcnt(4)\n"
    "v_mul_f32 v148, v148, v193\n"
    "v_mul_f32 v149, v149, v193\n"
    "v_mfma_f32_16x16x32_bf16 v[176:179], v[224:227], a[8:11], v[176:179]\n"
    "s_add_u32 m0, s69, 106496\n"
    "v_mul_f32 v150, v150, v193\n"
    "v_mul_f32 v151, v151, v193\n"
    "v_mfma_f32_16x16x32_bf16 v[176:179], v[228:231], a[12:15], v[176:179]\n"
    "s_mov_b32 s96, 1835264\n"
    "ds_read_b64_tr_b16 v[216:217], v254 offset:45056\n"
    "ds_read_b64_tr_b16 v[218:219], v254 offset:45312\n"
    "v_mfma_f32_16x16x32_bf16 v[180:183], v[224:227], a[64:67], v[180:183]\n"
    "s_nop 0\n"
    "ds_read_b64_tr_b16 v[220:221], v254 offset:49152\n"
    "ds_read_b64_tr_b16 v[222:223], v254 offset:49408\n"
    "v_mfma_f32_16x16x32_bf16 v[180:183], v[228:231], a[68:71], v[180:183]\n"
    "v_cvt_pk_bf16_f32 v40, v132, v133\n"
    "v_cvt_pk_bf16_f32 v41, v134, v135\n"
    "v_mfma_f32_16x16x32_bf16 v[184:187], v[224:227], a[120:123], v[184:187]\n"
    "v_cvt_pk_bf16_f32 v42, v148, v149\n"
    "v_cvt_pk_bf16_f32 v43, v150, v151\n"
    "v_mfma_f32_16x16x32_bf16 v[184:187], v[228:231], a[124:127], v[184:187]\n"
    "v_permlane32_swap_b32 v8, v10\n"
    "v_permlane32_swap_b32 v9, v11\n"
    "v_mfma_f32_16x16x32_bf16 v[188:191], v[224:227], a[176:179], v[188:191]\n"
    "v_permlane32_swap_b32 v40, v42\n"
    "v_permlane32_swap_b32 v41, v43\n"
    "v_mfma_f32_16x16x32_bf16 v[188:191], v[228:231], a[180:183], v[188:191]\n"
    "s_waitcnt lgkmcnt(4)\n"
    "v_permlane16_swap_b32 v8, v10\n"
    "v_permlane16_swap_b32 v9, v11\n"
    "v_mfma_f32_16x16x32_bf16 v[160:163], v[208:211], a[16:19], v[160:163]\n"
    "v_permlane16_swap_b32 v40, v42\n"
    "v_permlane16_swap_b32 v41, v43\n"
    "v_mfma_f32_16x16x32_bf16 v[160:163], v[212:215], a[20:23], v[160:163]\n"
    "s_nop 0\n"
    "ds_read_b64_tr_b16 v[224:225], v253 offset:53248\n"
    "ds_read_b64_tr_b16 v[226:227], v253 offset:53504\n"
    "v_mfma_f32_16x16x32_bf16 v[164:167], v[208:211], a[72:75], v[164:167]\n"
    "s_nop 0\n"
    "ds_read_b64_tr_b16 v[228:229], v254 offset:53248\n"
    "ds_read_b64_tr_b16 v[230:231], v254 offset:53504\n"
    "v_mfma_f32_16x16x32_bf16 v[164:167], v[212:215], a[76:79], v[164:167]\n"
    "ds_write_b128 v204, v[8:11] offset:49152\n"
    "v_mfma_f32_16x16x32_bf16 v[168:171], v[208:211], a[128:131], v[168:171]\n"
    "ds_write_b128 v204, v[40:43] offset:50176\n"
    "v_mfma_f32_16x16x32_bf16 v[168:171], v[212:215], a[132:135], v[168:171]\n"
    "v_mul_f32 v136, v136, v194\n"
    "v_mul_f32 v137, v137, v194\n"
    "v_mfma_f32_16x16x32_bf16 v[172:175], v[208:211], a[184:187], v[172:175]\n"
    "v_mul_f32 v138, v138, v194\n"
    "v_mul_f32 v139, v139, v194\n"
    "v_mfma_f32_16x16x32_bf16 v[172:175], v[212:215], a[188:191], v[172:175]\n"
    "s_waitcnt lgkmcnt(6)\n"
    "v_mul_f32 v152, v152, v194\n"
    "v_mul_f32 v153, v153, v194\n"
    "v_mfma_f32_16x16x32_bf16 v[176:179], v[216:219], a[16:19], v[176:179]\n"
    "s_waitcnt lgkmcnt(2) vmcnt(7)\n"
    "s_barrier\n"
    "v_mul_f32 v154, v154, v194\n"
    "v_mul_f32 v155, v155, v194\n"
    "v_mfma_f32_16x16x32_bf16 v[176:179], v[220:223], a[20:23], v[176:179]\n"
    "s_nop 0\n"
    "ds_read_b64_tr_b16 v[208:209], v251 offset:0\n"
    "ds_read_b64_tr_b16 v[210:211], v251 offset:256\n"
    "v_mfma_f32_16x16x32_bf16 v[180:183], v[216:219], a[72:75], v[180:183]\n"
    "s_nop 0\n"
    "ds_read_b64_tr_b16 v[212:213], v251 offset:4096\n"
    "ds_read_b64_tr_b16 v[214:215], v251 offset:4352\n"
    "v_mfma_f32_16x16x32_bf16 v[180:183], v[220:223], a[76:79], v[180:183]\n"
    "buffer_load_dwordx4 v249, s[88:91], s96 offen offset:0 lds\n"
    "v_mfma_f32_16x16x32_bf16 v[184:187], v[216:219], a[128:131], v[184:187]\n"
    "s_add_u32 m0, s69, 107520\n"
    "v_cvt_pk_bf16_f32 v72, v136, v137\n"
    "v_cvt_pk_bf16_f32 v73, v138, v139\n"
    "v_mfma_f32_16x16x32_bf16 v[184:187], v[220:223], a[132:135], v[184:187]\n"
    "s_mov_b32 s97, 1900800\n"
    "v_cvt_pk_bf16_f32 v74, v152, v153\n"
    "v_cvt_pk_bf16_f32 v75, v154, v155\n"
    "v_mfma_f32_16x16x32_bf16 v[188:191], v[216:219], a[184:187], v[188:191]\n"
    "v_mul_f32 v140, v140, v195\n"
    "v_mul_f32 v141, v141, v195\n"
    "v_mfma_f32_16x16x32_bf16 v[188:191], v[220:223], a[188:191], v[188:191]\n"
    "s_nop 0\n"
    "v_mul_f32 v142, v142, v195\n"
    "v_mul_f32 v143, v143, v195\n"
    "v_mfma_f32_16x16x32_bf16 v[160:163], v[224:227], a[24:27], v[160:163]\n"
    "buffer_load_dwordx4 v255, s[88:91], s97 offen offset:0 lds\n"
    "v_mfma_f32_16x16x32_bf16 v[176:179], v[228:231], a[24:27], v[176:179]\n"
    "s_add_u32 m0, s69, 108544\n"
    "ds_read_b64_tr_b16 v[216:217], v252 offset:0\n"
    "ds_read_b64_tr_b16 v[218:219], v252 offset:256\n"
    "v_mfma_f32_16x16x32_bf16 v[164:167], v[224:227], a[80:83], v[164:167]\n"
    "s_mov_b32 s96, 1966336\n"
    "ds_read_b64_tr_b16 v[220:221], v252 offset:4096\n"
    "ds_read_b64_tr_b16 v[222:223], v252 offset:4352\n"
    "v_mfma_f32_16x16x32_bf16 v[180:183], v[228:231], a[80:83], v[180:183]\n"
    "v_mul_f32 v156, v156, v195\n"
    "v_mul_f32 v157, v157, v195\n"
    "v_mfma_f32_16x16x32_bf16 v[168:171], v[224:227], a[136:139], v[168:171]\n"
    "v_mul_f32 v158, v158, v195\n"
    "v_mul_f32 v159, v159, v195\n"
    "v_mfma_f32_16x16x32_bf16 v[184:187], v[228:231], a[136:139], v[184:187]\n"
    "v_cvt_pk_bf16_f32 v104, v140, v141\n"
    "v_cvt_pk_bf16_f32 v105, v142, v143\n"
    "v_mfma_f32_16x16x32_bf16 v[172:175], v[224:227], a[192:195], v[172:175]\n"
    "buffer_load_dwordx4 v249, s[88:91], s96 offen offset:0 lds\n"
    "v_mfma_f32_16x16x32_bf16 v[188:191], v[228:231], a[192:195], v[188:191]\n"
    "s_waitcnt lgkmcnt(4)\n"
    "v_cvt_pk_bf16_f32 v106, v156, v157\n"
    "v_cvt_pk_bf16_f32 v107, v158, v159\n"
    "v_mfma_f32_16x16x32_bf16 v[160:163], v[208:211], a[28:31], v[160:163]\n"
    "s_add_u32 m0, s69, 109568\n"
    "v_permlane32_swap_b32 v72, v74\n"
    "v_permlane32_swap_b32 v73, v75\n"
    "v_mfma_f32_16x16x32_bf16 v[160:163], v[212:215], a[32:35], v[160:163]\n"
    "s_mov_b32 s97, 2031872\n"
    "ds_read_b64_tr_b16 v[224:225], v251 offset:8192\n"
    "ds_read_b64_tr_b16 v[226:227], v251 offset:8448\n"
    "v_mfma_f32_16x16x32_bf16 v[164:167], v[208:211], a[84:87], v[164:167]\n"
    "s_nop 0\n"
    "ds_read_b64_tr_b16 v[228:229], v251 offset:12288\n"
    "ds_read_b64_tr_b16 v[230:231], v251 offset:12544\n"
    "v_mfma_f32_16x16x32_bf16 v[164:167], v[212:215], a[88:91], v[164:167]\n"
    "v_permlane32_swap_b32 v104, v106\n"
    "v_permlane32_swap_b32 v105, v107\n"
    "v_mfma_f32_16x16x32_bf16 v[168:171], v[208:211], a[140:143], v[168:171]\n"
    "buffer_load_dwordx4 v255, s[88:91], s97 offen offset:0 lds\n"
    "v_mfma_f32_16x16x32_bf16 v[168:171], v[212:215], a[144:147], v[168:171]\n"
    "s_add_u32 m0, s69, 110592\n"
    "v_permlane16_swap_b32 v72, v74\n"
    "v_permlane16_swap_b32 v73, v75\n"
    "v_mfma_f32_16x16x32_bf16 v[172:175], v[208:211], a[196:199], v[172:175]\n"
    "s_mov_b32 s96, 2097408\n"
    "v_permlane16_swap_b32 v104, v106\n"
    "v_permlane16_swap_b32 v105, v107\n"
    "v_mfma_f32_16x16x32_bf16 v[172:175], v[212:215], a[200:203], v[172:175]\n"
    "s_waitcnt lgkmcnt(4)\n"
    "ds_write_b128 v204, v[72:75] offset:51200\n"
    "v_mfma_f32_16x16x32_bf16 v[176:179], v[216:219], a[28:31], v[176:179]\n"
    "ds_write_b128 v204, v[104:107] offset:52224\n"
    "v_mfma_f32_16x16x32_bf16 v[176:179], v[220:223], a[32:35], v[176:179]\n"
    "s_nop 0\n"
    "ds_read_b64_tr_b16 v[208:209], v252 offset:8192\n"
    "ds_read_b64_tr_b16 v[210:211], v252 offset:8448\n"
    "v_mfma_f32_16x16x32_bf16 v[180:183], v[216:219], a[84:87], v[180:183]\n"
    "s_nop 0\n"
    "ds_read_b64_tr_b16 v[212:213], v252 offset:12288\n"
    "ds_read_b64_tr_b16 v[214:215], v252 offset:12544\n"
    "v_mfma_f32_16x16x32_bf16 v[180:183], v[220:223], a[88:91], v[180:183]\n"
    "buffer_load_dwordx4 v249, s[88:91], s96 offen offset:0 lds\n"
    "v_mfma_f32_16x16x32_bf16 v[184:187], v[216:219], a[140:143], v[184:187]\n"
    "s_add_u32 m0, s69, 111616\n"
    "v_mfma_f32_16x16x32_bf16 v[184:187], v[220:223], a[144:147], v[184:187]\n"
    "s_mov_b32 s97, 2162944\n"
    "v_mfma_f32_16x16x32_bf16 v[188:191], v[216:219], a[196:199], v[188:191]\n"
    "buffer_load_dwordx4 v255, s[88:91], s97 offen offset:0 lds\n"
    "v_mfma_f32_16x16x32_bf16 v[188:191], v[220:223], a[200:203], v[188:191]\n"
    "s_waitcnt lgkmcnt(6)\n"
    "v_mfma_f32_16x16x32_bf16 v[160:163], v[224:227], a[36:39], v[160:163]\n"
    "s_add_u32 m0, s69, 112640\n"
    "v_mfma_f32_16x16x32_bf16 v[160:163], v[228:231], a[40:43], v[160:163]\n"
    "s_mov_b32 s96, 2228480\n"
    "ds_read_b64_tr_b16 v[216:217], v251 offset:16384\n"
    "ds_read_b64_tr_b16 v[218:219], v251 offset:16640\n"
    "v_mfma_f32_16x16x32_bf16 v[164:167], v[224:227], a[92:95], v[164:167]\n"
    "s_nop 0\n"
    "ds_read_b64_tr_b16 v[220:221], v251 offset:20480\n"
    "ds_read_b64_tr_b16 v[222:223], v251 offset:20736\n"
    "v_mfma_f32_16x16x32_bf16 v[164:167], v[228:231], a[96:99], v[164:167]\n"
    "v_mfma_f32_16x16x32_bf16 v[168:171], v[224:227], a[148:151], v[168:171]\n"
    "buffer_load_dwordx4 v249, s[88:91], s96 offen offset:0 lds\n"
    "v_mfma_f32_16x16x32_bf16 v[168:171], v[228:231], a[152:155], v[168:171]\n"
    "s_add_u32 m0, s69, 135168\n"
    "v_mfma_f32_16x16x32_bf16 v[172:175], v[224:227], a[204:207], v[172:175]\n"
    "s_mov_b32 s96, 384\n"
    "v_mfma_f32_16x16x32_bf16 v[172:175], v[228:231], a[208:211], v[172:175]\n"
    "s_waitcnt lgkmcnt(4)\n"
    "v_mfma_f32_16x16x32_bf16 v[176:179], v[208:211], a[36:39], v[176:179]\n"
    "v_mfma_f32_16x16x32_bf16 v[176:179], v[212:215], a[40:43], v[176:179]\n"
    "s_nop 0\n"
    "ds_read_b64_tr_b16 v[224:225], v252 offset:16384\n"
    "ds_read_b64_tr_b16 v[226:227], v252 offset:16640\n"
    "v_mfma_f32_16x16x32_bf16 v[180:183], v[208:211], a[92:95], v[180:183]\n"
    "s_nop 0\n"
    "ds_read_b64_tr_b16 v[228:229], v252 offset:20480\n"
    "ds_read_b64_tr_b16 v[230:231], v252 offset:20736\n"
    "v_mfma_f32_16x16x32_bf16 v[180:183], v[212:215], a[96:99], v[180:183]\n"
    "v_mfma_f32_16x16x32_bf16 v[184:187], v[208:211], a[148:151], v[184:187]\n"
    "v_mfma_f32_16x16x32_bf16 v[184:187], v[212:215], a[152:155], v[184:187]\n"
    "v_mfma_f32_16x16x32_bf16 v[188:191], v[208:211], a[204:207], v[188:191]\n"
    "v_mfma_f32_16x16x32_bf16 v[188:191], v[212:215], a[208:211], v[188:191]\n"
    "s_waitcnt lgkmcnt(4)\n"
    "v_mfma_f32_16x16x32_bf16 v[160:163], v[216:219], a[44:47], v[160:163]\n"
    "v_mfma_f32_16x16x32_bf16 v[160:163], v[220:223], a[48:51], v[160:163]\n"
    "s_nop 0\n"
    "ds_read_b64_tr_b16 v[208:209], v251 offset:24576\n"
    "ds_read_b64_tr_b16 v[210:211], v251 offset:24832\n"
    "v_mfma_f32_16x16x32_bf16 v[164:167], v[216:219], a[100:103], v[164:167]\n"
    "s_nop 0\n"
    "ds_read_b64_tr_b16 v[212:213], v252 offset:24576\n"
    "ds_read_b64_tr_b16 v[214:215], v252 offset:24832\n"
    "v_mfma_f32_16x16x32_bf16 v[164:167], v[220:223], a[104:107], v[164:167]\n"
    "v_mfma_f32_16x16x32_bf16 v[168:171], v[216:219], a[156:159], v[168:171]\n"
    "v_mfma_f32_16x16x32_bf16 v[168:171], v[220:223], a[160:163], v[168:171]\n"
    "v_mfma_f32_16x16x32_bf16 v[172:175], v[216:219], a[212:215], v[172:175]\n"
    "v_mfma_f32_16x16x32_bf16 v[172:175], v[220:223], a[216:219], v[172:175]\n"
    "s_waitcnt lgkmcnt(4)\n"
    "v_mfma_f32_16x16x32_bf16 v[176:179], v[224:227], a[44:47], v[176:179]\n"
    "s_waitcnt lgkmcnt(0) vmcnt(7)\n"
    "s_barrier\n"
    "v_mfma_f32_16x16x32_bf16 v[176:179], v[228:231], a[48:51], v[176:179]\n"
    "s_nop 0\n"
    "ds_read_b64_tr_b16 v[216:217], v253 offset:0\n"
    "ds_read_b64_tr_b16 v[218:219], v253 offset:256\n"
    "v_mfma_f32_16x16x32_bf16 v[180:183], v[224:227], a[100:103], v[180:183]\n"
    "s_nop 0\n"
    "ds_read_b64_tr_b16 v[220:221], v253 offset:4096\n"
    "ds_read_b64_tr_b16 v[222:223], v253 offset:4352\n"
    "v_mfma_f32_16x16x32_bf16 v[180:183], v[228:231], a[104:107], v[180:183]\n"
    "buffer_load_dwordx4 v249, s[88:91], s96 offen offset:0 lds\n"
    "v_mfma_f32_16x16x32_bf16 v[184:187], v[224:227], a[156:159], v[184:187]\n"
    "s_add_u32 m0, s69, 136192\n"
    "v_mfma_f32_16x16x32_bf16 v[184:187], v[228:231], a[160:163], v[184:187]\n"
    "s_mov_b32 s97, 65920\n"
    "v_mfma_f32_16x16x32_bf16 v[188:191], v[224:227], a[212:215], v[188:191]\n"
    "buffer_load_dwordx4 v255, s[88:91], s97 offen offset:0 lds\n"
    "v_mfma_f32_16x16x32_bf16 v[188:191], v[228:231], a[216:219], v[188:191]\n"
    "s_nop 0\n"
    "v_mfma_f32_16x16x32_bf16 v[160:163], v[208:211], a[52:55], v[160:163]\n"
    "s_add_u32 m0, s69, 137216\n"
    "v_mfma_f32_16x16x32_bf16 v[176:179], v[212:215], a[52:55], v[176:179]\n"
    "s_mov_b32 s96, 131456\n"
    "ds_read_b64_tr_b16 v[224:225], v254 offset:0\n"
    "ds_read_b64_tr_b16 v[226:227], v254 offset:256\n"
    "v_mfma_f32_16x16x32_bf16 v[164:167], v[208:211], a[108:111], v[164:167]\n"
    "s_nop 0\n"
    "ds_read_b64_tr_b16 v[228:229], v254 offset:4096\n"
    "ds_read_b64_tr_b16 v[230:231], v254 offset:4352\n"
    "v_mfma_f32_16x16x32_bf16 v[180:183], v[212:215], a[108:111], v[180:183]\n"
    "v_mfma_f32_16x16x32_bf16 v[168:171], v[208:211], a[164:167], v[168:171]\n"
    "buffer_load_dwordx4 v249, s[88:91], s96 offen offset:0 lds\n"
    "v_mfma_f32_16x16x32_bf16 v[184:187], v[212:215], a[164:167], v[184:187]\n"
    "s_add_u32 m0, s69, 138240\n"
    "v_mfma_f32_16x16x32_bf16 v[172:175], v[208:211], a[220:223], v[172:175]\n"
    "s_mov_b32 s97, 196992\n"
    "v_mfma_f32_16x16x32_bf16 v[188:191], v[212:215], a[220:223], v[188:191]\n"
    "s_waitcnt lgkmcnt(4)\n"
    "v_mfma_f32_16x16x32_bf16 v[20:23], v[216:219], a[0:3], 0\n"
    "v_mfma_f32_16x16x32_bf16 v[20:23], v[220:223], a[4:7], v[20:23]\n"
    "s_nop 0\n"
    "ds_read_b64_tr_b16 v[208:209], v253 offset:8192\n"
    "ds_read_b64_tr_b16 v[210:211], v253 offset:8448\n"
    "v_mfma_f32_16x16x32_bf16 v[52:55], v[216:219], a[56:59], 0\n"
    "s_nop 0\n"
    "ds_read_b64_tr_b16 v[212:213], v253 offset:12288\n"
    "ds_read_b64_tr_b16 v[214:215], v253 offset:12544\n"
    "v_mfma_f32_16x16x32_bf16 v[52:55], v[220:223], a[60:63], v[52:55]\n"
    "buffer_load_dwordx4 v255, s[88:91], s97 offen offset:0 lds\n"
    "v_mfma_f32_16x16x32_bf16 v[84:87], v[216:219], a[112:115], 0\n"
    "s_add_u32 m0, s69, 139264\n"
    "v_mfma_f32_16x16x32_bf16 v[84:87], v[220:223], a[116:119], v[84:87]\n"
    "s_mov_b32 s96, 262528\n"
    "v_mfma_f32_16x16x32_bf16 v[116:119], v[216:219], a[168:171], 0\n"
    "v_mfma_f32_16x16x32_bf16 v[116:119], v[220:223], a[172:175], v[116:119]\n"
    "s_waitcnt lgkmcnt(4)\n"
    "v_mfma_f32_16x16x32_bf16 v[28:31], v[224:227], a[0:3], 0\n"
    "v_mul_f32 v160, v160, v196\n"
    "v_mul_f32 v161, v161, v196\n"
    "v_mfma_f32_16x16x32_bf16 v[28:31], v[228:231], a[4:7], v[28:31]\n"
    "s_nop 0\n"
    "ds_read_b64_tr_b16 v[216:217], v254 offset:8192\n"
    "ds_read_b64_tr_b16 v[218:219], v254 offset:8448\n"
    "v_mfma_f32_16x16x32_bf16 v[60:63], v[224:227], a[56:59], 0\n"
    "s_nop 0\n"
    "ds_read_b64_tr_b16 v[220:221], v254 offset:12288\n"
    "ds_read_b64_tr_b16 v[222:223], v254 offset:12544\n"
    "v_mfma_f32_16x16x32_bf16 v[60:63], v[228:231], a[60:63], v[60:63]\n"
    "buffer_load_dwordx4 v249, s[88:91], s96 offen offset:0 lds\n"
    "v_mfma_f32_16x16x32_bf16 v[92:95], v[224:227], a[112:115], 0\n"
    "s_add_u32 m0, s69, 140288\n"
    "v_mul_f32 v162, v162, v196\n"
    "v_mul_f32 v163, v163, v196\n"
    "v_mfma_f32_16x16x32_bf16 v[92:95], v[228:231], a[116:119], v[92:95]\n"
    "s_mov_b32 s97, 328064\n"
    "v_exp_f32 v160, v160\n"
    "v_mfma_f32_16x16x32_bf16 v[124:127], v[224:227], a[168:171], 0\n"
    "v_exp_f32 v161, v161\n"
    "v_mfma_f32_16x16x32_bf16 v[124:127], v[228:231], a[172:175], v[124:127]\n"
    "s_waitcnt lgkmcnt(4)\n"
    "v_mul_f32 v176, v176, v196\n"
    "v_mul_f32 v177, v177, v196\n"
    "v_mfma_f32_16x16x32_bf16 v[20:23], v[208:211], a[8:11], v[20:23]\n"
    "buffer_load_dwordx4 v255, s[88:91], s97 offen offset:0 lds\n"
    "v_mfma_f32_16x16x32_bf16 v[20:23], v[212:215], a[12:15], v[20:23]\n"
    "s_add_u32 m0, s69, 141312\n"
    "ds_read_b64_tr_b16 v[224:225], v253 offset:16384\n"
    "ds_read_b64_tr_b16 v[226:227], v253 offset:16640\n"
    "v_mfma_f32_16x16x32_bf16 v[52:55], v[208:211], a[64:67], v[52:55]\n"
    "s_mov_b32 s96, 393600\n"
    "ds_read_b64_tr_b16 v[228:229], v253 offset:20480\n"
    "ds_read_b64_tr_b16 v[230:231], v253 offset:20736\n"
    "v_mfma_f32_16x16x32_bf16 v[52:55], v[212:215], a[68:71], v[52:55]\n"
    "v_exp_f32 v162, v162\n"
    "v_mfma_f32_16x16x32_bf16 v[84:87], v[208:211], a[120:123], v[84:87]\n"
    "v_exp_f32 v163, v163\n"
    "v_mfma_f32_16x16x32_bf16 v[84:87], v[212:215], a[124:127], v[84:87]\n"
    "v_mul_f32 v178, v178, v196\n"
    "v_mul_f32 v179, v179, v196\n"
    "v_mfma_f32_16x16x32_bf16 v[116:119], v[208:211], a[176:179], v[116:119]\n"
    "buffer_load_dwordx4 v249, s[88:91], s96 offen offset:0 lds\n"
    "v_mfma_f32_16x16x32_bf16 v[116:119], v[212:215], a[180:183], v[116:119]\n"
    "s_waitcnt lgkmcnt(4)\n"
    "v_exp_f32 v176, v176\n"
    "v_mfma_f32_16x16x32_bf16 v[28:31], v[216:219], a[8:11], v[28:31]\n"
    "s_add_u32 m0, s69, 77824\n"
    "v_exp_f32 v177, v177\n"
    "v_mfma_f32_16x16x32_bf16 v[28:31], v[220:223], a[12:15], v[28:31]\n"
    "s_mov_b32 s96, 1835392\n"
    "ds_read_b64_tr_b16 v[208:209], v254 offset:16384\n"
    "ds_read_b64_tr_b16 v[210:211], v254 offset:16640\n"
    "v_mfma_f32_16x16x32_bf16 v[60:63], v[216:219], a[64:67], v[60:63]\n"
    "s_nop 0\n"
    "ds_read_b64_tr_b16 v[212:213], v254 offset:20480\n"
    "ds_read_b64_tr_b16 v[214:215], v254 offset:20736\n"
    "v_mfma_f32_16x16x32_bf16 v[60:63], v[220:223], a[68:71], v[60:63]\n"
    "v_fma_f32 v160, v160, v200, v200\n"
    "v_fma_f32 v161, v161, v200, v200\n"
    "v_mfma_f32_16x16x32_bf16 v[92:95], v[216:219], a[120:123], v[92:95]\n"
    "v_exp_f32 v178, v178\n"
    "v_mfma_f32_16x16x32_bf16 v[92:95], v[220:223], a[124:127], v[92:95]\n"
    "v_exp_f32 v179, v179\n"
    "v_mfma_f32_16x16x32_bf16 v[124:127], v[216:219], a[176:179], v[124:127]\n"
    "v_fma_f32 v162, v162, v200, v200\n"
    "v_fma_f32 v163, v163, v200, v200\n"
    "v_mfma_f32_16x16x32_bf16 v[124:127], v[220:223], a[180:183], v[124:127]\n"
    "s_waitcnt lgkmcnt(4)\n"
    "v_rcp_f32 v160, v160\n"
    "v_mfma_f32_16x16x32_bf16 v[20:23], v[224:227], a[16:19], v[20:23]\n"
    "v_rcp_f32 v161, v161\n"
    "v_mfma_f32_16x16x32_bf16 v[20:23], v[228:231], a[20:23], v[20:23]\n"
    "s_nop 0\n"
    "ds_read_b64_tr_b16 v[216:217], v253 offset:24576\n"
    "ds_read_b64_tr_b16 v[218:219], v253 offset:24832\n"
    "v_mfma_f32_16x16x32_bf16 v[52:55], v[224:227], a[72:75], v[52:55]\n"
    "s_nop 0\n"
    "ds_read_b64_tr_b16 v[220:221], v254 offset:24576\n"
    "ds_read_b64_tr_b16 v[222:223], v254 offset:24832\n"
    "v_mfma_f32_16x16x32_bf16 v[52:55], v[228:231], a[76:79], v[52:55]\n"
    "v_fma_f32 v176, v176, v200, v200\n"
    "v_fma_f32 v177, v177, v200, v200\n"
    "v_mfma_f32_16x16x32_bf16 v[84:87], v[224:227], a[128:131], v[84:87]\n"
    "v_rcp_f32 v162, v162\n"
    "v_mfma_f32_16x16x32_bf16 v[84:87], v[228:231], a[132:135], v[84:87]\n"
    "v_rcp_f32 v163, v163\n"
    "v_mfma_f32_16x16x32_bf16 v[116:119], v[224:227], a[184:187], v[116:119]\n"
    "v_fma_f32 v178, v178, v200, v200\n"
    "v_fma_f32 v179, v179, v200, v200\n"
    "v_mfma_f32_16x16x32_bf16 v[116:119], v[228:231], a[188:191], v[116:119]\n"
    "s_waitcnt lgkmcnt(4)\n"
    "v_rcp_f32 v176, v176\n"
    "v_mfma_f32_16x16x32_bf16 v[28:31], v[208:211], a[16:19], v[28:31]\n"
    "s_waitcnt lgkmcnt(0) vmcnt(7)\n"
    "s_barrier\n"
    "v_rcp_f32 v177, v177\n"
    "v_mfma_f32_16x16x32_bf16 v[28:31], v[212:215], a[20:23], v[28:31]\n"
    "s_nop 0\n"
    "ds_read_b64_tr_b16 v[224:225], v253 offset:28672\n"
    "ds_read_b64_tr_b16 v[226:227], v253 offset:28928\n"
    "v_mfma_f32_16x16x32_bf16 v[60:63], v[208:211], a[72:75], v[60:63]\n"
    "s_nop 0\n"
    "ds_read_b64_tr_b16 v[228:229], v253 offset:32768\n"
    "ds_read_b64_tr_b16 v[230:231], v253 offset:33024\n"
    "v_mfma_f32_16x16x32_bf16 v[60:63], v[212:215], a[76:79], v[60:63]\n"
    "buffer_load_dwordx4 v249, s[88:91], s96 offen offset:0 lds\n"
    "v_mfma_f32_16x16x32_bf16 v[92:95], v[208:211], a[128:131], v[92:95]\n"
    "s_add_u32 m0, s69, 78848\n"
    "v_rcp_f32 v178, v178\n"
    "v_mfma_f32_16x16x32_bf16 v[92:95], v[212:215], a[132:135], v[92:95]\n"
    "s_mov_b32 s97, 1900928\n"
    "v_rcp_f32 v179, v179\n"
    "v_mfma_f32_16x16x32_bf16 v[124:127], v[208:211], a[184:187], v[124:127]\n"
    "ds_read_b128 v[12:15], v205 offset:51200\n"
    "v_mul_f32 v164, v164, v197\n"
    "v_mfma_f32_16x16x32_bf16 v[124:127], v[212:215], a[188:191], v[124:127]\n"
    "s_nop 0\n"
    "v_mul_f32 v165, v165, v197\n"
    "v_mul_f32 v166, v166, v197\n"
    "v_mfma_f32_16x16x32_bf16 v[20:23], v[216:219], a[24:27], v[20:23]\n"
    "ds_read_b128 v[44:47], v205 offset:52224\n"
    "v_mul_f32 v167, v167, v197\n"
    "v_mfma_f32_16x16x32_bf16 v[28:31], v[220:223], a[24:27], v[28:31]\n"
    "s_nop 0\n"
    "ds_read_b64_tr_b16 v[208:209], v254 offset:28672\n"
    "ds_read_b64_tr_b16 v[210:211], v254 offset:28928\n"
    "v_mfma_f32_16x16x32_bf16 v[52:55], v[216:219], a[80:83], v[52:55]\n"
    "s_nop 0\n"
    "ds_read_b64_tr_b16 v[212:213], v254 offset:32768\n"
    "ds_read_b64_tr_b16 v[214:215], v254 offset:33024\n"
    "v_mfma_f32_16x16x32_bf16 v[60:63], v[220:223], a[80:83], v[60:63]\n"
    "ds_read_b128 v[76:79], v205 offset:49152\n"
    "v_mfma_f32_16x16x32_bf16 v[84:87], v[216:219], a[136:139], v[84:87]\n"
    "ds_read_b128 v[108:111], v205 offset:50176\n"
    "v_mfma_f32_16x16x32_bf16 v[92:95], v[220:223], a[136:139], v[92:95]\n"
    "buffer_load_dwordx4 v255, s[88:91], s97 offen offset:0 lds\n"
    "v_mfma_f32_16x16x32_bf16 v[116:119], v[216:219], a[192:195], v[116:119]\n"
    "s_add_u32 m0, s69, 79872\n"
    "v_exp_f32 v164, v164\n"
    "v_mfma_f32_16x16x32_bf16 v[124:127], v[220:223], a[192:195], v[124:127]\n"
    "s_waitcnt lgkmcnt(8)\n"
    "v_exp_f32 v165, v165\n"
    "v_mfma_f32_16x16x32_bf16 v[20:23], v[224:227], a[28:31], v[20:23]\n"
    "s_mov_b32 s96, 1966464\n"
    "v_mul_f32 v180, v180, v197\n"
    "v_mul_f32 v181, v181, v197\n"
    "v_mfma_f32_16x16x32_bf16 v[20:23], v[228:231], a[32:35], v[20:23]\n"
    "s_nop 0\n"
    "ds_read_b64_tr_b16 v[216:217], v253 offset:36864\n"
    "ds_read_b64_tr_b16 v[218:219], v253 offset:37120\n"
    "v_mfma_f32_16x16x32_bf16 v[52:55], v[224:227], a[84:87], v[52:55]\n"
    "s_nop 0\n"
    "ds_read_b64_tr_b16 v[220:221], v253 offset:40960\n"
    "ds_read_b64_tr_b16 v[222:223], v253 offset:41216\n"
    "v_mfma_f32_16x16x32_bf16 v[52:55], v[228:231], a[88:91], v[52:55]\n"
    "buffer_load_dwordx4 v249, s[88:91], s96 offen offset:0 lds\n"
    "v_mfma_f32_16x16x32_bf16 v[84:87], v[224:227], a[140:143], v[84:87]\n"
    "s_add_u32 m0, s69, 80896\n"
    "v_exp_f32 v166, v166\n"
    "v_mfma_f32_16x16x32_bf16 v[84:87], v[228:231], a[144:147], v[84:87]\n"
    "s_mov_b32 s97, 2032000\n"
    "v_exp_f32 v167, v167\n"
    "v_mfma_f32_16x16x32_bf16 v[116:119], v[224:227], a[196:199], v[116:119]\n"
    "buffer_load_dwordx4 v255, s[88:91], s97 offen offset:0 lds\n"
    "v_mfma_f32_16x16x32_bf16 v[116:119], v[228:231], a[200:203], v[116:119]\n"
    "s_waitcnt lgkmcnt(6)\n"
    "v_mul_f32 v182, v182, v197\n"
    "v_mul_f32 v183, v183, v197\n"
    "v_mfma_f32_16x16x32_bf16 v[28:31], v[208:211], a[28:31], v[28:31]\n"
    "s_add_u32 m0, s69, 81920\n"
    "v_exp_f32 v180, v180\n"
    "v_mfma_f32_16x16x32_bf16 v[28:31], v[212:215], a[32:35], v[28:31]\n"
    "s_mov_b32 s96, 2097536\n"
    "ds_read_b64_tr_b16 v[224:225], v254 offset:36864\n"
    "ds_read_b64_tr_b16 v[226:227], v254 offset:37120\n"
    "v_mfma_f32_16x16x32_bf16 v[60:63], v[208:211], a[84:87], v[60:63]\n"
    "s_nop 0\n"
    "ds_read_b64_tr_b16 v[228:229], v254 offset:40960\n"
    "ds_read_b64_tr_b16 v[230:231], v254 offset:41216\n"
    "v_mfma_f32_16x16x32_bf16 v[60:63], v[212:215], a[88:91], v[60:63]\n"
    "buffer_load_dwordx4 v249, s[88:91], s96 offen offset:0 lds\n"
    "v_mfma_f32_16x16x32_bf16 v[92:95], v[208:211], a[140:143], v[92:95]\n"
    "s_add_u32 m0, s69, 82944\n"
    "v_exp_f32 v181, v181\n"
    "v_mfma_f32_16x16x32_bf16 v[92:95], v[212:215], a[144:147], v[92:95]\n"
    "s_mov_b32 s97, 2163072\n"
    "v_fma_f32 v164, v164, v201, v201\n"
    "v_fma_f32 v165, v165, v201, v201\n"
    "v_mfma_f32_16x16x32_bf16 v[124:127], v[208:211], a[196:199], v[124:127]\n"
    "v_exp_f32 v182, v182\n"
    "v_mfma_f32_16x16x32_bf16 v[124:127], v[212:215], a[200:203], v[124:127]\n"
    "s_waitcnt lgkmcnt(4)\n"
    "v_exp_f32 v183, v183\n"
    "v_mfma_f32_16x16x32_bf16 v[20:23], v[216:219], a[36:39], v[20:23]\n"
    "buffer_load_dwordx4 v255, s[88:91], s97 offen offset:0 lds\n"
    "v_mfma_f32_16x16x32_bf16 v[20:23], v[220:223], a[40:43], v[20:23]\n"
    "s_add_u32 m0, s69, 83968\n"
    "ds_read_b64_tr_b16 v[208:209], v253 offset:45056\n"
    "ds_read_b64_tr_b16 v[210:211], v253 offset:45312\n"
    "v_mfma_f32_16x16x32_bf16 v[52:55], v[216:219], a[92:95], v[52:55]\n"
    "s_mov_b32 s96, 2228608\n"
    "ds_read_b64_tr_b16 v[212:213], v253 offset:49152\n"
    "ds_read_b64_tr_b16 v[214:215], v253 offset:49408\n"
    "v_mfma_f32_16x16x32_bf16 v[52:55], v[220:223], a[96:99], v[52:55]\n"
    "v_fma_f32 v166, v166, v201, v201\n"
    "v_fma_f32 v167, v167, v201, v201\n"
    "v_mfma_f32_16x16x32_bf16 v[84:87], v[216:219], a[148:151], v[84:87]\n"
    "v_rcp_f32 v164, v164\n"
    "v_mfma_f32_16x16x32_bf16 v[84:87], v[220:223], a[152:155], v[84:87]\n"
    "v_rcp_f32 v165, v165\n"
    "v_mfma_f32_16x16x32_bf16 v[116:119], v[216:219], a[204:207], v[116:119]\n"
    "buffer_load_dwordx4 v249, s[88:91], s96 offen offset:0 lds\n"
    "v_mfma_f32_16x16x32_bf16 v[116:119], v[220:223], a[208:211], v[116:119]\n"
    "s_waitcnt lgkmcnt(4)\n"
    "v_fma_f32 v180, v180, v201, v201\n"
    "v_fma_f32 v181, v181, v201, v201\n"
    "v_mfma_f32_16x16x32_bf16 v[28:31], v[224:227], a[36:39], v[28:31]\n"
    "s_cmp_lt_u32 s28, 12\n"
    "v_rcp_f32 v166, v166\n"
    "v_mfma_f32_16x16x32_bf16 v[28:31], v[228:231], a[40:43], v[28:31]\n"
    "s_cselect_b32 s82, s82, 0\n"
    "ds_read_b64_tr_b16 v[216:217], v254 offset:45056\n"
    "ds_read_b64_tr_b16 v[218:219], v254 offset:45312\n"
    "v_mfma_f32_16x16x32_bf16 v[60:63], v[224:227], a[92:95], v[60:63]\n"
    "s_add_u32 m0, s69, 106496\n"
    "ds_read_b64_tr_b16 v[220:221], v254 offset:49152\n"
    "ds_read_b64_tr_b16 v[222:223], v254 offset:49408\n"
    "v_mfma_f32_16x16x32_bf16 v[60:63], v[228:231], a[96:99], v[60:63]\n"
    "s_mov_b32 s96, 2048\n"
    "v_rcp_f32 v167, v167\n"
    "v_mfma_f32_16x16x32_bf16 v[92:95], v[224:227], a[148:151], v[92:95]\n"
    "v_fma_f32 v182, v182, v201, v201\n"
    "v_fma_f32 v183, v183, v201, v201\n"
    "v_mfma_f32_16x16x32_bf16 v[92:95], v[228:231], a[152:155], v[92:95]\n"
    "v_rcp_f32 v180, v180\n"
    "v_mfma_f32_16x16x32_bf16 v[124:127], v[224:227], a[204:207], v[124:127]\n"
    "v_rcp_f32 v181, v181\n"
    "v_mfma_f32_16x16x32_bf16 v[124:127], v[228:231], a[208:211], v[124:127]\n"
    "s_waitcnt lgkmcnt(4)\n"
    "v_rcp_f32 v182, v182\n"
    "v_mfma_f32_16x16x32_bf16 v[20:23], v[208:211], a[44:47], v[20:23]\n"
    "v_rcp_f32 v183, v183\n"
    "v_mfma_f32_16x16x32_bf16 v[20:23], v[212:215], a[48:51], v[20:23]\n"
    "s_nop 0\n"
    "ds_read_b64_tr_b16 v[224:225], v253 offset:53248\n"
    "ds_read_b64_tr_b16 v[226:227], v253 offset:53504\n"
    "v_mfma_f32_16x16x32_bf16 v[52:55], v[208:211], a[100:103], v[52:55]\n"
    "s_nop 0\n"
    "ds_read_b64_tr_b16 v[228:229], v254 offset:53248\n"
    "ds_read_b64_tr_b16 v[230:231], v254 offset:53504\n"
    "v_mfma_f32_16x16x32_bf16 v[52:55], v[212:215], a[104:107], v[52:55]\n"
    "v_mul_f32 v168, v168, v198\n"
    "v_mul_f32 v169, v169, v198\n"
    "v_mfma_f32_16x16x32_bf16 v[84:87], v[208:211], a[156:159], v[84:87]\n"
    "v_mul_f32 v170, v170, v198\n"
    "v_mul_f32 v171, v171, v198\n"
    "v_mfma_f32_16x16x32_bf16 v[84:87], v[212:215], a[160:163], v[84:87]\n"
    "v_exp_f32 v168, v168\n"
    "v_mfma_f32_16x16x32_bf16 v[116:119], v[208:211], a[212:215], v[116:119]\n"
    "v_exp_f32 v169, v169\n"
    "v_mfma_f32_16x16x32_bf16 v[116:119], v[212:215], a[216:219], v[116:119]\n"
    "s_waitcnt lgkmcnt(4)\n"
    "v_mul_f32 v184, v184, v198\n"
    "v_mul_f32 v185, v185, v198\n"
    "v_mfma_f32_16x16x32_bf16 v[28:31], v[216:219], a[44:47], v[28:31]\n"
    "s_waitcnt lgkmcnt(0) vmcnt(7)\n"
    "s_barrier\n"
    "v_exp_f32 v170, v170\n"
    "v_mfma_f32_16x16x32_bf16 v[28:31], v[220:223], a[48:51], v[28:31]\n"
    "s_nop 0\n"
    "ds_read_b64_tr_b16 v[208:209], v251 offset:0\n"
    "ds_read_b64_tr_b16 v[210:211], v251 offset:256\n"
    "v_mfma_f32_16x16x32_bf16 v[60:63], v[216:219], a[100:103], v[60:63]\n"
    "s_nop 0\n"
    "ds_read_b64_tr_b16 v[212:213], v251 offset:4096\n"
    "ds_read_b64_tr_b16 v[214:215], v251 offset:4352\n"
    "v_mfma_f32_16x16x32_bf16 v[60:63], v[220:223], a[104:107], v[60:63]\n"
    "buffer_load_dwordx4 v250, s[80:83], s96 offen offset:0 lds\n"
    "v_mfma_f32_16x16x32_bf16 v[92:95], v[216:219], a[156:159], v[92:95]\n"
    "s_add_u32 m0, s69, 107520\n"
    "v_exp_f32 v171, v171\n"
    "v_mfma_f32_16x16x32_bf16 v[92:95], v[220:223], a[160:163], v[92:95]\n"
    "s_mov_b32 s97, 30720\n"
    "v_mul_f32 v186, v186, v198\n"
    "v_mul_f32 v187, v187, v198\n"
    "v_mfma_f32_16x16x32_bf16 v[124:127], v[216:219], a[212:215], v[124:127]\n"
    "v_exp_f32 v184, v184\n"
    "v_mfma_f32_16x16x32_bf16 v[124:127], v[220:223], a[216:219], v[124:127]\n"
    "s_nop 0\n"
    "v_exp_f32 v185, v185\n"
    "v_mfma_f32_16x16x32_bf16 v[20:23], v[224:227], a[52:55], v[20:23]\n"
    "buffer_load_dwordx4 v207, s[80:83], s97 offen offset:0 lds\n"
    "v_mfma_f32_16x16x32_bf16 v[28:31], v[228:231], a[52:55], v[28:31]\n"
    "s_add_u32 m0, s69, 108544\n"
    "ds_read_b64_tr_b16 v[216:217], v252 offset:0\n"
    "ds_read_b64_tr_b16 v[218:219], v252 offset:256\n"
    "v_mfma_f32_16x16x32_bf16 v[52:55], v[224:227], a[108:111], v[52:55]\n"
    "s_mov_b32 s96, 59392\n"
    "ds_read_b64_tr_b16 v[220:221], v252 offset:4096\n"
    "ds_read_b64_tr_b16 v[222:223], v252 offset:4352\n"
    "v_mfma_f32_16x16x32_bf16 v[60:63], v[228:231], a[108:111], v[60:63]\n"
    "v_fma_f32 v168, v168, v202, v202\n"
    "v_fma_f32 v169, v169, v202, v202\n"
    "v_mfma_f32_16x16x32_bf16 v[84:87], v[224:227], a[164:167], v[84:87]\n"
    "v_exp_f32 v186, v186\n"
    "v_mfma_f32_16x16x32_bf16 v[92:95], v[228:231], a[164:167], v[92:95]\n"
    "v_exp_f32 v187, v187\n"
    "v_mfma_f32_16x16x32_bf16 v[116:119], v[224:227], a[220:223], v[116:119]\n"
    "buffer_load_dwordx4 v250, s[80:83], s96 offen offset:0 lds\n"
    "v_mfma_f32_16x16x32_bf16 v[124:127], v[228:231], a[220:223], v[124:127]\n"
    "s_waitcnt lgkmcnt(4)\n"
    "v_fma_f32 v170, v170, v202, v202\n"
    "v_fma_f32 v171, v171, v202, v202\n"
    "v_mfma_f32_16x16x32_bf16 v[128:131], v[208:211], a[0:3], 0\n"
    "s_add_u32 m0, s69, 109568\n"
    "v_rcp_f32 v168, v168\n"
    "v_mfma_f32_16x16x32_bf16 v[128:131], v[212:215], a[4:7], v[128:131]\n"
    "s_mov_b32 s97, 88064\n"
    "ds_read_b64_tr_b16 v[224:225], v251 offset:8192\n"
    "ds_read_b64_tr_b16 v[226:227], v251 offset:8448\n"
    "v_mfma_f32_16x16x32_bf16 v[132:135], v[208:211], a[56:59], 0\n"
    "s_nop 0\n"
    "ds_read_b64_tr_b16 v[228:229], v251 offset:12288\n"
    "ds_read_b64_tr_b16 v[230:231], v251 offset:12544\n"
    "v_mfma_f32_16x16x32_bf16 v[132:135], v[212:215], a[60:63], v[132:135]\n"
    "v_rcp_f32 v169, v169\n"
    "v_mfma_f32_16x16x32_bf16 v[136:139], v[208:211], a[112:115], 0\n"
    "buffer_load_dwordx4 v207, s[80:83], s97 offen offset:0 lds\n"
    "v_mfma_f32_16x16x32_bf16 v[136:139], v[212:215], a[116:119], v[136:139]\n"
    "s_add_u32 m0, s69, 110592\n"
    "v_fma_f32 v184, v184, v202, v202\n"
    "v_fma_f32 v185, v185, v202, v202\n"
    "v_mfma_f32_16x16x32_bf16 v[140:143], v[208:211], a[168:171], 0\n"
    "s_mov_b32 s96, 116736\n"
    "v_rcp_f32 v170, v170\n"
    "v_mfma_f32_16x16x32_bf16 v[140:143], v[212:215], a[172:175], v[140:143]\n"
    "s_waitcnt lgkmcnt(4)\n"
    "v_rcp_f32 v171, v171\n"
    "v_mfma_f32_16x16x32_bf16 v[144:147], v[216:219], a[0:3], 0\n"
    "v_fma_f32 v186, v186, v202, v202\n"
    "v_fma_f32 v187, v187, v202, v202\n"
    "v_mfma_f32_16x16x32_bf16 v[144:147], v[220:223], a[4:7], v[144:147]\n"
    "s_nop 0\n"
    "ds_read_b64_tr_b16 v[208:209], v252 offset:8192\n"
    "ds_read_b64_tr_b16 v[210:211], v252 offset:8448\n"
    "v_mfma_f32_16x16x32_bf16 v[148:151], v[216:219], a[56:59], 0\n"
    "s_nop 0\n"
    "ds_read_b64_tr_b16 v[212:213], v252 offset:12288\n"
    "ds_read_b64_tr_b16 v[214:215], v252 offset:12544\n"
    "v_mfma_f32_16x16x32_bf16 v[148:151], v[220:223], a[60:63], v[148:151]\n"
    "buffer_load_dwordx4 v250, s[80:83], s96 offen offset:0 lds\n"
    "v_mfma_f32_16x16x32_bf16 v[152:155], v[216:219], a[112:115], 0\n"
    "s_add_u32 m0, s69, 111616\n"
    "v_rcp_f32 v184, v184\n"
    "v_mfma_f32_16x16x32_bf16 v[152:155], v[220:223], a[116:119], v[152:155]\n"
    "s_mov_b32 s97, 145408\n"
    "v_rcp_f32 v185, v185\n"
    "v_mfma_f32_16x16x32_bf16 v[156:159], v[216:219], a[168:171], 0\n"
    "v_rcp_f32 v186, v186\n"
    "v_mfma_f32_16x16x32_bf16 v[156:159], v[220:223], a[172:175], v[156:159]\n"
    "s_waitcnt lgkmcnt(4)\n"
    "v_rcp_f32 v187, v187\n"
    "v_mfma_f32_16x16x32_bf16 v[128:131], v[224:227], a[8:11], v[128:131]\n"
    "buffer_load_dwordx4 v207, s[80:83], s97 offen offset:0 lds\n"
    "v_mfma_f32_16x16x32_bf16 v[128:131], v[228:231], a[12:15], v[128:131]\n"
    "s_add_u32 m0, s69, 112640\n"
    "ds_read_b64_tr_b16 v[216:217], v251 offset:16384\n"
    "ds_read_b64_tr_b16 v[218:219], v251 offset:16640\n"
    "v_mfma_f32_16x16x32_bf16 v[132:135], v[224:227], a[64:67], v[132:135]\n"
    "s_mov_b32 s96, 174080\n"
    "ds_read_b64_tr_b16 v[220:221], v251 offset:20480\n"
    "ds_read_b64_tr_b16 v[222:223], v251 offset:20736\n"
    "v_mfma_f32_16x16x32_bf16 v[132:135], v[228:231], a[68:71], v[132:135]\n"
    "v_mul_f32 v172, v172, v199\n"
    "v_mul_f32 v173, v173, v199\n"
    "v_mfma_f32_16x16x32_bf16 v[136:139], v[224:227], a[120:123], v[136:139]\n"
    "v_mul_f32 v174, v174, v199\n"
    "v_mul_f32 v175, v175, v199\n"
    "v_mfma_f32_16x16x32_bf16 v[136:139], v[228:231], a[124:127], v[136:139]\n"
    "v_exp_f32 v172, v172\n"
    "v_mfma_f32_16x16x32_bf16 v[140:143], v[224:227], a[176:179], v[140:143]\n"
    "buffer_load_dwordx4 v250, s[80:83], s96 offen offset:0 lds\n"
    "v_mfma_f32_16x16x32_bf16 v[140:143], v[228:231], a[180:183], v[140:143]\n"
    "s_waitcnt lgkmcnt(4)\n"
    "v_exp_f32 v173, v173\n"
    "v_mfma_f32_16x16x32_bf16 v[144:147], v[208:211], a[8:11], v[144:147]\n"
    "s_add_u32 m0, s69, 135168\n"
    "v_mul_f32 v188, v188, v199\n"
    "v_mul_f32 v189, v189, v199\n"
    "v_mfma_f32_16x16x32_bf16 v[144:147], v[212:215], a[12:15], v[144:147]\n"
    "s_mov_b32 s96, 804864\n"
    "ds_read_b64_tr_b16 v[224:225], v252 offset:16384\n"
    "ds_read_b64_tr_b16 v[226:227], v252 offset:16640\n"
    "v_mfma_f32_16x16x32_bf16 v[148:151], v[208:211], a[64:67], v[148:151]\n"
    "s_nop 0\n"
    "ds_read_b64_tr_b16 v[228:229], v252 offset:20480\n"
    "ds_read_b64_tr_b16 v[230:231], v252 offset:20736\n"
    "v_mfma_f32_16x16x32_bf16 v[148:151], v[212:215], a[68:71], v[148:151]\n"
    "v_exp_f32 v174, v174\n"
    "v_mfma_f32_16x16x32_bf16 v[152:155], v[208:211], a[120:123], v[152:155]\n"
    "v_exp_f32 v175, v175\n"
    "v_mfma_f32_16x16x32_bf16 v[152:155], v[212:215], a[124:127], v[152:155]\n"
    "v_mul_f32 v190, v190, v199\n"
    "v_mul_f32 v191, v191, v199\n"
    "v_mfma_f32_16x16x32_bf16 v[156:159], v[208:211], a[176:179], v[156:159]\n"
    "v_exp_f32 v188, v188\n"
    "v_mfma_f32_16x16x32_bf16 v[156:159], v[212:215], a[180:183], v[156:159]\n"
    "s_waitcnt lgkmcnt(4)\n"
    "v_exp_f32 v189, v189\n"
    "v_mfma_f32_16x16x32_bf16 v[128:131], v[216:219], a[16:19], v[128:131]\n"
    "v_fma_f32 v172, v172, v203, v203\n"
    "v_fma_f32 v173, v173, v203, v203\n"
    "v_mfma_f32_16x16x32_bf16 v[128:131], v[220:223], a[20:23], v[128:131]\n"
    "s_nop 0\n"
    "ds_read_b64_tr_b16 v[208:209], v251 offset:24576\n"
    "ds_read_b64_tr_b16 v[210:211], v251 offset:24832\n"
    "v_mfma_f32_16x16x32_bf16 v[132:135], v[216:219], a[72:75], v[132:135]\n"
    "s_nop 0\n"
    "ds_read_b64_tr_b16 v[212:213], v252 offset:24576\n"
    "ds_read_b64_tr_b16 v[214:215], v252 offset:24832\n"
    "v_mfma_f32_16x16x32_bf16 v[132:135], v[220:223], a[76:79], v[132:135]\n"
    "v_exp_f32 v190, v190\n"
    "v_mfma_f32_16x16x32_bf16 v[136:139], v[216:219], a[128:131], v[136:139]\n"
    "v_exp_f32 v191, v191\n"
    "v_mfma_f32_16x16x32_bf16 v[136:139], v[220:223], a[132:135], v[136:139]\n"
    "v_fma_f32 v174, v174, v203, v203\n"
    "v_fma_f32 v175, v175, v203, v203\n"
    "v_mfma_f32_16x16x32_bf16 v[140:143], v[216:219], a[184:187], v[140:143]\n"
    "v_rcp_f32 v172, v172\n"
    "v_mfma_f32_16x16x32_bf16 v[140:143], v[220:223], a[188:191], v[140:143]\n"
    "s_waitcnt lgkmcnt(4)\n"
    "v_rcp_f32 v173, v173\n"
    "v_mfma_f32_16x16x32_bf16 v[144:147], v[224:227], a[16:19], v[144:147]\n"
    "s_waitcnt lgkmcnt(0) vmcnt(7)\n"
    "s_barrier\n"
    "v_fma_f32 v188, v188, v203, v203\n"
    "v_fma_f32 v189, v189, v203, v203\n"
    "v_mfma_f32_16x16x32_bf16 v[144:147], v[228:231], a[20:23], v[144:147]\n"
    "s_nop 0\n"
    "ds_read_b64_tr_b16 v[216:217], v253 offset:0\n"
    "ds_read_b64_tr_b16 v[218:219], v253 offset:256\n"
    "v_mfma_f32_16x16x32_bf16 v[148:151], v[224:227], a[72:75], v[148:151]\n"
    "s_nop 0\n"
    "ds_read_b64_tr_b16 v[220:221], v253 offset:4096\n"
    "ds_read_b64_tr_b16 v[222:223], v253 offset:4352\n"
    "v_mfma_f32_16x16x32_bf16 v[148:151], v[228:231], a[76:79], v[148:151]\n"
    "buffer_load_dwordx4 v250, s[80:83], s96 offen offset:0 lds\n"
    "v_mfma_f32_16x16x32_bf16 v[152:155], v[224:227], a[128:131], v[152:155]\n"
    "s_add_u32 m0, s69, 136192\n"
    "v_rcp_f32 v174, v174\n"
    "v_mfma_f32_16x16x32_bf16 v[152:155], v[228:231], a[132:135], v[152:155]\n"
    "s_mov_b32 s97, 833536\n"
    "v_rcp_f32 v175, v175\n"
    "v_mfma_f32_16x16x32_bf16 v[156:159], v[224:227], a[184:187], v[156:159]\n"
    "v_fma_f32 v190, v190, v203, v203\n"
    "v_fma_f32 v191, v191, v203, v203\n"
    "v_mfma_f32_16x16x32_bf16 v[156:159], v[228:231], a[188:191], v[156:159]\n"
    "s_nop 0\n"
    "v_rcp_f32 v188, v188\n"
    "v_mfma_f32_16x16x32_bf16 v[128:131], v[208:211], a[24:27], v[128:131]\n"
    "buffer_load_dwordx4 v207, s[80:83], s97 offen offset:0 lds\n"
    "v_mfma_f32_16x16x32_bf16 v[144:147], v[212:215], a[24:27], v[144:147]\n"
    "s_add_u32 m0, s69, 137216\n"
    "ds_read_b64_tr_b16 v[224:225], v254 offset:0\n"
    "ds_read_b64_tr_b16 v[226:227], v254 offset:256\n"
    "v_mfma_f32_16x16x32_bf16 v[132:135], v[208:211], a[80:83], v[132:135]\n"
    "s_mov_b32 s96, 862208\n"
    "ds_read_b64_tr_b16 v[228:229], v254 offset:4096\n"
    "ds_read_b64_tr_b16 v[230:231], v254 offset:4352\n"
    "v_mfma_f32_16x16x32_bf16 v[148:151], v[212:215], a[80:83], v[148:151]\n"
    "v_rcp_f32 v189, v189\n"
    "v_mfma_f32_16x16x32_bf16 v[136:139], v[208:211], a[136:139], v[136:139]\n"
    "v_rcp_f32 v190, v190\n"
    "v_mfma_f32_16x16x32_bf16 v[152:155], v[212:215], a[136:139], v[152:155]\n"
    "v_rcp_f32 v191, v191\n"
    "v_mfma_f32_16x16x32_bf16 v[140:143], v[208:211], a[192:195], v[140:143]\n"
    "buffer_load_dwordx4 v250, s[80:83], s96 offen offset:0 lds\n"
    "v_mfma_f32_16x16x32_bf16 v[156:159], v[212:215], a[192:195], v[156:159]\n"
    "s_waitcnt lgkmcnt(4)\n"
    "v_mul_f32 v20, v20, v192\n"
    "v_mul_f32 v21, v21, v192\n"
    "v_mfma_f32_16x16x32_bf16 v[128:131], v[216:219], a[28:31], v[128:131]\n"
    "s_add_u32 m0, s69, 138240\n"
    "v_mul_f32 v22, v22, v192\n"
    "v_mul_f32 v23, v23, v192\n"
    "v_mfma_f32_16x16x32_bf16 v[128:131], v[220:223], a[32:35], v[128:131]\n"
    "s_mov_b32 s97, 890880\n"
    "ds_read_b64_tr_b16 v[208:209], v253 offset:8192\n"
    "ds_read_b64_tr_b16 v[210:211], v253 offset:8448\n"
    "v_mfma_f32_16x16x32_bf16 v[132:135], v[216:219], a[84:87], v[132:135]\n"
    "s_nop 0\n"
    "ds_read_b64_tr_b16 v[212:213], v253 offset:12288\n"
    "ds_read_b64_tr_b16 v[214:215], v253 offset:12544\n"
    "v_mfma_f32_16x16x32_bf16 v[132:135], v[220:223], a[88:91], v[132:135]\n"
    "v_mul_f32 v28, v28, v192\n"
    "v_mul_f32 v29, v29, v192\n"
    "v_mfma_f32_16x16x32_bf16 v[136:139], v[216:219], a[140:143], v[136:139]\n"
    "buffer_load_dwordx4 v207, s[80:83], s97 offen offset:0 lds\n"
    "v_mfma_f32_16x16x32_bf16 v[136:139], v[220:223], a[144:147], v[136:139]\n"
    "s_add_u32 m0, s69, 139264\n"
    "v_mul_f32 v30, v30, v192\n"
    "v_mul_f32 v31, v31, v192\n"
    "v_mfma_f32_16x16x32_bf16 v[140:143], v[216:219], a[196:199], v[140:143]\n"
    "s_mov_b32 s96, 919552\n"
    "v_cvt_pk_bf16_f32 v16, v20, v21\n"
    "v_cvt_pk_bf16_f32 v17, v22, v23\n"
    "v_mfma_f32_16x16x32_bf16 v[140:143], v[220:223], a[200:203], v[140:143]\n"
    "s_waitcnt lgkmcnt(4)\n"
    "v_cvt_pk_bf16_f32 v18, v28, v29\n"
    "v_cvt_pk_bf16_f32 v19, v30, v31\n"
    "v_mfma_f32_16x16x32_bf16 v[144:147], v[224:227], a[28:31], v[144:147]\n"
    "v_mul_f32 v52, v52, v193\n"
    "v_mul_f32 v53, v53, v193\n"
    "v_mfma_f32_16x16x32_bf16 v[144:147], v[228:231], a[32:35], v[144:147]\n"
    "s_nop 0\n"
    "ds_read_b64_tr_b16 v[216:217], v254 offset:8192\n"
    "ds_read_b64_tr_b16 v[218:219], v254 offset:8448\n"
    "v_mfma_f32_16x16x32_bf16 v[148:151], v[224:227], a[84:87], v[148:151]\n"
    "s_nop 0\n"
    "ds_read_b64_tr_b16 v[220:221], v254 offset:12288\n"
    "ds_read_b64_tr_b16 v[222:223], v254 offset:12544\n"
    "v_mfma_f32_16x16x32_bf16 v[148:151], v[228:231], a[88:91], v[148:151]\n"
    "buffer_load_dwordx4 v250, s[80:83], s96 offen offset:0 lds\n"
    "v_mfma_f32_16x16x32_bf16 v[152:155], v[224:227], a[140:143], v[152:155]\n"
    "s_add_u32 m0, s69, 140288\n"
    "v_mul_f32 v54, v54, v193\n"
    "v_mul_f32 v55, v55, v193\n"
    "v_mfma_f32_16x16x32_bf16 v[152:155], v[228:231], a[144:147], v[152:155]\n"
    "s_mov_b32 s97, 948224\n"
    "v_mul_f32 v60, v60, v193\n"
    "v_mul_f32 v61, v61, v193\n"
    "v_mfma_f32_16x16x32_bf16 v[156:159], v[224:227], a[196:199], v[156:159]\n"
    "v_mul_f32 v62, v62, v193\n"
    "v_mul_f32 v63, v63, v193\n"
    "v_mfma_f32_16x16x32_bf16 v[156:159], v[228:231], a[200:203], v[156:159]\n"
    "s_waitcnt lgkmcnt(4)\n"
    "v_cvt_pk_bf16_f32 v48, v52, v53\n"
    "v_cvt_pk_bf16_f32 v49, v54, v55\n"
    "v_mfma_f32_16x16x32_bf16 v[128:131], v[208:211], a[36:39], v[128:131]\n"
    "buffer_load_dwordx4 v207, s[80:83], s97 offen offset:0 lds\n"
    "v_mfma_f32_16x16x32_bf16 v[128:131], v[212:215], a[40:43], v[128:131]\n"
    "s_add_u32 m0, s69, 141312\n"
    "ds_read_b64_tr_b16 v[224:225], v253 offset:16384\n"
    "ds_read_b64_tr_b16 v[226:227], v253 offset:16640\n"
    "v_mfma_f32_16x16x32_bf16 v[132:135], v[208:211], a[92:95], v[132:135]\n"
    "s_mov_b32 s96, 976896\n"
    "ds_read_b64_tr_b16 v[228:229], v253 offset:20480\n"
    "ds_read_b64_tr_b16 v[230:231], v253 offset:20736\n"
    "v_mfma_f32_16x16x32_bf16 v[132:135], v[212:215], a[96:99], v[132:135]\n"
    "v_cvt_pk_bf16_f32 v50, v60, v61\n"
    "v_cvt_pk_bf16_f32 v51, v62, v63\n"
    "v_mfma_f32_16x16x32_bf16 v[136:139], v[208:211], a[148:151], v[136:139]\n"
    "v_permlane32_swap_b32 v16, v18\n"
    "v_permlane32_swap_b32 v17, v19\n"
    "v_mfma_f32_16x16x32_bf16 v[136:139], v[212:215], a[152:155], v[136:139]\n"
    "v_permlane32_swap_b32 v48, v50\n"
    "v_permlane32_swap_b32 v49, v51\n"
    "v_mfma_f32_16x16x32_bf16 v[140:143], v[208:211], a[204:207], v[140:143]\n"
    "buffer_load_dwordx4 v250, s[80:83], s96 offen offset:0 lds\n"
    "v_mfma_f32_16x16x32_bf16 v[140:143], v[212:215], a[208:211], v[140:143]\n"
    "s_waitcnt lgkmcnt(4)\n"
    "v_permlane16_swap_b32 v16, v18\n"
    "v_permlane16_swap_b32 v17, v19\n"
    "v_mfma_f32_16x16x32_bf16 v[144:147], v[216:219], a[36:39], v[144:147]\n"
    "s_and_b32 s31, s68, 32767\n"
    "v_permlane16_swap_b32 v48, v50\n"
    "v_permlane16_swap_b32 v49, v51\n"
    "v_mfma_f32_16x16x32_bf16 v[144:147], v[220:223], a[40:43], v[144:147]\n"
    "s_nop 0\n"
    "ds_read_b64_tr_b16 v[208:209], v254 offset:16384\n"
    "ds_read_b64_tr_b16 v[210:211], v254 offset:16640\n"
    "v_mfma_f32_16x16x32_bf16 v[148:151], v[216:219], a[92:95], v[148:151]\n"
    "s_nop 0\n"
    "ds_read_b64_tr_b16 v[212:213], v254 offset:20480\n"
    "ds_read_b64_tr_b16 v[214:215], v254 offset:20736\n"
    "v_mfma_f32_16x16x32_bf16 v[148:151], v[220:223], a[96:99], v[148:151]\n"
    "buffer_load_dword v255, v247, s[64:67], s31 offen offset:0\n"
    "v_mfma_f32_16x16x32_bf16 v[152:155], v[216:219], a[148:151], v[152:155]\n"
    "s_add_u32 s31, s31, 32768\n"
    "ds_write_b128 v204, v[16:19] offset:32768\n"
    "v_mfma_f32_16x16x32_bf16 v[152:155], v[220:223], a[152:155], v[152:155]\n"
    "ds_write_b128 v204, v[48:51] offset:33792\n"
    "v_mfma_f32_16x16x32_bf16 v[156:159], v[216:219], a[204:207], v[156:159]\n"
    "buffer_load_dword v255, v247, s[64:67], s31 offen offset:0\n"
    "v_mfma_f32_16x16x32_bf16 v[156:159], v[220:223], a[208:211], v[156:159]\n"
    "s_waitcnt lgkmcnt(6)\n"
    "v_mul_f32 v84, v84, v194\n"
    "v_mul_f32 v85, v85, v194\n"
    "v_mfma_f32_16x16x32_bf16 v[128:131], v[224:227], a[44:47], v[128:131]\n"
    "s_add_u32 s31, s31, 32768\n"
    "v_mul_f32 v86, v86, v194\n"
    "v_mul_f32 v87, v87, v194\n"
    "v_mfma_f32_16x16x32_bf16 v[128:131], v[228:231], a[48:51], v[128:131]\n"
    "s_nop 0\n"
    "ds_read_b64_tr_b16 v[216:217], v253 offset:24576\n"
    "ds_read_b64_tr_b16 v[218:219], v253 offset:24832\n"
    "v_mfma_f32_16x16x32_bf16 v[132:135], v[224:227], a[100:103], v[132:135]\n"
    "s_nop 0\n"
    "ds_read_b64_tr_b16 v[220:221], v254 offset:24576\n"
    "ds_read_b64_tr_b16 v[222:223], v254 offset:24832\n"
    "v_mfma_f32_16x16x32_bf16 v[132:135], v[228:231], a[104:107], v[132:135]\n"
    "v_mul_f32 v92, v92, v194\n"
    "v_mul_f32 v93, v93, v194\n"
    "v_mfma_f32_16x16x32_bf16 v[136:139], v[224:227], a[156:159], v[136:139]\n"
    "buffer_load_dword v255, v247, s[64:67], s31 offen offset:0\n"
    "v_mfma_f32_16x16x32_bf16 v[136:139], v[228:231], a[160:163], v[136:139]\n"
    "s_add_u32 s31, s31, 32768\n"
    "v_mul_f32 v94, v94, v194\n"
    "v_mul_f32 v95, v95, v194\n"
    "v_mfma_f32_16x16x32_bf16 v[140:143], v[224:227], a[212:215], v[140:143]\n"
    "v_cvt_pk_bf16_f32 v80, v84, v85\n"
    "v_cvt_pk_bf16_f32 v81, v86, v87\n"
    "v_mfma_f32_16x16x32_bf16 v[140:143], v[228:231], a[216:219], v[140:143]\n"
    "s_waitcnt lgkmcnt(6)\n"
    "v_cvt_pk_bf16_f32 v82, v92, v93\n"
    "v_cvt_pk_bf16_f32 v83, v94, v95\n"
    "v_mfma_f32_16x16x32_bf16 v[144:147], v[208:211], a[44:47], v[144:147]\n"
    "s_waitcnt lgkmcnt(0) vmcnt(10)\n"
    "s_barrier\n"
    "v_mul_f32 v116, v116, v195\n"
    "v_mul_f32 v117, v117, v195\n"
    "v_mfma_f32_16x16x32_bf16 v[144:147], v[212:215], a[48:51], v[144:147]\n"
    "s_nop 0\n"
    "ds_read_b64_tr_b16 v[224:225], v253 offset:28672\n"
    "ds_read_b64_tr_b16 v[226:227], v253 offset:28928\n"
    "v_mfma_f32_16x16x32_bf16 v[148:151], v[208:211], a[100:103], v[148:151]\n"
    "s_nop 0\n"
    "ds_read_b64_tr_b16 v[228:229], v253 offset:32768\n"
    "ds_read_b64_tr_b16 v[230:231], v253 offset:33024\n"
    "v_mfma_f32_16x16x32_bf16 v[148:151], v[212:215], a[104:107], v[148:151]\n"
    "buffer_load_dword v255, v247, s[64:67], s31 offen offset:0\n"
    "v_mfma_f32_16x16x32_bf16 v[152:155], v[208:211], a[156:159], v[152:155]\n"
    "s_add_u32 m0, s52, 0\n"
    "v_mul_f32 v118, v118, v195\n"
    "v_mul_f32 v119, v119, v195\n"
    "v_mfma_f32_16x16x32_bf16 v[152:155], v[212:215], a[160:163], v[152:155]\n"
    "buffer_load_dwordx4 v247, s[72:75], s44 offen offset:0 lds\n"
    "v_mfma_f32_16x16x32_bf16 v[156:159], v[208:211], a[212:215], v[156:159]\n"
    "v_mul_f32 v124, v124, v195\n"
    "v_mul_f32 v125, v125, v195\n"
    "v_mfma_f32_16x16x32_bf16 v[156:159], v[212:215], a[216:219], v[156:159]\n"
    "s_nop 0\n"
    "v_mul_f32 v126, v126, v195\n"
    "v_mul_f32 v127, v127, v195\n"
    "v_mfma_f32_16x16x32_bf16 v[128:131], v[216:219], a[52:55], v[128:131]\n"
    "buffer_load_dwordx4 v247, s[72:75], s45 offen offset:1024 lds\n"
    "v_mfma_f32_16x16x32_bf16 v[144:147], v[220:223], a[52:55], v[144:147]\n"
    "s_nop 0\n"
    "ds_read_b64_tr_b16 v[208:209], v254 offset:28672\n"
    "ds_read_b64_tr_b16 v[210:211], v254 offset:28928\n"
    "v_mfma_f32_16x16x32_bf16 v[132:135], v[216:219], a[108:111], v[132:135]\n"
    "s_nop 0\n"
    "ds_read_b64_tr_b16 v[212:213], v254 offset:32768\n"
    "ds_read_b64_tr_b16 v[214:215], v254 offset:33024\n"
    "v_mfma_f32_16x16x32_bf16 v[148:151], v[220:223], a[108:111], v[148:151]\n"
    "v_cvt_pk_bf16_f32 v112, v116, v117\n"
    "v_cvt_pk_bf16_f32 v113, v118, v119\n"
    "v_mfma_f32_16x16x32_bf16 v[136:139], v[216:219], a[164:167], v[136:139]\n"
    "v_cvt_pk_bf16_f32 v114, v124, v125\n"
    "v_cvt_pk_bf16_f32 v115, v126, v127\n"
    "v_mfma_f32_16x16x32_bf16 v[152:155], v[220:223], a[164:167], v[152:155]\n"
    "v_permlane32_swap_b32 v80, v82\n"
    "v_permlane32_swap_b32 v81, v83\n"
    "v_mfma_f32_16x16x32_bf16 v[140:143], v[216:219], a[220:223], v[140:143]\n"
    "buffer_load_dwordx4 v247, s[72:75], s46 offen offset:2048 lds\n"
    "v_mfma_f32_16x16x32_bf16 v[156:159], v[220:223], a[220:223], v[156:159]\n"
    "s_waitcnt lgkmcnt(4)\n"
    "v_permlane32_swap_b32 v112, v114\n"
    "v_permlane32_swap_b32 v113, v115\n"
    "v_mfma_f32_16x16x32_bf16 a[224:227], v[224:227], a[0:3], 0\n"
    "v_permlane16_swap_b32 v80, v82\n"
    "v_permlane16_swap_b32 v81, v83\n"
    "v_mfma_f32_16x16x32_bf16 a[224:227], v[228:231], a[4:7], a[224:227]\n"
    "s_nop 0\n"
    "ds_read_b64_tr_b16 v[216:217], v253 offset:36864\n"
    "ds_read_b64_tr_b16 v[218:219], v253 offset:37120\n"
    "v_mfma_f32_16x16x32_bf16 a[228:231], v[224:227], a[56:59], 0\n"
    "s_nop 0\n"
    "ds_read_b64_tr_b16 v[220:221], v253 offset:40960\n"
    "ds_read_b64_tr_b16 v[222:223], v253 offset:41216\n"
    "v_mfma_f32_16x16x32_bf16 a[228:231], v[228:231], a[60:63], a[228:231]\n"
    "v_permlane16_swap_b32 v112, v114\n"
    "v_permlane16_swap_b32 v113, v115\n"
    "v_mfma_f32_16x16x32_bf16 a[232:235], v[224:227], a[112:115], 0\n"
    "buffer_load_dwordx4 v247, s[72:75], s47 offen offset:3072 lds\n"
    "v_mfma_f32_16x16x32_bf16 a[232:235], v[228:231], a[116:119], a[232:235]\n"
    "s_add_u32 m0, s52, 4096\n"
    "ds_write_b128 v204, v[80:83] offset:34816\n"
    "v_mfma_f32_16x16x32_bf16 a[236:239], v[224:227], a[168:171], 0\n"
    "ds_write_b128 v204, v[112:115] offset:35840\n"
    "v_mfma_f32_16x16x32_bf16 a[236:239], v[228:231], a[172:175], a[236:239]\n"
    "s_waitcnt lgkmcnt(6)\n"
    "v_mfma_f32_16x16x32_bf16 a[240:243], v[208:211], a[0:3], 0\n"
    "v_add_u32 v204, 65536, v204\n"
    "v_add_u32 v207, 65536, v205\n"
    "v_mfma_f32_16x16x32_bf16 a[240:243], v[212:215], a[4:7], a[240:243]\n"
    "s_nop 0\n"
    "ds_read_b64_tr_b16 v[224:225], v254 offset:36864\n"
    "ds_read_b64_tr_b16 v[226:227], v254 offset:37120\n"
    "v_mfma_f32_16x16x32_bf16 a[244:247], v[208:211], a[56:59], 0\n"
    "s_nop 0\n"
    "ds_read_b64_tr_b16 v[228:229], v254 offset:40960\n"
    "ds_read_b64_tr_b16 v[230:231], v254 offset:41216\n"
    "v_mfma_f32_16x16x32_bf16 a[244:247], v[212:215], a[60:63], a[244:247]\n"
    "buffer_load_dwordx4 v247, s[72:75], s48 offen offset:0 lds\n"
    "v_mfma_f32_16x16x32_bf16 a[248:251], v[208:211], a[112:115], 0\n"
    "v_mul_f32 v128, v128, v192\n"
    "v_mul_f32 v129, v129, v192\n"
    "v_mfma_f32_16x16x32_bf16 a[248:251], v[212:215], a[116:119], a[248:251]\n"
    "v_mul_f32 v130, v130, v192\n"
    "v_mul_f32 v131, v131, v192\n"
    "v_mfma_f32_16x16x32_bf16 a[252:255], v[208:211], a[168:171], 0\n"
    "v_mul_f32 v144, v144, v192\n"
    "v_mul_f32 v145, v145, v192\n"
    "v_mfma_f32_16x16x32_bf16 a[252:255], v[212:215], a[172:175], a[252:255]\n"
    "s_waitcnt lgkmcnt(6)\n"
    "v_mul_f32 v146, v146, v192\n"
    "v_mul_f32 v147, v147, v192\n"
    "v_mfma_f32_16x16x32_bf16 a[224:227], v[216:219], a[8:11], a[224:227]\n"
    "buffer_load_dwordx4 v247, s[72:75], s49 offen offset:1024 lds\n"
    "v_mfma_f32_16x16x32_bf16 a[224:227], v[220:223], a[12:15], a[224:227]\n"
    "s_nop 0\n"
    "ds_read_b64_tr_b16 v[208:209], v253 offset:45056\n"
    "ds_read_b64_tr_b16 v[210:211], v253 offset:45312\n"
    "v_mfma_f32_16x16x32_bf16 a[228:231], v[216:219], a[64:67], a[228:231]\n"
    "s_nop 0\n"
    "ds_read_b64_tr_b16 v[212:213], v253 offset:49152\n"
    "ds_read_b64_tr_b16 v[214:215], v253 offset:49408\n"
    "v_mfma_f32_16x16x32_bf16 a[228:231], v[220:223], a[68:71], a[228:231]\n"
    "v_cvt_pk_bf16_f32 v24, v128, v129\n"
    "v_cvt_pk_bf16_f32 v25, v130, v131\n"
    "v_mfma_f32_16x16x32_bf16 a[232:235], v[216:219], a[120:123], a[232:235]\n"
    "v_cvt_pk_bf16_f32 v26, v144, v145\n"
    "v_cvt_pk_bf16_f32 v27, v146, v147\n"
    "v_mfma_f32_16x16x32_bf16 a[232:235], v[220:223], a[124:127], a[232:235]\n"
    "v_mul_f32 v132, v132, v193\n"
    "v_mul_f32 v133, v133, v193\n"
    "v_mfma_f32_16x16x32_bf16 a[236:239], v[216:219], a[176:179], a[236:239]\n"
    "buffer_load_dwordx4 v247, s[72:75], s50 offen offset:2048 lds\n"
    "v_mfma_f32_16x16x32_bf16 a[236:239], v[220:223], a[180:183], a[236:239]\n"
    "s_waitcnt lgkmcnt(4)\n"
    "v_mul_f32 v134, v134, v193\n"
    "v_mul_f32 v135, v135, v193\n"
    "v_mfma_f32_16x16x32_bf16 a[240:243], v[224:227], a[8:11], a[240:243]\n"
    "v_mul_f32 v148, v148, v193\n"
    "v_mul_f32 v149, v149, v193\n"
    "v_mfma_f32_16x16x32_bf16 a[240:243], v[228:231], a[12:15], a[240:243]\n"
    "s_nop 0\n"
    "ds_read_b64_tr_b16 v[216:217], v254 offset:45056\n"
    "ds_read_b64_tr_b16 v[218:219], v254 offset:45312\n"
    "v_mfma_f32_16x16x32_bf16 a[244:247], v[224:227], a[64:67], a[244:247]\n"
    "s_nop 0\n"
    "ds_read_b64_tr_b16 v[220:221], v254 offset:49152\n"
    "ds_read_b64_tr_b16 v[222:223], v254 offset:49408\n"
    "v_mfma_f32_16x16x32_bf16 a[244:247], v[228:231], a[68:71], a[244:247]\n"
    "v_mul_f32 v150, v150, v193\n"
    "v_mul_f32 v151, v151, v193\n"
    "v_mfma_f32_16x16x32_bf16 a[248:251], v[224:227], a[120:123], a[248:251]\n"
    "buffer_load_dwordx4 v247, s[72:75], s51 offen offset:3072 lds\n"
    "v_mfma_f32_16x16x32_bf16 a[248:251], v[228:231], a[124:127], a[248:251]\n"
    "s_add_u32 m0, s52, 32768\n"
    "v_cvt_pk_bf16_f32 v56, v132, v133\n"
    "v_cvt_pk_bf16_f32 v57, v134, v135\n"
    "v_mfma_f32_16x16x32_bf16 a[252:255], v[224:227], a[176:179], a[252:255]\n"
    "v_cvt_pk_bf16_f32 v58, v148, v149\n"
    "v_cvt_pk_bf16_f32 v59, v150, v151\n"
    "v_mfma_f32_16x16x32_bf16 a[252:255], v[228:231], a[180:183], a[252:255]\n"
    "s_waitcnt lgkmcnt(4)\n"
    "v_permlane32_swap_b32 v24, v26\n"
    "v_permlane32_swap_b32 v25, v27\n"
    "v_mfma_f32_16x16x32_bf16 a[224:227], v[208:211], a[16:19], a[224:227]\n"
    "v_permlane32_swap_b32 v56, v58\n"
    "v_permlane32_swap_b32 v57, v59\n"
    "v_mfma_f32_16x16x32_bf16 a[224:227], v[212:215], a[20:23], a[224:227]\n"
    "s_nop 0\n"
    "ds_read_b64_tr_b16 v[224:225], v253 offset:53248\n"
    "ds_read_b64_tr_b16 v[226:227], v253 offset:53504\n"
    "v_mfma_f32_16x16x32_bf16 a[228:231], v[208:211], a[72:75], a[228:231]\n"
    "s_nop 0\n"
    "ds_read_b64_tr_b16 v[228:229], v254 offset:53248\n"
    "ds_read_b64_tr_b16 v[230:231], v254 offset:53504\n"
    "v_mfma_f32_16x16x32_bf16 a[228:231], v[212:215], a[76:79], a[228:231]\n"
    "v_permlane16_swap_b32 v24, v26\n"
    "v_permlane16_swap_b32 v25, v27\n"
    "v_mfma_f32_16x16x32_bf16 a[232:235], v[208:211], a[128:131], a[232:235]\n"
    "v_permlane16_swap_b32 v56, v58\n"
    "v_permlane16_swap_b32 v57, v59\n"
    "v_mfma_f32_16x16x32_bf16 a[232:235], v[212:215], a[132:135], a[232:235]\n"
    "ds_write_b128 v204, v[24:27] offset:0\n"
    "v_mfma_f32_16x16x32_bf16 a[236:239], v[208:211], a[184:187], a[236:239]\n"
    "ds_write_b128 v204, v[56:59] offset:1024\n"
    "v_mfma_f32_16x16x32_bf16 a[236:239], v[212:215], a[188:191], a[236:239]\n"
    "s_waitcnt lgkmcnt(6)\n"
    "v_mul_f32 v136, v136, v194\n"
    "v_mul_f32 v137, v137, v194\n"
    "v_mfma_f32_16x16x32_bf16 a[240:243], v[216:219], a[16:19], a[240:243]\n"
    "s_waitcnt lgkmcnt(2) vmcnt(12)\n"
    "s_barrier\n"
    "v_mul_f32 v138, v138, v194\n"
    "v_mul_f32 v139, v139, v194\n"
    "v_mfma_f32_16x16x32_bf16 a[240:243], v[220:223], a[20:23], a[240:243]\n"
    "s_nop 0\n"
    "ds_read_b64_tr_b16 v[208:209], v251 offset:0\n"
    "ds_read_b64_tr_b16 v[210:211], v251 offset:256\n"
    "v_mfma_f32_16x16x32_bf16 a[244:247], v[216:219], a[72:75], a[244:247]\n"
    "s_nop 0\n"
    "ds_read_b64_tr_b16 v[212:213], v251 offset:4096\n"
    "ds_read_b64_tr_b16 v[214:215], v251 offset:4352\n"
    "v_mfma_f32_16x16x32_bf16 a[244:247], v[220:223], a[76:79], a[244:247]\n"
    "v_mul_f32 v152, v152, v194\n"
    "v_mul_f32 v153, v153, v194\n"
    "v_mfma_f32_16x16x32_bf16 a[248:251], v[216:219], a[128:131], a[248:251]\n"
    "v_mul_f32 v154, v154, v194\n"
    "v_mul_f32 v155, v155, v194\n"
    "v_mfma_f32_16x16x32_bf16 a[248:251], v[220:223], a[132:135], a[248:251]\n"
    "v_cvt_pk_bf16_f32 v88, v136, v137\n"
    "v_cvt_pk_bf16_f32 v89, v138, v139\n"
    "v_mfma_f32_16x16x32_bf16 a[252:255], v[216:219], a[184:187], a[252:255]\n"
    "ds_read_b128 v[20:23], v205 offset:34816\n"
    "v_cvt_pk_bf16_f32 v90, v152, v153\n"
    "v_mfma_f32_16x16x32_bf16 a[252:255], v[220:223], a[188:191], a[252:255]\n"
    "s_nop 0\n"
    "v_cvt_pk_bf16_f32 v91, v154, v155\n"
    "v_mul_f32 v140, v140, v195\n"
    "v_mfma_f32_16x16x32_bf16 a[224:227], v[224:227], a[24:27], a[224:227]\n"
    "ds_read_b128 v[52:55], v205 offset:35840\n"
    "v_mul_f32 v141, v141, v195\n"
    "v_mfma_f32_16x16x32_bf16 a[240:243], v[228:231], a[24:27], a[240:243]\n"
    "s_nop 0\n"
    "ds_read_b64_tr_b16 v[216:217], v252 offset:0\n"
    "ds_read_b64_tr_b16 v[218:219], v252 offset:256\n"
    "v_mfma_f32_16x16x32_bf16 a[228:231], v[224:227], a[80:83], a[228:231]\n"
    "s_nop 0\n"
    "ds_read_b64_tr_b16 v[220:221], v252 offset:4096\n"
    "ds_read_b64_tr_b16 v[222:223], v252 offset:4352\n"
    "v_mfma_f32_16x16x32_bf16 a[244:247], v[228:231], a[80:83], a[244:247]\n"
    "ds_read_b128 v[84:87], v205 offset:32768\n"
    "v_mul_f32 v142, v142, v195\n"
    "v_mfma_f32_16x16x32_bf16 a[232:235], v[224:227], a[136:139], a[232:235]\n"
    "ds_read_b128 v[116:119], v205 offset:33792\n"
    "v_mul_f32 v143, v143, v195\n"
    "v_mfma_f32_16x16x32_bf16 a[248:251], v[228:231], a[136:139], a[248:251]\n"
    "v_mul_f32 v156, v156, v195\n"
    "v_mul_f32 v157, v157, v195\n"
    "v_mfma_f32_16x16x32_bf16 a[236:239], v[224:227], a[192:195], a[236:239]\n"
    "v_mul_f32 v158, v158, v195\n"
    "v_mul_f32 v159, v159, v195\n"
    "v_mfma_f32_16x16x32_bf16 a[252:255], v[228:231], a[192:195], a[252:255]\n"
    "s_waitcnt lgkmcnt(8)\n"
    "v_cvt_pk_bf16_f32 v120, v140, v141\n"
    "v_cvt_pk_bf16_f32 v121, v142, v143\n"
    "v_mfma_f32_16x16x32_bf16 a[224:227], v[208:211], a[28:31], a[224:227]\n"
    "v_cvt_pk_bf16_f32 v122, v156, v157\n"
    "v_cvt_pk_bf16_f32 v123, v158, v159\n"
    "v_mfma_f32_16x16x32_bf16 a[224:227], v[212:215], a[32:35], a[224:227]\n"
    "s_nop 0\n"
    "ds_read_b64_tr_b16 v[224:225], v251 offset:8192\n"
    "ds_read_b64_tr_b16 v[226:227], v251 offset:8448\n"
    "v_mfma_f32_16x16x32_bf16 a[228:231], v[208:211], a[84:87], a[228:231]\n"
    "s_cmp_lt_u32 s28, 12\n"
    "s_cselect_b32 s86, s86, 0\n"
    "ds_read_b64_tr_b16 v[228:229], v251 offset:12288\n"
    "ds_read_b64_tr_b16 v[230:231], v251 offset:12544\n"
    "v_mfma_f32_16x16x32_bf16 a[228:231], v[212:215], a[88:91], a[228:231]\n"
    "v_permlane32_swap_b32 v88, v90\n"
    "v_permlane32_swap_b32 v89, v91\n"
    "v_mfma_f32_16x16x32_bf16 a[232:235], v[208:211], a[140:143], a[232:235]\n"
    "v_permlane32_swap_b32 v120, v122\n"
    "v_permlane32_swap_b32 v121, v123\n"
    "v_mfma_f32_16x16x32_bf16 a[232:235], v[212:215], a[144:147], a[232:235]\n"
    "v_permlane16_swap_b32 v88, v90\n"
    "v_permlane16_swap_b32 v89, v91\n"
    "v_mfma_f32_16x16x32_bf16 a[236:239], v[208:211], a[196:199], a[236:239]\n"
    "v_permlane16_swap_b32 v120, v122\n"
    "v_permlane16_swap_b32 v121, v123\n"
    "v_mfma_f32_16x16x32_bf16 a[236:239], v[212:215], a[200:203], a[236:239]\n"
    "s_waitcnt lgkmcnt(6)\n"
    "ds_write_b128 v204, v[88:91] offset:2048\n"
    "v_mfma_f32_16x16x32_bf16 a[240:243], v[216:219], a[28:31], a[240:243]\n"
    "ds_write_b128 v204, v[120:123] offset:3072\n"
    "v_mfma_f32_16x16x32_bf16 a[240:243], v[220:223], a[32:35], a[240:243]\n"
    "s_nop 0\n"
    "ds_read_b64_tr_b16 v[208:209], v252 offset:8192\n"
    "ds_read_b64_tr_b16 v[210:211], v252 offset:8448\n"
    "v_mfma_f32_16x16x32_bf16 a[244:247], v[216:219], a[84:87], a[244:247]\n"
    "s_nop 0\n"
    "ds_read_b64_tr_b16 v[212:213], v252 offset:12288\n"
    "ds_read_b64_tr_b16 v[214:215], v252 offset:12544\n"
    "v_mfma_f32_16x16x32_bf16 a[244:247], v[220:223], a[88:91], a[244:247]\n"
    "v_mfma_f32_16x16x32_bf16 a[248:251], v[216:219], a[140:143], a[248:251]\n"
    "v_mfma_f32_16x16x32_bf16 a[248:251], v[220:223], a[144:147], a[248:251]\n"
    "v_mfma_f32_16x16x32_bf16 a[252:255], v[216:219], a[196:199], a[252:255]\n"
    "v_mfma_f32_16x16x32_bf16 a[252:255], v[220:223], a[200:203], a[252:255]\n"
    "s_waitcnt lgkmcnt(6)\n"
    "v_mfma_f32_16x16x32_bf16 a[224:227], v[224:227], a[36:39], a[224:227]\n"
    "v_mfma_f32_16x16x32_bf16 a[224:227], v[228:231], a[40:43], a[224:227]\n"
    "s_nop 0\n"
    "ds_read_b64_tr_b16 v[216:217], v251 offset:16384\n"
    "ds_read_b64_tr_b16 v[218:219], v251 offset:16640\n"
    "v_mfma_f32_16x16x32_bf16 a[228:231], v[224:227], a[92:95], a[228:231]\n"
    "s_nop 0\n"
    "ds_read_b64_tr_b16 v[220:221], v251 offset:20480\n"
    "ds_read_b64_tr_b16 v[222:223], v251 offset:20736\n"
    "v_mfma_f32_16x16x32_bf16 a[228:231], v[228:231], a[96:99], a[228:231]\n"
    "v_mfma_f32_16x16x32_bf16 a[232:235], v[224:227], a[148:151], a[232:235]\n"
    "v_mfma_f32_16x16x32_bf16 a[232:235], v[228:231], a[152:155], a[232:235]\n"
    "v_mfma_f32_16x16x32_bf16 a[236:239], v[224:227], a[204:207], a[236:239]\n"
    "v_mfma_f32_16x16x32_bf16 a[236:239], v[228:231], a[208:211], a[236:239]\n"
    "s_waitcnt lgkmcnt(4)\n"
    "v_mfma_f32_16x16x32_bf16 a[240:243], v[208:211], a[36:39], a[240:243]\n"
    "v_mfma_f32_16x16x32_bf16 a[240:243], v[212:215], a[40:43], a[240:243]\n"
    "s_nop 0\n"
    "ds_read_b64_tr_b16 v[224:225], v252 offset:16384\n"
    "ds_read_b64_tr_b16 v[226:227], v252 offset:16640\n"
    "v_mfma_f32_16x16x32_bf16 a[244:247], v[208:211], a[92:95], a[244:247]\n"
    "s_nop 0\n"
    "ds_read_b64_tr_b16 v[228:229], v252 offset:20480\n"
    "ds_read_b64_tr_b16 v[230:231], v252 offset:20736\n"
    "v_mfma_f32_16x16x32_bf16 a[244:247], v[212:215], a[96:99], a[244:247]\n"
    "v_mfma_f32_16x16x32_bf16 a[248:251], v[208:211], a[148:151], a[248:251]\n"
    "v_mfma_f32_16x16x32_bf16 a[248:251], v[212:215], a[152:155], a[248:251]\n"
    "v_mfma_f32_16x16x32_bf16 a[252:255], v[208:211], a[204:207], a[252:255]\n"
    "v_mfma_f32_16x16x32_bf16 a[252:255], v[212:215], a[208:211], a[252:255]\n"
    "s_waitcnt lgkmcnt(4)\n"
    "v_mfma_f32_16x16x32_bf16 a[224:227], v[216:219], a[44:47], a[224:227]\n"
    "v_mfma_f32_16x16x32_bf16 a[224:227], v[220:223], a[48:51], a[224:227]\n"
    "s_nop 0\n"
    "ds_read_b64_tr_b16 v[208:209], v251 offset:24576\n"
    "ds_read_b64_tr_b16 v[210:211], v251 offset:24832\n"
    "v_mfma_f32_16x16x32_bf16 a[228:231], v[216:219], a[100:103], a[228:231]\n"
    "s_nop 0\n"
    "ds_read_b64_tr_b16 v[212:213], v252 offset:24576\n"
    "ds_read_b64_tr_b16 v[214:215], v252 offset:24832\n"
    "v_mfma_f32_16x16x32_bf16 a[228:231], v[220:223], a[104:107], a[228:231]\n"
    "v_mfma_f32_16x16x32_bf16 a[232:235], v[216:219], a[156:159], a[232:235]\n"
    "v_mfma_f32_16x16x32_bf16 a[232:235], v[220:223], a[160:163], a[232:235]\n"
    "v_mfma_f32_16x16x32_bf16 a[236:239], v[216:219], a[212:215], a[236:239]\n"
    "v_mfma_f32_16x16x32_bf16 a[236:239], v[220:223], a[216:219], a[236:239]\n"
    "s_waitcnt lgkmcnt(4)\n"
    "v_mfma_f32_16x16x32_bf16 a[240:243], v[224:227], a[44:47], a[240:243]\n"
    "s_waitcnt lgkmcnt(0) vmcnt(0)\n"
    "s_barrier\n"
    "v_mfma_f32_16x16x32_bf16 a[240:243], v[228:231], a[48:51], a[240:243]\n"
    "s_nop 0\n"
    "ds_read_b128 v[216:219], v244 offset:0\n"
    "ds_read_b128 v[220:223], v248 offset:512\n"
    "v_mfma_f32_16x16x32_bf16 a[244:247], v[224:227], a[100:103], a[244:247]\n"
    "buffer_load_dwordx4 v247, s[36:39], s44 offen offset:0 lds\n"
    "v_mfma_f32_16x16x32_bf16 a[244:247], v[228:231], a[104:107], a[244:247]\n"
    "v_mfma_f32_16x16x32_bf16 a[248:251], v[224:227], a[156:159], a[248:251]\n"
    "buffer_load_dwordx4 v247, s[36:39], s45 offen offset:1024 lds\n"
    "v_mfma_f32_16x16x32_bf16 a[248:251], v[228:231], a[160:163], a[248:251]\n"
    "v_mfma_f32_16x16x32_bf16 a[252:255], v[224:227], a[212:215], a[252:255]\n"
    "ds_read_b128 v[28:31], v207 offset:2048\n"
    "v_mfma_f32_16x16x32_bf16 a[252:255], v[228:231], a[216:219], a[252:255]\n"
    "s_nop 0\n"
    "v_mfma_f32_16x16x32_bf16 a[224:227], v[208:211], a[52:55], a[224:227]\n"
    "ds_read_b128 v[60:63], v207 offset:3072\n"
    "v_mfma_f32_16x16x32_bf16 a[240:243], v[212:215], a[52:55], a[240:243]\n"
    "s_nop 0\n"
    "ds_read_b128 v[224:227], v244 offset:1024\n"
    "ds_read_b128 v[228:231], v248 offset:1536\n"
    "v_mfma_f32_16x16x32_bf16 a[228:231], v[208:211], a[108:111], a[228:231]\n"
    "ds_read_b128 v[92:95], v207 offset:0\n"
    "v_mfma_f32_16x16x32_bf16 a[244:247], v[212:215], a[108:111], a[244:247]\n"
    "buffer_load_dwordx4 v247, s[36:39], s46 offen offset:2048 lds\n"
    "v_mfma_f32_16x16x32_bf16 a[232:235], v[208:211], a[164:167], a[232:235]\n"
    "ds_read_b128 v[124:127], v207 offset:1024\n"
    "v_mfma_f32_16x16x32_bf16 a[248:251], v[212:215], a[164:167], a[248:251]\n"
    "buffer_load_dwordx4 v247, s[36:39], s47 offen offset:3072 lds\n"
    "v_mfma_f32_16x16x32_bf16 a[236:239], v[208:211], a[220:223], a[236:239]\n"
    "s_add_u32 m0, s52, 36864\n"
    "v_mfma_f32_16x16x32_bf16 a[252:255], v[212:215], a[220:223], a[252:255]\n"
    "s_waitcnt lgkmcnt(6)\n"
    "v_mfma_f32_16x16x32_bf16 v[128:131], v[216:219], v[0:3], 0\n"
    "buffer_load_dwordx4 v247, s[36:39], s48 offen offset:0 lds\n"
    "v_mfma_f32_16x16x32_bf16 v[128:131], v[220:223], v[4:7], v[128:131]\n"
    "s_nop 0\n"
    "ds_read_b128 v[208:211], v244 offset:2048\n"
    "ds_read_b128 v[212:215], v248 offset:2560\n"
    "v_mfma_f32_16x16x32_bf16 v[132:135], v[216:219], v[32:35], 0\n"
    "v_mfma_f32_16x16x32_bf16 v[132:135], v[220:223], v[36:39], v[132:135]\n"
    "v_mfma_f32_16x16x32_bf16 v[136:139], v[216:219], v[64:67], 0\n"
    "buffer_load_dwordx4 v247, s[36:39], s49 offen offset:1024 lds\n"
    "v_mfma_f32_16x16x32_bf16 v[136:139], v[220:223], v[68:71], v[136:139]\n"
    "v_mfma_f32_16x16x32_bf16 v[140:143], v[216:219], v[96:99], 0\n"
    "v_mfma_f32_16x16x32_bf16 v[140:143], v[220:223], v[100:103], v[140:143]\n"
    "s_waitcnt lgkmcnt(4)\n"
    "v_mfma_f32_16x16x32_bf16 v[128:131], v[224:227], v[8:11], v[128:131]\n"
    "v_add_u32 v255, 32768, v206\n"
    "v_accvgpr_read_b32 v240, a224\n"
    "v_mfma_f32_16x16x32_bf16 v[128:131], v[228:231], v[12:15], v[128:131]\n"
    "s_nop 0\n"
    "ds_read_b128 v[216:219], v244 offset:3072\n"
    "ds_read_b128 v[220:223], v248 offset:3584\n"
    "v_mfma_f32_16x16x32_bf16 v[132:135], v[224:227], v[40:43], v[132:135]\n"
    "buffer_load_dwordx4 v247, s[36:39], s50 offen offset:2048 lds\n"
    "v_mfma_f32_16x16x32_bf16 v[132:135], v[228:231], v[44:47], v[132:135]\n"
    "v_accvgpr_read_b32 v241, a225\n"
    "v_accvgpr_read_b32 v242, a226\n"
    "v_mfma_f32_16x16x32_bf16 v[136:139], v[224:227], v[72:75], v[136:139]\n"
    "s_waitcnt lgkmcnt(4)\n"
    "s_barrier\n"
    "v_accvgpr_read_b32 v243, a227\n"
    "v_mul_f32 v160, v160, v240\n"
    "v_mfma_f32_16x16x32_bf16 v[136:139], v[228:231], v[76:79], v[136:139]\n"
    "v_mul_f32 v161, v161, v241\n"
    "v_mul_f32 v162, v162, v242\n"
    "v_mfma_f32_16x16x32_bf16 v[140:143], v[224:227], v[104:107], v[140:143]\n"
    "buffer_load_dwordx4 v247, s[36:39], s51 offen offset:3072 lds\n"
    "v_mfma_f32_16x16x32_bf16 v[140:143], v[228:231], v[108:111], v[140:143]\n"
    "s_waitcnt lgkmcnt(2)\n"
    "v_mul_f32 v163, v163, v243\n"
    "v_accvgpr_read_b32 v240, a240\n"
    "v_mfma_f32_16x16x32_bf16 v[128:131], v[208:211], v[16:19], v[128:131]\n"
    "s_add_u32 m0, s53, 0\n"
    "v_accvgpr_read_b32 v241, a241\n"
    "v_accvgpr_read_b32 v242, a242\n"
    "v_mfma_f32_16x16x32_bf16 v[128:131], v[212:215], v[20:23], v[128:131]\n"
    "s_nop 0\n"
    "ds_read_b128 v[224:227], v244 offset:8192\n"
    "ds_read_b128 v[228:231], v248 offset:8704\n"
    "v_mfma_f32_16x16x32_bf16 v[132:135], v[208:211], v[48:51], v[132:135]\n"
    "v_accvgpr_read_b32 v243, a243\n"
    "v_mul_f32 v176, v176, v240\n"
    "v_mfma_f32_16x16x32_bf16 v[132:135], v[212:215], v[52:55], v[132:135]\n"
    "v_mul_f32 v177, v177, v241\n"
    "v_mul_f32 v178, v178, v242\n"
    "v_mfma_f32_16x16x32_bf16 v[136:139], v[208:211], v[80:83], v[136:139]\n"
    "buffer_load_dwordx4 v247, s[40:43], s44 offen offset:0 lds\n"
    "v_mfma_f32_16x16x32_bf16 v[136:139], v[212:215], v[84:87], v[136:139]\n"
    "v_mul_f32 v179, v179, v243\n"
    "v_cvt_pk_bf16_f32 v179, v178, v179\n"
    "v_mfma_f32_16x16x32_bf16 v[140:143], v[208:211], v[112:115], v[140:143]\n"
    "v_cvt_pk_bf16_f32 v178, v176, v177\n"
    "v_cvt_pk_bf16_f32 v176, v160, v161\n"
    "v_mfma_f32_16x16x32_bf16 v[140:143], v[212:215], v[116:119], v[140:143]\n"
    "s_waitcnt lgkmcnt(2)\n"
    "v_cvt_pk_bf16_f32 v177, v162, v163\n"
    "v_accvgpr_read_b32 v240, a228\n"
    "v_mfma_f32_16x16x32_bf16 v[128:131], v[216:219], v[24:27], v[128:131]\n"
    "buffer_load_dwordx4 v247, s[40:43], s45 offen offset:1024 lds\n"
    "v_mfma_f32_16x16x32_bf16 v[128:131], v[220:223], v[28:31], v[128:131]\n"
    "s_nop 0\n"
    "ds_read_b128 v[208:211], v244 offset:9216\n"
    "ds_read_b128 v[212:215], v248 offset:9728\n"
    "v_mfma_f32_16x16x32_bf16 v[132:135], v[216:219], v[56:59], v[132:135]\n"
    "v_accvgpr_read_b32 v241, a229\n"
    "v_accvgpr_read_b32 v242, a230\n"
    "v_mfma_f32_16x16x32_bf16 v[132:135], v[220:223], v[60:63], v[132:135]\n"
    "v_accvgpr_read_b32 v243, a231\n"
    "v_mul_f32 v164, v164, v240\n"
    "v_mfma_f32_16x16x32_bf16 v[136:139], v[216:219], v[88:91], v[136:139]\n"
    "buffer_load_dwordx4 v247, s[40:43], s46 offen offset:2048 lds\n"
    "v_mfma_f32_16x16x32_bf16 v[136:139], v[220:223], v[92:95], v[136:139]\n"
    "v_mul_f32 v165, v165, v241\n"
    "v_mul_f32 v166, v166, v242\n"
    "v_mfma_f32_16x16x32_bf16 v[140:143], v[216:219], v[120:123], v[140:143]\n"
    "v_mul_f32 v167, v167, v243\n"
    "v_accvgpr_read_b32 v240, a244\n"
    "v_mfma_f32_16x16x32_bf16 v[140:143], v[220:223], v[124:127], v[140:143]\n"
    "s_waitcnt lgkmcnt(2)\n"
    "v_accvgpr_read_b32 v241, a245\n"
    "v_accvgpr_read_b32 v242, a246\n"
    "v_mfma_f32_16x16x32_bf16 v[144:147], v[224:227], v[0:3], 0\n"
    "buffer_load_dwordx4 v247, s[40:43], s47 offen offset:3072 lds\n"
    "v_mfma_f32_16x16x32_bf16 v[144:147], v[228:231], v[4:7], v[144:147]\n"
    "s_add_u32 m0, s53, 4096\n"
    "ds_read_b128 v[216:219], v244 offset:10240\n"
    "ds_read_b128 v[220:223], v248 offset:10752\n"
    "v_mfma_f32_16x16x32_bf16 v[148:151], v[224:227], v[32:35], 0\n"
    "v_accvgpr_read_b32 v243, a247\n"
    "v_mul_f32 v180, v180, v240\n"
    "v_mfma_f32_16x16x32_bf16 v[148:151], v[228:231], v[36:39], v[148:151]\n"
    "v_mul_f32 v181, v181, v241\n"
    "v_mul_f32 v182, v182, v242\n"
    "v_mfma_f32_16x16x32_bf16 v[152:155], v[224:227], v[64:67], 0\n"
    "buffer_load_dwordx4 v247, s[40:43], s48 offen offset:0 lds\n"
    "v_mfma_f32_16x16x32_bf16 v[152:155], v[228:231], v[68:71], v[152:155]\n"
    "v_mul_f32 v183, v183, v243\n"
    "v_cvt_pk_bf16_f32 v183, v182, v183\n"
    "v_mfma_f32_16x16x32_bf16 v[156:159], v[224:227], v[96:99], 0\n"
    "v_cvt_pk_bf16_f32 v182, v180, v181\n"
    "v_cvt_pk_bf16_f32 v180, v164, v165\n"
    "v_mfma_f32_16x16x32_bf16 v[156:159], v[228:231], v[100:103], v[156:159]\n"
    "s_waitcnt lgkmcnt(2)\n"
    "v_cvt_pk_bf16_f32 v181, v166, v167\n"
    "v_accvgpr_read_b32 v240, a232\n"
    "v_mfma_f32_16x16x32_bf16 v[144:147], v[208:211], v[8:11], v[144:147]\n"
    "buffer_load_dwordx4 v247, s[40:43], s49 offen offset:1024 lds\n"
    "v_mfma_f32_16x16x32_bf16 v[144:147], v[212:215], v[12:15], v[144:147]\n"
    "s_nop 0\n"
    "ds_read_b128 v[224:227], v244 offset:11264\n"
    "ds_read_b128 v[228:231], v248 offset:11776\n"
    "v_mfma_f32_16x16x32_bf16 v[148:151], v[208:211], v[40:43], v[148:151]\n"
    "v_accvgpr_read_b32 v241, a233\n"
    "v_accvgpr_read_b32 v242, a234\n"
    "v_mfma_f32_16x16x32_bf16 v[148:151], v[212:215], v[44:47], v[148:151]\n"
    "v_accvgpr_read_b32 v243, a235\n"
    "v_mul_f32 v168, v168, v240\n"
    "v_mfma_f32_16x16x32_bf16 v[152:155], v[208:211], v[72:75], v[152:155]\n"
    "buffer_load_dwordx4 v247, s[40:43], s50 offen offset:2048 lds\n"
    "v_mfma_f32_16x16x32_bf16 v[152:155], v[212:215], v[76:79], v[152:155]\n"
    "v_mul_f32 v169, v169, v241\n"
    "v_mul_f32 v170, v170, v242\n"
    "v_mfma_f32_16x16x32_bf16 v[156:159], v[208:211], v[104:107], v[156:159]\n"
    "v_mul_f32 v171, v171, v243\n"
    "v_accvgpr_read_b32 v240, a248\n"
    "v_mfma_f32_16x16x32_bf16 v[156:159], v[212:215], v[108:111], v[156:159]\n"
    "s_waitcnt lgkmcnt(2)\n"
    "v_accvgpr_read_b32 v241, a249\n"
    "v_accvgpr_read_b32 v242, a250\n"
    "v_mfma_f32_16x16x32_bf16 v[144:147], v[216:219], v[16:19], v[144:147]\n"
    "buffer_load_dwordx4 v247, s[40:43], s51 offen offset:3072 lds\n"
    "v_mfma_f32_16x16x32_bf16 v[144:147], v[220:223], v[20:23], v[144:147]\n"
    "v_accvgpr_read_b32 v243, a251\n"
    "v_mul_f32 v184, v184, v240\n"
    "v_mfma_f32_16x16x32_bf16 v[148:151], v[216:219], v[48:51], v[148:151]\n"
    "v_mul_f32 v185, v185, v241\n"
    "v_mul_f32 v186, v186, v242\n"
    "v_mfma_f32_16x16x32_bf16 v[148:151], v[220:223], v[52:55], v[148:151]\n"
    "v_mul_f32 v187, v187, v243\n"
    "v_cvt_pk_bf16_f32 v187, v186, v187\n"
    "v_mfma_f32_16x16x32_bf16 v[152:155], v[216:219], v[80:83], v[152:155]\n"
    "v_cvt_pk_bf16_f32 v186, v184, v185\n"
    "v_cvt_pk_bf16_f32 v184, v168, v169\n"
    "v_mfma_f32_16x16x32_bf16 v[152:155], v[220:223], v[84:87], v[152:155]\n"
    "v_cvt_pk_bf16_f32 v185, v170, v171\n"
    "v_accvgpr_read_b32 v240, a236\n"
    "v_mfma_f32_16x16x32_bf16 v[156:159], v[216:219], v[112:115], v[156:159]\n"
    "v_accvgpr_read_b32 v241, a237\n"
    "v_accvgpr_read_b32 v242, a238\n"
    "v_mfma_f32_16x16x32_bf16 v[156:159], v[220:223], v[116:119], v[156:159]\n"
    "s_waitcnt lgkmcnt(0)\n"
    "v_accvgpr_read_b32 v243, a239\n"
    "v_mul_f32 v172, v172, v240\n"
    "v_mfma_f32_16x16x32_bf16 v[144:147], v[224:227], v[24:27], v[144:147]\n"
    "v_mul_f32 v173, v173, v241\n"
    "v_mul_f32 v174, v174, v242\n"
    "v_mfma_f32_16x16x32_bf16 v[144:147], v[228:231], v[28:31], v[144:147]\n"
    "v_mul_f32 v175, v175, v243\n"
    "v_accvgpr_read_b32 v240, a252\n"
    "v_mfma_f32_16x16x32_bf16 v[148:151], v[224:227], v[56:59], v[148:151]\n"
    "v_accvgpr_read_b32 v241, a253\n"
    "v_accvgpr_read_b32 v242, a254\n"
    "v_mfma_f32_16x16x32_bf16 v[148:151], v[228:231], v[60:63], v[148:151]\n"
    "v_accvgpr_read_b32 v243, a255\n"
    "v_mul_f32 v188, v188, v240\n"
    "v_mfma_f32_16x16x32_bf16 v[152:155], v[224:227], v[88:91], v[152:155]\n"
    "v_mul_f32 v189, v189, v241\n"
    "v_mul_f32 v190, v190, v242\n"
    "v_mfma_f32_16x16x32_bf16 v[152:155], v[228:231], v[92:95], v[152:155]\n"
    "v_mul_f32 v191, v191, v243\n"
    "v_cvt_pk_bf16_f32 v191, v190, v191\n"
    "v_mfma_f32_16x16x32_bf16 v[156:159], v[224:227], v[120:123], v[156:159]\n"
    "v_cvt_pk_bf16_f32 v190, v188, v189\n"
    "v_cvt_pk_bf16_f32 v188, v172, v173\n"
    "v_mfma_f32_16x16x32_bf16 v[156:159], v[228:231], v[124:127], v[156:159]\n"
    "v_cvt_pk_bf16_f32 v189, v174, v175\n"
    "s_nop 7\n"
    "s_mov_b32 s54, 14\n"
    "buffer_store_dwordx4 v[176:179], v255, s[84:87], s92 offen offset:0 sc1\n"
    "buffer_store_dwordx4 v[180:183], v255, s[84:87], s93 offen offset:0 sc1\n"
    "buffer_store_dwordx4 v[184:187], v255, s[84:87], s94 offen offset:0 sc1\n"
    "buffer_store_dwordx4 v[188:191], v255, s[84:87], s95 offen offset:0 sc1\n"
    "s_nop 5\n"
    "v_max3_f32 v249, v128, v129, v130\n"
    "v_max3_f32 v250, v132, v133, v134\n"
    "v_max3_f32 v251, v136, v137, v138\n"
    "v_max3_f32 v252, v140, v141, v142\n"
    "v_max3_f32 v249, v249, v131, v144\n"
    "v_max3_f32 v250, v250, v135, v148\n"
    "v_max3_f32 v251, v251, v139, v152\n"
    "v_max3_f32 v252, v252, v143, v156\n"
    "v_max3_f32 v249, v249, v145, v146\n"
    "v_max3_f32 v250, v250, v149, v150\n"
    "v_max3_f32 v251, v251, v153, v154\n"
    "v_max3_f32 v252, v252, v157, v158\n"
    "v_max_f32 v249, v249, v147\n"
    "v_max_f32 v250, v250, v151\n"
    "v_max_f32 v251, v251, v155\n"
    "v_max_f32 v252, v252, v159\n"
    "v_mov_b32 v232, v249\n"
    "v_mov_b32 v233, v250\n"
    "v_mov_b32 v234, v251\n"
    "v_mov_b32 v235, v252\n"
    "v_permlane32_swap_b32 v249, v232\n"
    "v_permlane32_swap_b32 v250, v233\n"
    "v_permlane32_swap_b32 v251, v234\n"
    "v_permlane32_swap_b32 v252, v235\n"
    "v_max_f32 v249, v249, v232\n"
    "v_max_f32 v250, v250, v233\n"
    "v_max_f32 v251, v251, v234\n"
    "v_max_f32 v252, v252, v235\n"
    "v_mov_b32 v232, v249\n"
    "v_mov_b32 v233, v250\n"
    "v_mov_b32 v234, v251\n"
    "v_mov_b32 v235, v252\n"
    "v_permlane16_swap_b32 v249, v232\n"
    "v_permlane16_swap_b32 v250, v233\n"
    "v_permlane16_swap_b32 v251, v234\n"
    "v_permlane16_swap_b32 v252, v235\n"
    "v_max_f32 v249, v249, v232\n"
    "v_max_f32 v250, v250, v233\n"
    "v_max_f32 v251, v251, v234\n"
    "v_max_f32 v252, v252, v235\n"
    "v_sub_f32 v192, 0, v249\n"
    "v_sub_f32 v193, 0, v249\n"
    "v_sub_f32 v194, 0, v249\n"
    "v_sub_f32 v195, 0, v249\n"
    "v_mov_b32 v240, 0\n"
    "v_sub_f32 v232, v128, v249\n"
    "v_sub_f32 v233, v129, v249\n"
    "v_sub_f32 v234, v130, v249\n"
    "v_sub_f32 v235, v131, v249\n"
    "v_exp_f32 v232, v232\n"
    "v_exp_f32 v233, v233\n"
    "v_exp_f32 v234, v234\n"
    "v_exp_f32 v235, v235\n"
    "s_nop 0\n"
    "v_add_f32 v236, v232, v233\n"
    "v_add_f32 v236, v236, v234\n"
    "v_add_f32 v236, v236, v235\n"
    "v_cvt_pk_bf16_f32 v160, v232, v233\n"
    "v_cvt_pk_bf16_f32 v161, v234, v235\n"
    "v_sub_f32 v232, v144, v249\n"
    "v_sub_f32 v233, v145, v249\n"
    "v_sub_f32 v234, v146, v249\n"
    "v_sub_f32 v235, v147, v249\n"
    "v_exp_f32 v232, v232\n"
    "v_exp_f32 v233, v233\n"
    "v_exp_f32 v234, v234\n"
    "v_exp_f32 v235, v235\n"
    "s_nop 0\n"
    "v_add_f32 v236, v236, v232\n"
    "v_add_f32 v236, v236, v233\n"
    "v_add_f32 v236, v236, v234\n"
    "v_add_f32 v236, v236, v235\n"
    "v_cvt_pk_bf16_f32 v162, v232, v233\n"
    "v_cvt_pk_bf16_f32 v163, v234, v235\n"
    "v_sub_f32 v196, 0, v250\n"
    "v_sub_f32 v197, 0, v250\n"
    "v_sub_f32 v198, 0, v250\n"
    "v_sub_f32 v199, 0, v250\n"
    "v_mov_b32 v241, 0\n"
    "v_sub_f32 v232, v132, v250\n"
    "v_sub_f32 v233, v133, v250\n"
    "v_sub_f32 v234, v134, v250\n"
    "v_sub_f32 v235, v135, v250\n"
    "v_exp_f32 v232, v232\n"
    "v_exp_f32 v233, v233\n"
    "v_exp_f32 v234, v234\n"
    "v_exp_f32 v235, v235\n"
    "s_nop 0\n"
    "v_add_f32 v237, v232, v233\n"
    "v_add_f32 v237, v237, v234\n"
    "v_add_f32 v237, v237, v235\n"
    "v_cvt_pk_bf16_f32 v164, v232, v233\n"
    "v_cvt_pk_bf16_f32 v165, v234, v235\n"
    "v_sub_f32 v232, v148, v250\n"
    "v_sub_f32 v233, v149, v250\n"
    "v_sub_f32 v234, v150, v250\n"
    "v_sub_f32 v235, v151, v250\n"
    "v_exp_f32 v232, v232\n"
    "v_exp_f32 v233, v233\n"
    "v_exp_f32 v234, v234\n"
    "v_exp_f32 v235, v235\n"
    "s_nop 0\n"
    "v_add_f32 v237, v237, v232\n"
    "v_add_f32 v237, v237, v233\n"
    "v_add_f32 v237, v237, v234\n"
    "v_add_f32 v237, v237, v235\n"
    "v_cvt_pk_bf16_f32 v166, v232, v233\n"
    "v_cvt_pk_bf16_f32 v167, v234, v235\n"
    "v_sub_f32 v200, 0, v251\n"
    "v_sub_f32 v201, 0, v251\n"
    "v_sub_f32 v202, 0, v251\n"
    "v_sub_f32 v203, 0, v251\n"
    "v_mov_b32 v242, 0\n"
    "v_sub_f32 v232, v136, v251\n"
    "v_sub_f32 v233, v137, v251\n"
    "v_sub_f32 v234, v138, v251\n"
    "v_sub_f32 v235, v139, v251\n"
    "v_exp_f32 v232, v232\n"
    "v_exp_f32 v233, v233\n"
    "v_exp_f32 v234, v234\n"
    "v_exp_f32 v235, v235\n"
    "s_nop 0\n"
    "v_add_f32 v238, v232, v233\n"
    "v_add_f32 v238, v238, v234\n"
    "v_add_f32 v238, v238, v235\n"
    "v_cvt_pk_bf16_f32 v168, v232, v233\n"
    "v_cvt_pk_bf16_f32 v169, v234, v235\n"
    "v_sub_f32 v232, v152, v251\n"
    "v_sub_f32 v233, v153, v251\n"
    "v_sub_f32 v234, v154, v251\n"
    "v_sub_f32 v235, v155, v251\n"
    "v_exp_f32 v232, v232\n"
    "v_exp_f32 v233, v233\n"
    "v_exp_f32 v234, v234\n"
    "v_exp_f32 v235, v235\n"
    "s_nop 0\n"
    "v_add_f32 v238, v238, v232\n"
    "v_add_f32 v238, v238, v233\n"
    "v_add_f32 v238, v238, v234\n"
    "v_add_f32 v238, v238, v235\n"
    "v_cvt_pk_bf16_f32 v170, v232, v233\n"
    "v_cvt_pk_bf16_f32 v171, v234, v235\n"
    "v_sub_f32 v204, 0, v252\n"
    "v_sub_f32 v205, 0, v252\n"
    "v_sub_f32 v206, 0, v252\n"
    "v_sub_f32 v207, 0, v252\n"
    "v_mov_b32 v243, 0\n"
    "v_sub_f32 v232, v140, v252\n"
    "v_sub_f32 v233, v141, v252\n"
    "v_sub_f32 v234, v142, v252\n"
    "v_sub_f32 v235, v143, v252\n"
    "v_exp_f32 v232, v232\n"
    "v_exp_f32 v233, v233\n"
    "v_exp_f32 v234, v234\n"
    "v_exp_f32 v235, v235\n"
    "s_nop 0\n"
    "v_add_f32 v239, v232, v233\n"
    "v_add_f32 v239, v239, v234\n"
    "v_add_f32 v239, v239, v235\n"
    "v_cvt_pk_bf16_f32 v172, v232, v233\n"
    "v_cvt_pk_bf16_f32 v173, v234, v235\n"
    "v_sub_f32 v232, v156, v252\n"
    "v_sub_f32 v233, v157, v252\n"
    "v_sub_f32 v234, v158, v252\n"
    "v_sub_f32 v235, v159, v252\n"
    "v_exp_f32 v232, v232\n"
    "v_exp_f32 v233, v233\n"
    "v_exp_f32 v234, v234\n"
    "v_exp_f32 v235, v235\n"
    "s_nop 0\n"
    "v_add_f32 v239, v239, v232\n"
    "v_add_f32 v239, v239, v233\n"
    "v_add_f32 v239, v239, v234\n"
    "v_add_f32 v239, v239, v235\n"
    "v_cvt_pk_bf16_f32 v174, v232, v233\n"
    "v_cvt_pk_bf16_f32 v175, v234, v235\n"
    "s_waitcnt vmcnt(4) lgkmcnt(0)\n"
    "s_barrier\n"
    "v_add_u32 v247, 32768, v247\n"
    "s_add_u32 m0, s52, 0\n"
    "s_nop 0\n"
    "ds_read_b128 v[208:211], v244 offset:32768\n"
    "ds_read_b128 v[212:215], v248 offset:33280\n"
    "buffer_load_dwordx4 v247, s[36:39], s44 offen offset:0 lds\n"
    "buffer_load_dwordx4 v247, s[36:39], s45 offen offset:1024 lds\n"
    "ds_read_b128 v[216:219], v244 offset:33792\n"
    "ds_read_b128 v[220:223], v248 offset:34304\n"
    "buffer_load_dwordx4 v247, s[36:39], s46 offen offset:2048 lds\n"
    "s_waitcnt lgkmcnt(2)\n"
    "v_mfma_f32_16x16x32_bf16 v[128:131], v[208:211], v[0:3], v[192:195]\n"
    "buffer_load_dwordx4 v247, s[36:39], s47 offen offset:3072 lds\n"
    "v_mfma_f32_16x16x32_bf16 v[128:131], v[212:215], v[4:7], v[128:131]\n"
    "s_add_u32 m0, s52, 4096\n"
    "ds_read_b128 v[224:227], v244 offset:34816\n"
    "ds_read_b128 v[228:231], v248 offset:35328\n"
    "v_mfma_f32_16x16x32_bf16 v[132:135], v[208:211], v[32:35], v[196:199]\n"
    "v_add_f32 v240, v240, v236\n"
    "v_add_f32 v241, v241, v237\n"
    "v_mfma_f32_16x16x32_bf16 v[132:135], v[212:215], v[36:39], v[132:135]\n"
    "v_add_f32 v242, v242, v238\n"
    "v_add_f32 v243, v243, v239\n"
    "v_mfma_f32_16x16x32_bf16 v[136:139], v[208:211], v[64:67], v[200:203]\n"
    "buffer_load_dwordx4 v247, s[36:39], s48 offen offset:0 lds\n"
    "v_mfma_f32_16x16x32_bf16 v[136:139], v[212:215], v[68:71], v[136:139]\n"
    "v_mbcnt_lo_u32_b32 v251, -1, 0\n"
    "v_mbcnt_hi_u32_b32 v251, -1, v251\n"
    "v_mfma_f32_16x16x32_bf16 v[140:143], v[208:211], v[96:99], v[204:207]\n"
    "s_mul_i32 s31, s28, 229376\n"
    "s_add_u32 s72, s16, s31\n"
    "s_addc_u32 s73, s17, 0\n"
    "v_mfma_f32_16x16x32_bf16 v[140:143], v[212:215], v[100:103], v[140:143]\n"
    "s_waitcnt lgkmcnt(2)\n"
    "s_and_b32 s73, s73, 65535\n"
    "s_mov_b32 s74, 229376\n"
    "v_mfma_f32_16x16x32_bf16 v[128:131], v[216:219], v[8:11], v[128:131]\n"
    "s_mov_b32 s75, 131072\n"
    "buffer_load_dwordx4 v247, s[36:39], s49 offen offset:1024 lds\n"
    "v_mfma_f32_16x16x32_bf16 v[128:131], v[220:223], v[12:15], v[128:131]\n"
    "s_mov_b32 s80, s22\n"
    "ds_read_b128 v[208:211], v244 offset:35840\n"
    "ds_read_b128 v[212:215], v248 offset:36352\n"
    "v_mfma_f32_16x16x32_bf16 v[132:135], v[216:219], v[40:43], v[132:135]\n"
    "s_and_b32 s81, s23, 65535\n"
    "s_mov_b32 s82, 1605632\n"
    "s_mov_b32 s83, 131072\n"
    "v_mfma_f32_16x16x32_bf16 v[132:135], v[220:223], v[44:47], v[132:135]\n"
    "s_getpc_b64 s[88:89]\n"
    "K1PC1:\n"
    "s_add_u32 s88, s88, K1TAIL-K1PC1\n"
    "s_addc_u32 s89, s89, 0\n"
    "v_mfma_f32_16x16x32_bf16 v[136:139], v[216:219], v[72:75], v[136:139]\n"
    "s_and_b32 s89, s89, 0xffff\n"
    "buffer_load_dwordx4 v247, s[36:39], s50 offen offset:2048 lds\n"
    "v_mfma_f32_16x16x32_bf16 v[136:139], v[220:223], v[76:79], v[136:139]\n"
    "s_mov_b32 s90, K1END-K1TAIL\n"
    "s_mov_b32 s91, 131072\n"
    "s_mov_b32 s90, 0\n"
    "v_mfma_f32_16x16x32_bf16 v[140:143], v[216:219], v[104:107], v[140:143]\n"
    "s_mov_b32 s18, s88\n"
    "s_mov_b32 s19, s89\n"
    "s_mov_b32 s99, 0\n"
    "v_mfma_f32_16x16x32_bf16 v[140:143], v[220:223], v[108:111], v[140:143]\n"
    "s_waitcnt lgkmcnt(2)\n"
    "s_lshl_b32 s58, s29, 2\n"
    "s_add_u32 s58, s58, s30\n"
    "v_mfma_f32_16x16x32_bf16 v[128:131], v[224:227], v[16:19], v[128:131]\n"
    "s_mul_i32 s96, s58, 3584\n"
    "buffer_load_dwordx4 v247, s[36:39], s51 offen offset:3072 lds\n"
    "v_mfma_f32_16x16x32_bf16 v[128:131], v[228:231], v[20:23], v[128:131]\n"
    "s_add_u32 m0, s53, 32768\n"
    "ds_read_b128 v[216:219], v244 offset:40960\n"
    "ds_read_b128 v[220:223], v248 offset:41472\n"
    "v_mfma_f32_16x16x32_bf16 v[132:135], v[224:227], v[48:51], v[132:135]\n"
    "s_lshr_b32 s58, %1, 3\n"
    "s_lshl_b32 s58, s58, 2\n"
    "s_add_u32 s58, s58, s30\n"
    "v_mfma_f32_16x16x32_bf16 v[132:135], v[228:231], v[52:55], v[132:135]\n"
    "s_mul_i32 s97, s58, 12544\n"
    "s_lshl_b32 s98, s58, 13\n"
    "v_lshlrev_b32 v254, 7, v251\n"
    "v_mfma_f32_16x16x32_bf16 v[136:139], v[224:227], v[80:83], v[136:139]\n"
    "s_mov_b32 s62, s8\n"
    "buffer_load_dwordx4 v247, s[40:43], s44 offen offset:0 lds\n"
    "v_mfma_f32_16x16x32_bf16 v[136:139], v[228:231], v[84:87], v[136:139]\n"
    "s_mov_b32 s63, s9\n"
    "s_lshr_b32 s9, %1, 3\n"
    "s_min_u32 s9, s9, 27\n"
    "v_mfma_f32_16x16x32_bf16 v[140:143], v[224:227], v[112:115], v[140:143]\n"
    "s_mul_i32 s31, s9, 37\n"
    "s_lshr_b32 s31, s31, 8\n"
    "s_mul_i32 s8, s31, 7\n"
    "v_mfma_f32_16x16x32_bf16 v[140:143], v[228:231], v[116:119], v[140:143]\n"
    "s_waitcnt lgkmcnt(2)\n"
    "s_sub_u32 s9, s9, s8\n"
    "s_and_b32 s8, %1, 7\n"
    "v_mfma_f32_16x16x32_bf16 v[128:131], v[208:211], v[24:27], v[128:131]\n"
    "s_lshl_b32 s8, s8, 2\n"
    "buffer_load_dwordx4 v247, s[40:43], s45 offen offset:1024 lds\n"
    "v_mfma_f32_16x16x32_bf16 v[128:131], v[212:215], v[28:31], v[128:131]\n"
    "s_add_u32 s8, s8, s31\n"
    "ds_read_b128 v[224:227], v244 offset:41984\n"
    "ds_read_b128 v[228:231], v248 offset:42496\n"
    "v_mfma_f32_16x16x32_bf16 v[132:135], v[208:211], v[56:59], v[132:135]\n"
    "s_lshr_b32 s61, s8, 1\n"
    "s_mul_i32 s31, s30, 401408\n"
    "s_lshl_b32 s57, s9, 7\n"
    "v_mfma_f32_16x16x32_bf16 v[132:135], v[212:215], v[60:63], v[132:135]\n"
    "s_add_u32 s31, s31, s57\n"
    "s_add_u32 s76, s22, s31\n"
    "s_addc_u32 s77, s23, 0\n"
    "v_mfma_f32_16x16x32_bf16 v[136:139], v[208:211], v[88:91], v[136:139]\n"
    "s_and_b32 s77, s77, 65535\n"
    "buffer_load_dwordx4 v247, s[40:43], s46 offen offset:2048 lds\n"
    "v_mfma_f32_16x16x32_bf16 v[136:139], v[212:215], v[92:95], v[136:139]\n"
    "s_mov_b32 s79, 131072\n"
    "s_sub_u32 s78, 401408, s57\n"
    "v_mfma_f32_16x16x32_bf16 v[140:143], v[208:211], v[120:123], v[140:143]\n"
    "s_cmp_lt_u32 %1, 224\n"
    "s_cselect_b32 s78, s78, 0\n"
    "v_mfma_f32_16x16x32_bf16 v[140:143], v[212:215], v[124:127], v[140:143]\n"
    "s_waitcnt lgkmcnt(2)\n"
    "buffer_load_dwordx4 v247, s[40:43], s47 offen offset:3072 lds\n"
    "v_mfma_f32_16x16x32_bf16 v[144:147], v[216:219], v[0:3], v[192:195]\n"
    "s_add_u32 m0, s53, 36864\n"
    "v_mfma_f32_16x16x32_bf16 v[144:147], v[220:223], v[4:7], v[144:147]\n"
    "s_nop 0\n"
    "ds_read_b128 v[208:211], v244 offset:43008\n"
    "ds_read_b128 v[212:215], v248 offset:43520\n"
    "v_mfma_f32_16x16x32_bf16 v[148:151], v[216:219], v[32:35], v[196:199]\n"
    "v_exp_f32 v232, v128\n"
    "v_mfma_f32_16x16x32_bf16 v[148:151], v[220:223], v[36:39], v[148:151]\n"
    "v_exp_f32 v233, v129\n"
    "v_mfma_f32_16x16x32_bf16 v[152:155], v[216:219], v[64:67], v[200:203]\n"
    "buffer_load_dwordx4 v247, s[40:43], s48 offen offset:0 lds\n"
    "v_mfma_f32_16x16x32_bf16 v[152:155], v[220:223], v[68:71], v[152:155]\n"
    "v_add_f32 v236, v232, v233\n"
    "v_cvt_pk_bf16_f32 v176, v232, v233\n"
    "v_mfma_f32_16x16x32_bf16 v[156:159], v[216:219], v[96:99], v[204:207]\n"
    "v_exp_f32 v234, v130\n"
    "v_mfma_f32_16x16x32_bf16 v[156:159], v[220:223], v[100:103], v[156:159]\n"
    "s_waitcnt lgkmcnt(2)\n"
    "v_exp_f32 v235, v131\n"
    "v_mfma_f32_16x16x32_bf16 v[144:147], v[224:227], v[8:11], v[144:147]\n"
    "buffer_load_dwordx4 v247, s[40:43], s49 offen offset:1024 lds\n"
    "v_mfma_f32_16x16x32_bf16 v[144:147], v[228:231], v[12:15], v[144:147]\n"
    "s_nop 0\n"
    "ds_read_b128 v[216:219], v244 offset:44032\n"
    "ds_read_b128 v[220:223], v248 offset:44544\n"
    "v_mfma_f32_16x16x32_bf16 v[148:151], v[224:227], v[40:43], v[148:151]\n"
    "v_add_f32 v236, v236, v234\n"
    "v_cvt_pk_bf16_f32 v177, v234, v235\n"
    "v_mfma_f32_16x16x32_bf16 v[148:151], v[228:231], v[44:47], v[148:151]\n"
    "v_add_f32 v236, v236, v235\n"
    "v_mfma_f32_16x16x32_bf16 v[152:155], v[224:227], v[72:75], v[152:155]\n"
    "buffer_load_dwordx4 v247, s[40:43], s50 offen offset:2048 lds\n"
    "v_mfma_f32_16x16x32_bf16 v[152:155], v[228:231], v[76:79], v[152:155]\n"
    "v_exp_f32 v232, v132\n"
    "v_mfma_f32_16x16x32_bf16 v[156:159], v[224:227], v[104:107], v[156:159]\n"
    "v_exp_f32 v233, v133\n"
    "v_mfma_f32_16x16x32_bf16 v[156:159], v[228:231], v[108:111], v[156:159]\n"
    "s_waitcnt lgkmcnt(2)\n"
    "ds_read_b64_tr_b16 v[224:225], v245 offset:0\n"
    "ds_read_b64_tr_b16 v[226:227], v245 offset:8192\n"
    "v_mfma_f32_16x16x32_bf16 v[144:147], v[208:211], v[16:19], v[144:147]\n"
    "buffer_load_dwordx4 v247, s[40:43], s51 offen offset:3072 lds\n"
    "v_mfma_f32_16x16x32_bf16 v[144:147], v[212:215], v[20:23], v[144:147]\n"
    "s_mov_b32 m0, s55\n"
    "v_add_f32 v237, v232, v233\n"
    "v_cvt_pk_bf16_f32 v180, v232, v233\n"
    "v_mfma_f32_16x16x32_bf16 v[148:151], v[208:211], v[48:51], v[148:151]\n"
    "v_exp_f32 v234, v134\n"
    "v_mfma_f32_16x16x32_bf16 v[148:151], v[212:215], v[52:55], v[148:151]\n"
    "s_nop 0\n"
    "ds_read_b64_tr_b16 v[228:229], v246 offset:0\n"
    "ds_read_b64_tr_b16 v[230:231], v246 offset:8192\n"
    "v_mfma_f32_16x16x32_bf16 v[152:155], v[208:211], v[80:83], v[152:155]\n"
    "buffer_load_dword v247, s[64:67], s68 offen lds\n"
    "v_mfma_f32_16x16x32_bf16 v[152:155], v[212:215], v[84:87], v[152:155]\n"
    "v_add_u32 v247, 32768, v247\n"
    "v_mfma_f32_16x16x32_bf16 v[156:159], v[208:211], v[112:115], v[156:159]\n"
    "s_add_u32 m0, s52, 32768\n"
    "v_exp_f32 v235, v135\n"
    "v_mfma_f32_16x16x32_bf16 v[156:159], v[212:215], v[116:119], v[156:159]\n"
    "s_waitcnt lgkmcnt(4)\n"
    "ds_read_b64_tr_b16 v[208:209], v245 offset:512\n"
    "ds_read_b64_tr_b16 v[210:211], v245 offset:8704\n"
    "v_mfma_f32_16x16x32_bf16 v[144:147], v[216:219], v[24:27], v[144:147]\n"
    "v_add_f32 v237, v237, v234\n"
    "v_cvt_pk_bf16_f32 v181, v234, v235\n"
    "v_mfma_f32_16x16x32_bf16 v[144:147], v[220:223], v[28:31], v[144:147]\n"
    "v_add_f32 v237, v237, v235\n"
    "v_mfma_f32_16x16x32_bf16 v[148:151], v[216:219], v[56:59], v[148:151]\n"
    "v_exp_f32 v232, v136\n"
    "v_mfma_f32_16x16x32_bf16 v[148:151], v[220:223], v[60:63], v[148:151]\n"
    "s_nop 0\n"
    "ds_read_b64_tr_b16 v[212:213], v246 offset:512\n"
    "ds_read_b64_tr_b16 v[214:215], v246 offset:8704\n"
    "v_mfma_f32_16x16x32_bf16 v[152:155], v[216:219], v[88:91], v[152:155]\n"
    "v_exp_f32 v233, v137\n"
    "v_mfma_f32_16x16x32_bf16 v[152:155], v[220:223], v[92:95], v[152:155]\n"
    "v_add_f32 v238, v232, v233\n"
    "v_cvt_pk_bf16_f32 v184, v232, v233\n"
    "v_mfma_f32_16x16x32_bf16 v[156:159], v[216:219], v[120:123], v[156:159]\n"
    "v_exp_f32 v234, v138\n"
    "v_mfma_f32_16x16x32_bf16 v[156:159], v[220:223], v[124:127], v[156:159]\n"
    "s_waitcnt lgkmcnt(6)\n"
    "ds_read_b64_tr_b16 v[216:217], v245 offset:1024\n"
    "ds_read_b64_tr_b16 v[218:219], v245 offset:9216\n"
    "v_mfma_f32_16x16x32_bf16 a[0:3], v[224:227], v[160:163], 0\n"
    "v_exp_f32 v235, v139\n"
    "v_mfma_f32_16x16x32_bf16 a[4:7], v[224:227], v[164:167], 0\n"
    "v_add_f32 v238, v238, v234\n"
    "v_cvt_pk_bf16_f32 v185, v234, v235\n"
    "v_mfma_f32_16x16x32_bf16 a[8:11], v[224:227], v[168:171], 0\n"
    "v_add_f32 v238, v238, v235\n"
    "v_mfma_f32_16x16x32_bf16 a[12:15], v[224:227], v[172:175], 0\n"
    "s_waitcnt lgkmcnt(6)\n"
    "ds_read_b64_tr_b16 v[224:225], v246 offset:1024\n"
    "ds_read_b64_tr_b16 v[226:227], v246 offset:9216\n"
    "v_mfma_f32_16x16x32_bf16 a[16:19], v[228:231], v[160:163], 0\n"
    "v_exp_f32 v232, v140\n"
    "v_mfma_f32_16x16x32_bf16 a[20:23], v[228:231], v[164:167], 0\n"
    "v_exp_f32 v233, v141\n"
    "v_mfma_f32_16x16x32_bf16 a[24:27], v[228:231], v[168:171], 0\n"
    "v_add_f32 v239, v232, v233\n"
    "v_cvt_pk_bf16_f32 v188, v232, v233\n"
    "v_mfma_f32_16x16x32_bf16 a[28:31], v[228:231], v[172:175], 0\n"
    "s_waitcnt lgkmcnt(6)\n"
    "ds_read_b64_tr_b16 v[220:221], v245 offset:1536\n"
    "ds_read_b64_tr_b16 v[222:223], v245 offset:9728\n"
    "v_mfma_f32_16x16x32_bf16 a[32:35], v[208:211], v[160:163], 0\n"
    "v_exp_f32 v234, v142\n"
    "v_mfma_f32_16x16x32_bf16 a[36:39], v[208:211], v[164:167], 0\n"
    "v_exp_f32 v235, v143\n"
    "v_mfma_f32_16x16x32_bf16 a[40:43], v[208:211], v[168:171], 0\n"
    "v_add_f32 v239, v239, v234\n"
    "v_cvt_pk_bf16_f32 v189, v234, v235\n"
    "v_mfma_f32_16x16x32_bf16 a[44:47], v[208:211], v[172:175], 0\n"
    "s_waitcnt lgkmcnt(6)\n"
    "ds_read_b64_tr_b16 v[208:209], v246 offset:1536\n"
    "ds_read_b64_tr_b16 v[210:211], v246 offset:9728\n"
    "v_mfma_f32_16x16x32_bf16 a[48:51], v[212:215], v[160:163], 0\n"
    "v_add_f32 v239, v239, v235\n"
    "v_mfma_f32_16x16x32_bf16 a[52:55], v[212:215], v[164:167], 0\n"
    "v_exp_f32 v232, v144\n"
    "v_mfma_f32_16x16x32_bf16 a[56:59], v[212:215], v[168:171], 0\n"
    "v_exp_f32 v233, v145\n"
    "v_mfma_f32_16x16x32_bf16 a[60:63], v[212:215], v[172:175], 0\n"
    "s_waitcnt lgkmcnt(6)\n"
    "ds_read_b64_tr_b16 v[212:213], v245 offset:2048\n"
    "ds_read_b64_tr_b16 v[214:215], v245 offset:10240\n"
    "v_mfma_f32_16x16x32_bf16 a[64:67], v[216:219], v[160:163], 0\n"
    "v_add_f32 v236, v236, v232\n"
    "v_cvt_pk_bf16_f32 v178, v232, v233\n"
    "v_mfma_f32_16x16x32_bf16 a[68:71], v[216:219], v[164:167], 0\n"
    "v_add_f32 v236, v236, v233\n"
    "v_mfma_f32_16x16x32_bf16 a[72:75], v[216:219], v[168:171], 0\n"
    "v_exp_f32 v234, v146\n"
    "v_mfma_f32_16x16x32_bf16 a[76:79], v[216:219], v[172:175], 0\n"
    "s_waitcnt lgkmcnt(6)\n"
    "ds_read_b64_tr_b16 v[228:229], v246 offset:2048\n"
    "ds_read_b64_tr_b16 v[230:231], v246 offset:10240\n"
    "v_mfma_f32_16x16x32_bf16 a[80:83], v[224:227], v[160:163], 0\n"
    "v_exp_f32 v235, v147\n"
    "v_mfma_f32_16x16x32_bf16 a[84:87], v[224:227], v[164:167], 0\n"
    "v_add_f32 v236, v236, v234\n"
    "v_cvt_pk_bf16_f32 v179, v234, v235\n"
    "v_mfma_f32_16x16x32_bf16 a[88:91], v[224:227], v[168:171], 0\n"
    "v_add_f32 v236, v236, v235\n"
    "v_mfma_f32_16x16x32_bf16 a[92:95], v[224:227], v[172:175], 0\n"
    "s_waitcnt lgkmcnt(6)\n"
    "ds_read_b64_tr_b16 v[224:225], v245 offset:2560\n"
    "ds_read_b64_tr_b16 v[226:227], v245 offset:10752\n"
    "v_mfma_f32_16x16x32_bf16 a[96:99], v[220:223], v[160:163], 0\n"
    "v_exp_f32 v232, v148\n"
    "v_mfma_f32_16x16x32_bf16 a[100:103], v[220:223], v[164:167], 0\n"
    "v_exp_f32 v233, v149\n"
    "v_mfma_f32_16x16x32_bf16 a[104:107], v[220:223], v[168:171], 0\n"
    "v_add_f32 v237, v237, v232\n"
    "v_cvt_pk_bf16_f32 v182, v232, v233\n"
    "v_mfma_f32_16x16x32_bf16 a[108:111], v[220:223], v[172:175], 0\n"
    "s_waitcnt lgkmcnt(6)\n"
    "ds_read_b64_tr_b16 v[216:217], v246 offset:2560\n"
    "ds_read_b64_tr_b16 v[218:219], v246 offset:10752\n"
    "v_mfma_f32_16x16x32_bf16 a[112:115], v[208:211], v[160:163], 0\n"
    "v_add_f32 v237, v237, v233\n"
    "v_mfma_f32_16x16x32_bf16 a[116:119], v[208:211], v[164:167], 0\n"
    "v_exp_f32 v234, v150\n"
    "v_mfma_f32_16x16x32_bf16 a[120:123], v[208:211], v[168:171], 0\n"
    "v_exp_f32 v235, v151\n"
    "v_mfma_f32_16x16x32_bf16 a[124:127], v[208:211], v[172:175], 0\n"
    "s_waitcnt lgkmcnt(6)\n"
    "ds_read_b64_tr_b16 v[208:209], v245 offset:3072\n"
    "ds_read_b64_tr_b16 v[210:211], v245 offset:11264\n"
    "v_mfma_f32_16x16x32_bf16 a[128:131], v[212:215], v[160:163], 0\n"
    "v_add_f32 v237, v237, v234\n"
    "v_cvt_pk_bf16_f32 v183, v234, v235\n"
    "v_mfma_f32_16x16x32_bf16 a[132:135], v[212:215], v[164:167], 0\n"
    "v_add_f32 v237, v237, v235\n"
    "v_mfma_f32_16x16x32_bf16 a[136:139], v[212:215], v[168:171], 0\n"
    "v_exp_f32 v232, v152\n"
    "v_mfma_f32_16x16x32_bf16 a[140:143], v[212:215], v[172:175], 0\n"
    "s_waitcnt lgkmcnt(6)\n"
    "ds_read_b64_tr_b16 v[220:221], v246 offset:3072\n"
    "ds_read_b64_tr_b16 v[222:223], v246 offset:11264\n"
    "v_mfma_f32_16x16x32_bf16 a[144:147], v[228:231], v[160:163], 0\n"
    "v_exp_f32 v233, v153\n"
    "v_mfma_f32_16x16x32_bf16 a[148:151], v[228:231], v[164:167], 0\n"
    "v_add_f32 v238, v238, v232\n"
    "v_cvt_pk_bf16_f32 v186, v232, v233\n"
    "v_mfma_f32_16x16x32_bf16 a[152:155], v[228:231], v[168:171], 0\n"
    "v_add_f32 v238, v238, v233\n"
    "v_mfma_f32_16x16x32_bf16 a[156:159], v[228:231], v[172:175], 0\n"
    "s_waitcnt lgkmcnt(6)\n"
    "ds_read_b64_tr_b16 v[228:229], v245 offset:3584\n"
    "ds_read_b64_tr_b16 v[230:231], v245 offset:11776\n"
    "v_mfma_f32_16x16x32_bf16 a[160:163], v[224:227], v[160:163], 0\n"
    "v_exp_f32 v234, v154\n"
    "v_mfma_f32_16x16x32_bf16 a[164:167], v[224:227], v[164:167], 0\n"
    "v_exp_f32 v235, v155\n"
    "v_mfma_f32_16x16x32_bf16 a[168:171], v[224:227], v[168:171], 0\n"
    "v_add_f32 v238, v238, v234\n"
    "v_cvt_pk_bf16_f32 v187, v234, v235\n"
    "v_mfma_f32_16x16x32_bf16 a[172:175], v[224:227], v[172:175], 0\n"
    "s_waitcnt lgkmcnt(6)\n"
    "ds_read_b64_tr_b16 v[224:225], v246 offset:3584\n"
    "ds_read_b64_tr_b16 v[226:227], v246 offset:11776\n"
    "v_mfma_f32_16x16x32_bf16 a[176:179], v[216:219], v[160:163], 0\n"
    "v_add_f32 v238, v238, v235\n"
    "v_mfma_f32_16x16x32_bf16 a[180:183], v[216:219], v[164:167], 0\n"
    "v_exp_f32 v232, v156\n"
    "v_mfma_f32_16x16x32_bf16 a[184:187], v[216:219], v[168:171], 0\n"
    "v_exp_f32 v233, v157\n"
    "v_mfma_f32_16x16x32_bf16 a[188:191], v[216:219], v[172:175], 0\n"
    "s_waitcnt lgkmcnt(6)\n"
    "v_add_f32 v239, v239, v232\n"
    "v_cvt_pk_bf16_f32 v190, v232, v233\n"
    "v_mfma_f32_16x16x32_bf16 a[192:195], v[208:211], v[160:163], 0\n"
    "v_add_f32 v239, v239, v233\n"
    "v_mfma_f32_16x16x32_bf16 a[196:199], v[208:211], v[164:167], 0\n"
    "v_exp_f32 v234, v158\n"
    "v_mfma_f32_16x16x32_bf16 a[200:203], v[208:211], v[168:171], 0\n"
    "s_waitcnt lgkmcnt(0) vmcnt(1)\n"
    "s_barrier\n"
    "v_exp_f32 v235, v159\n"
    "v_mfma_f32_16x16x32_bf16 a[204:207], v[208:211], v[172:175], 0\n"
    "s_nop 0\n"
    "ds_read_b128 v[208:211], v244 offset:0\n"
    "ds_read_b128 v[212:215], v248 offset:512\n"
    "v_mfma_f32_16x16x32_bf16 a[208:211], v[220:223], v[160:163], 0\n"
    "buffer_load_dwordx4 v247, s[36:39], s44 offen offset:0 lds\n"
    "v_mfma_f32_16x16x32_bf16 a[212:215], v[220:223], v[164:167], 0\n"
    "v_add_f32 v239, v239, v234\n"
    "v_cvt_pk_bf16_f32 v191, v234, v235\n"
    "v_mfma_f32_16x16x32_bf16 a[216:219], v[220:223], v[168:171], 0\n"
    "v_add_f32 v239, v239, v235\n"
    "v_max3_f32 v232, v236, v237, v238\n"
    "v_mfma_f32_16x16x32_bf16 a[220:223], v[220:223], v[172:175], 0\n"
    "s_nop 0\n"
    "v_max_f32 v232, v232, v239\n"
    "v_cmp_nge_f32 vcc, 0x4e800000, v232\n"
    "v_mfma_f32_16x16x32_bf16 a[224:227], v[228:231], v[160:163], 0\n"
    "buffer_load_dwordx4 v247, s[36:39], s45 offen offset:1024 lds\n"
    "v_mfma_f32_16x16x32_bf16 a[228:231], v[228:231], v[164:167], 0\n"
    "s_nop 0\n"
    "ds_read_b128 v[216:219], v244 offset:1024\n"
    "ds_read_b128 v[220:223], v248 offset:1536\n"
    "v_mfma_f32_16x16x32_bf16 a[232:235], v[228:231], v[168:171], 0\n"
    "buffer_load_dwordx4 v247, s[36:39], s46 offen offset:2048 lds\n"
    "v_mfma_f32_16x16x32_bf16 a[236:239], v[228:231], v[172:175], 0\n"
    "s_nop 0\n"
    "v_mfma_f32_16x16x32_bf16 a[240:243], v[224:227], v[160:163], 0\n"
    "v_mfma_f32_16x16x32_bf16 a[244:247], v[224:227], v[164:167], 0\n"
    "v_mfma_f32_16x16x32_bf16 a[248:251], v[224:227], v[168:171], 0\n"
    "v_mfma_f32_16x16x32_bf16 a[252:255], v[224:227], v[172:175], 0\n"
    "s_cbranch_vccnz STUB1z_m\n"
    "RET1z_m:\n"
    "s_lshl_b32 s31, s29, 4\n"
    "s_add_u32 s31, s31, s28\n"
    "s_lshl_b32 s31, s31, 3\n"
    "s_add_u32 s31, s31, 4096\n"
    "s_cmp_lg_u32 s30, 0\n"
    "s_cbranch_scc1 FHS\n"
    "v_mov_b32 v250, s70\n"
    "v_mov_b32 v251, s71\n"
    "v_mov_b32 v252, s31\n"
    "s_mov_b64 exec, 1\n"
    "global_store_dwordx2 v252, v[250:251], s[20:21] sc1\n"
    "s_mov_b64 exec, -1\n"
    "FHS:\n"
    "s_waitcnt lgkmcnt(2)\n"
    ".p2align 6\n"
    "LOOP_m:\n"
    "v_mfma_f32_16x16x32_bf16 v[128:131], v[208:211], v[0:3], v[192:195]\n"
    "buffer_load_dwordx4 v247, s[36:39], s47 offen offset:3072 lds\n"
    "v_mfma_f32_16x16x32_bf16 v[128:131], v[212:215], v[4:7], v[128:131]\n"
    "s_add_u32 m0, s52, 36864\n"
    "ds_read_b128 v[224:227], v244 offset:2048\n"
    "ds_read_b128 v[228:231], v248 offset:2560\n"
    "v_mfma_f32_16x16x32_bf16 v[132:135], v[208:211], v[32:35], v[196:199]\n"
    "v_add_f32 v240, v240, v236\n"
    "v_add_f32 v241, v241, v237\n"
    "v_mfma_f32_16x16x32_bf16 v[132:135], v[212:215], v[36:39], v[132:135]\n"
    "v_add_f32 v242, v242, v238\n"
    "v_add_f32 v243, v243, v239\n"
    "v_mfma_f32_16x16x32_bf16 v[136:139], v[208:211], v[64:67], v[200:203]\n"
    "buffer_load_dwordx4 v247, s[36:39], s48 offen offset:0 lds\n"
    "v_mfma_f32_16x16x32_bf16 v[136:139], v[212:215], v[68:71], v[136:139]\n"
    "v_mfma_f32_16x16x32_bf16 v[140:143], v[208:211], v[96:99], v[204:207]\n"
    "buffer_load_dwordx4 v247, s[36:39], s49 offen offset:1024 lds\n"
    "v_mfma_f32_16x16x32_bf16 v[140:143], v[212:215], v[100:103], v[140:143]\n"
    "s_waitcnt lgkmcnt(2)\n"
    "v_mfma_f32_16x16x32_bf16 v[128:131], v[216:219], v[8:11], v[128:131]\n"
    "v_mfma_f32_16x16x32_bf16 v[128:131], v[220:223], v[12:15], v[128:131]\n"
    "s_nop 0\n"
    "ds_read_b128 v[208:211], v244 offset:3072\n"
    "ds_read_b128 v[212:215], v248 offset:3584\n"
    "v_mfma_f32_16x16x32_bf16 v[132:135], v[216:219], v[40:43], v[132:135]\n"
    "buffer_load_dwordx4 v247, s[36:39], s50 offen offset:2048 lds\n"
    "v_mfma_f32_16x16x32_bf16 v[132:135], v[220:223], v[44:47], v[132:135]\n"
    "v_mfma_f32_16x16x32_bf16 v[136:139], v[216:219], v[72:75], v[136:139]\n"
    "v_mfma_f32_16x16x32_bf16 v[136:139], v[220:223], v[76:79], v[136:139]\n"
    "v_mfma_f32_16x16x32_bf16 v[140:143], v[216:219], v[104:107], v[140:143]\n"
    "buffer_load_dwordx4 v247, s[36:39], s51 offen offset:3072 lds\n"
    "v_mfma_f32_16x16x32_bf16 v[140:143], v[220:223], v[108:111], v[140:143]\n"
    "s_waitcnt lgkmcnt(2)\n"
    "v_mfma_f32_16x16x32_bf16 v[128:131], v[224:227], v[16:19], v[128:131]\n"
    "s_add_u32 m0, s53, 0\n"
    "v_mfma_f32_16x16x32_bf16 v[128:131], v[228:231], v[20:23], v[128:131]\n"
    "s_nop 0\n"
    "ds_read_b128 v[216:219], v244 offset:8192\n"
    "ds_read_b128 v[220:223], v248 offset:8704\n"
    "v_mfma_f32_16x16x32_bf16 v[132:135], v[224:227], v[48:51], v[132:135]\n"
    "s_cmp_le_u32 s54, 4\n"
    "buffer_load_dwordx4 v247, s[40:43], s44 offen offset:0 lds\n"
    "v_mfma_f32_16x16x32_bf16 v[132:135], v[228:231], v[52:55], v[132:135]\n"
    "s_cbranch_scc1 LATE_m\n"
    "LATERET_m:\n"
    "v_mfma_f32_16x16x32_bf16 v[136:139], v[224:227], v[80:83], v[136:139]\n"
    "v_mfma_f32_16x16x32_bf16 v[136:139], v[228:231], v[84:87], v[136:139]\n"
    "v_mfma_f32_16x16x32_bf16 v[140:143], v[224:227], v[112:115], v[140:143]\n"
    "buffer_load_dwordx4 v247, s[40:43], s45 offen offset:1024 lds\n"
    "v_mfma_f32_16x16x32_bf16 v[140:143], v[228:231], v[116:119], v[140:143]\n"
    "s_waitcnt lgkmcnt(2)\n"
    "v_mfma_f32_16x16x32_bf16 v[128:131], v[208:211], v[24:27], v[128:131]\n"
    "v_mfma_f32_16x16x32_bf16 v[128:131], v[212:215], v[28:31], v[128:131]\n"
    "s_nop 0\n"
    "ds_read_b128 v[224:227], v244 offset:9216\n"
    "ds_read_b128 v[228:231], v248 offset:9728\n"
    "v_mfma_f32_16x16x32_bf16 v[132:135], v[208:211], v[56:59], v[132:135]\n"
    "buffer_load_dwordx4 v247, s[40:43], s46 offen offset:2048 lds\n"
    "v_mfma_f32_16x16x32_bf16 v[132:135], v[212:215], v[60:63], v[132:135]\n"
    "v_mfma_f32_16x16x32_bf16 v[136:139], v[208:211], v[88:91], v[136:139]\n"
    "v_mfma_f32_16x16x32_bf16 v[136:139], v[212:215], v[92:95], v[136:139]\n"
    "v_mfma_f32_16x16x32_bf16 v[140:143], v[208:211], v[120:123], v[140:143]\n"
    "buffer_load_dwordx4 v247, s[40:43], s47 offen offset:3072 lds\n"
    "v_mfma_f32_16x16x32_bf16 v[140:143], v[212:215], v[124:127], v[140:143]\n"
    "s_waitcnt lgkmcnt(2)\n"
    "v_mfma_f32_16x16x32_bf16 v[144:147], v[216:219], v[0:3], v[192:195]\n"
    "s_add_u32 m0, s53, 4096\n"
    "v_mfma_f32_16x16x32_bf16 v[144:147], v[220:223], v[4:7], v[144:147]\n"
    "s_nop 0\n"
    "ds_read_b128 v[208:211], v244 offset:10240\n"
    "ds_read_b128 v[212:215], v248 offset:10752\n"
    "v_mfma_f32_16x16x32_bf16 v[148:151], v[216:219], v[32:35], v[196:199]\n"
    "v_exp_f32 v232, v128\n"
    "v_mfma_f32_16x16x32_bf16 v[148:151], v[220:223], v[36:39], v[148:151]\n"
    "v_exp_f32 v233, v129\n"
    "v_mfma_f32_16x16x32_bf16 v[152:155], v[216:219], v[64:67], v[200:203]\n"
    "buffer_load_dwordx4 v247, s[40:43], s48 offen offset:0 lds\n"
    "v_mfma_f32_16x16x32_bf16 v[152:155], v[220:223], v[68:71], v[152:155]\n"
    "v_add_f32 v236, v232, v233\n"
    "v_cvt_pk_bf16_f32 v160, v232, v233\n"
    "v_mfma_f32_16x16x32_bf16 v[156:159], v[216:219], v[96:99], v[204:207]\n"
    "v_exp_f32 v234, v130\n"
    "v_mfma_f32_16x16x32_bf16 v[156:159], v[220:223], v[100:103], v[156:159]\n"
    "s_waitcnt lgkmcnt(2)\n"
    "v_exp_f32 v235, v131\n"
    "v_mfma_f32_16x16x32_bf16 v[144:147], v[224:227], v[8:11], v[144:147]\n"
    "buffer_load_dwordx4 v247, s[40:43], s49 offen offset:1024 lds\n"
    "v_mfma_f32_16x16x32_bf16 v[144:147], v[228:231], v[12:15], v[144:147]\n"
    "s_nop 0\n"
    "ds_read_b128 v[216:219], v244 offset:11264\n"
    "ds_read_b128 v[220:223], v248 offset:11776\n"
    "v_mfma_f32_16x16x32_bf16 v[148:151], v[224:227], v[40:43], v[148:151]\n"
    "v_add_f32 v236, v236, v234\n"
    "v_cvt_pk_bf16_f32 v161, v234, v235\n"
    "v_mfma_f32_16x16x32_bf16 v[148:151], v[228:231], v[44:47], v[148:151]\n"
    "v_add_f32 v236, v236, v235\n"
    "v_mfma_f32_16x16x32_bf16 v[152:155], v[224:227], v[72:75], v[152:155]\n"
    "buffer_load_dwordx4 v247, s[40:43], s50 offen offset:2048 lds\n"
    "v_mfma_f32_16x16x32_bf16 v[152:155], v[228:231], v[76:79], v[152:155]\n"
    "v_exp_f32 v232, v132\n"
    "v_mfma_f32_16x16x32_bf16 v[156:159], v[224:227], v[104:107], v[156:159]\n"
    "v_exp_f32 v233, v133\n"
    "v_mfma_f32_16x16x32_bf16 v[156:159], v[228:231], v[108:111], v[156:159]\n"
    "s_waitcnt lgkmcnt(2)\n"
    "ds_read_b64_tr_b16 v[224:225], v245 offset:32768\n"
    "ds_read_b64_tr_b16 v[226:227], v245 offset:40960\n"
    "v_mfma_f32_16x16x32_bf16 v[144:147], v[208:211], v[16:19], v[144:147]\n"
    "buffer_load_dwordx4 v247, s[40:43], s51 offen offset:3072 lds\n"
    "v_mfma_f32_16x16x32_bf16 v[144:147], v[212:215], v[20:23], v[144:147]\n"
    "s_mov_b32 m0, s55\n"
    "v_add_f32 v237, v232, v233\n"
    "v_cvt_pk_bf16_f32 v164, v232, v233\n"
    "v_mfma_f32_16x16x32_bf16 v[148:151], v[208:211], v[48:51], v[148:151]\n"
    "v_exp_f32 v234, v134\n"
    "v_mfma_f32_16x16x32_bf16 v[148:151], v[212:215], v[52:55], v[148:151]\n"
    "s_nop 0\n"
    "ds_read_b64_tr_b16 v[228:229], v246 offset:32768\n"
    "ds_read_b64_tr_b16 v[230:231], v246 offset:40960\n"
    "v_mfma_f32_16x16x32_bf16 v[152:155], v[208:211], v[80:83], v[152:155]\n"
    "buffer_load_dword v247, s[64:67], s68 offen lds\n"
    "v_mfma_f32_16x16x32_bf16 v[152:155], v[212:215], v[84:87], v[152:155]\n"
    "v_exp_f32 v235, v135\n"
    "v_mfma_f32_16x16x32_bf16 v[156:159], v[208:211], v[112:115], v[156:159]\n"
    "v_add_f32 v237, v237, v234\n"
    "v_cvt_pk_bf16_f32 v165, v234, v235\n"
    "v_mfma_f32_16x16x32_bf16 v[156:159], v[212:215], v[116:119], v[156:159]\n"
    "s_waitcnt lgkmcnt(4)\n"
    "ds_read_b64_tr_b16 v[208:209], v245 offset:33280\n"
    "ds_read_b64_tr_b16 v[210:211], v245 offset:41472\n"
    "v_mfma_f32_16x16x32_bf16 v[144:147], v[216:219], v[24:27], v[144:147]\n"
    "buffer_load_dword v255, v254, s[88:91], s99 offen offset:0\n"
    "v_mfma_f32_16x16x32_bf16 v[144:147], v[220:223], v[28:31], v[144:147]\n"
    "v_add_u32 v247, 32768, v247\n"
    "v_add_f32 v237, v237, v235\n"
    "v_mfma_f32_16x16x32_bf16 v[148:151], v[216:219], v[56:59], v[148:151]\n"
    "s_add_u32 m0, s52, 0\n"
    "v_exp_f32 v232, v136\n"
    "v_mfma_f32_16x16x32_bf16 v[148:151], v[220:223], v[60:63], v[148:151]\n"
    "s_nop 0\n"
    "ds_read_b64_tr_b16 v[212:213], v246 offset:33280\n"
    "ds_read_b64_tr_b16 v[214:215], v246 offset:41472\n"
    "v_mfma_f32_16x16x32_bf16 v[152:155], v[216:219], v[88:91], v[152:155]\n"
    "v_exp_f32 v233, v137\n"
    "v_mfma_f32_16x16x32_bf16 v[152:155], v[220:223], v[92:95], v[152:155]\n"
    "v_add_f32 v238, v232, v233\n"
    "v_cvt_pk_bf16_f32 v168, v232, v233\n"
    "v_mfma_f32_16x16x32_bf16 v[156:159], v[216:219], v[120:123], v[156:159]\n"
    "v_exp_f32 v234, v138\n"
    "v_mfma_f32_16x16x32_bf16 v[156:159], v[220:223], v[124:127], v[156:159]\n"
    "s_waitcnt lgkmcnt(6)\n"
    "ds_read_b64_tr_b16 v[216:217], v245 offset:33792\n"
    "ds_read_b64_tr_b16 v[218:219], v245 offset:41984\n"
    "v_mfma_f32_16x16x32_bf16 a[0:3], v[224:227], v[176:179], a[0:3]\n"
    "v_exp_f32 v235, v139\n"
    "v_mfma_f32_16x16x32_bf16 a[4:7], v[224:227], v[180:183], a[4:7]\n"
    "v_add_f32 v238, v238, v234\n"
    "v_cvt_pk_bf16_f32 v169, v234, v235\n"
    "v_mfma_f32_16x16x32_bf16 a[8:11], v[224:227], v[184:187], a[8:11]\n"
    "v_add_f32 v238, v238, v235\n"
    "v_mfma_f32_16x16x32_bf16 a[12:15], v[224:227], v[188:191], a[12:15]\n"
    "s_waitcnt lgkmcnt(6)\n"
    "ds_read_b64_tr_b16 v[224:225], v246 offset:33792\n"
    "ds_read_b64_tr_b16 v[226:227], v246 offset:41984\n"
    "v_mfma_f32_16x16x32_bf16 a[16:19], v[228:231], v[176:179], a[16:19]\n"
    "v_exp_f32 v232, v140\n"
    "v_mfma_f32_16x16x32_bf16 a[20:23], v[228:231], v[180:183], a[20:23]\n"
    "v_exp_f32 v233, v141\n"
    "v_mfma_f32_16x16x32_bf16 a[24:27], v[228:231], v[184:187], a[24:27]\n"
    "v_add_f32 v239, v232, v233\n"
    "v_cvt_pk_bf16_f32 v172, v232, v233\n"
    "v_mfma_f32_16x16x32_bf16 a[28:31], v[228:231], v[188:191], a[28:31]\n"
    "s_waitcnt lgkmcnt(6)\n"
    "ds_read_b64_tr_b16 v[220:221], v245 offset:34304\n"
    "ds_read_b64_tr_b16 v[222:223], v245 offset:42496\n"
    "v_mfma_f32_16x16x32_bf16 a[32:35], v[208:211], v[176:179], a[32:35]\n"
    "v_exp_f32 v234, v142\n"
    "v_mfma_f32_16x16x32_bf16 a[36:39], v[208:211], v[180:183], a[36:39]\n"
    "v_exp_f32 v235, v143\n"
    "v_mfma_f32_16x16x32_bf16 a[40:43], v[208:211], v[184:187], a[40:43]\n"
    "v_add_f32 v239, v239, v234\n"
    "v_cvt_pk_bf16_f32 v173, v234, v235\n"
    "v_mfma_f32_16x16x32_bf16 a[44:47], v[208:211], v[188:191], a[44:47]\n"
    "s_waitcnt lgkmcnt(6)\n"
    "ds_read_b64_tr_b16 v[208:209], v246 offset:34304\n"
    "ds_read_b64_tr_b16 v[210:211], v246 offset:42496\n"
    "v_mfma_f32_16x16x32_bf16 a[48:51], v[212:215], v[176:179], a[48:51]\n"
    "v_add_f32 v239, v239, v235\n"
    "v_mfma_f32_16x16x32_bf16 a[52:55], v[212:215], v[180:183], a[52:55]\n"
    "v_exp_f32 v232, v144\n"
    "v_mfma_f32_16x16x32_bf16 a[56:59], v[212:215], v[184:187], a[56:59]\n"
    "v_exp_f32 v233, v145\n"
    "v_mfma_f32_16x16x32_bf16 a[60:63], v[212:215], v[188:191], a[60:63]\n"
    "s_waitcnt lgkmcnt(6)\n"
    "ds_read_b64_tr_b16 v[212:213], v245 offset:34816\n"
    "ds_read_b64_tr_b16 v[214:215], v245 offset:43008\n"
    "v_mfma_f32_16x16x32_bf16 a[64:67], v[216:219], v[176:179], a[64:67]\n"
    "v_add_f32 v236, v236, v232\n"
    "v_cvt_pk_bf16_f32 v162, v232, v233\n"
    "v_mfma_f32_16x16x32_bf16 a[68:71], v[216:219], v[180:183], a[68:71]\n"
    "v_add_f32 v236, v236, v233\n"
    "v_mfma_f32_16x16x32_bf16 a[72:75], v[216:219], v[184:187], a[72:75]\n"
    "v_exp_f32 v234, v146\n"
    "v_mfma_f32_16x16x32_bf16 a[76:79], v[216:219], v[188:191], a[76:79]\n"
    "s_waitcnt lgkmcnt(6)\n"
    "ds_read_b64_tr_b16 v[228:229], v246 offset:34816\n"
    "ds_read_b64_tr_b16 v[230:231], v246 offset:43008\n"
    "v_mfma_f32_16x16x32_bf16 a[80:83], v[224:227], v[176:179], a[80:83]\n"
    "v_exp_f32 v235, v147\n"
    "v_mfma_f32_16x16x32_bf16 a[84:87], v[224:227], v[180:183], a[84:87]\n"
    "v_add_f32 v236, v236, v234\n"
    "v_cvt_pk_bf16_f32 v163, v234, v235\n"
    "v_mfma_f32_16x16x32_bf16 a[88:91], v[224:227], v[184:187], a[88:91]\n"
    "v_add_f32 v236, v236, v235\n"
    "v_mfma_f32_16x16x32_bf16 a[92:95], v[224:227], v[188:191], a[92:95]\n"
    "s_waitcnt lgkmcnt(6)\n"
    "ds_read_b64_tr_b16 v[224:225], v245 offset:35328\n"
    "ds_read_b64_tr_b16 v[226:227], v245 offset:43520\n"
    "v_mfma_f32_16x16x32_bf16 a[96:99], v[220:223], v[176:179], a[96:99]\n"
    "v_exp_f32 v232, v148\n"
    "v_mfma_f32_16x16x32_bf16 a[100:103], v[220:223], v[180:183], a[100:103]\n"
    "v_exp_f32 v233, v149\n"
    "v_mfma_f32_16x16x32_bf16 a[104:107], v[220:223], v[184:187], a[104:107]\n"
    "v_add_f32 v237, v237, v232\n"
    "v_cvt_pk_bf16_f32 v166, v232, v233\n"
    "v_mfma_f32_16x16x32_bf16 a[108:111], v[220:223], v[188:191], a[108:111]\n"
    "s_waitcnt lgkmcnt(6)\n"
    "ds_read_b64_tr_b16 v[216:217], v246 offset:35328\n"
    "ds_read_b64_tr_b16 v[218:219], v246 offset:43520\n"
    "v_mfma_f32_16x16x32_bf16 a[112:115], v[208:211], v[176:179], a[112:115]\n"
    "v_add_f32 v237, v237, v233\n"
    "v_mfma_f32_16x16x32_bf16 a[116:119], v[208:211], v[180:183], a[116:119]\n"
    "v_exp_f32 v234, v150\n"
    "v_mfma_f32_16x16x32_bf16 a[120:123], v[208:211], v[184:187], a[120:123]\n"
    "v_exp_f32 v235, v151\n"
    "v_mfma_f32_16x16x32_bf16 a[124:127], v[208:211], v[188:191], a[124:127]\n"
    "s_waitcnt lgkmcnt(6)\n"
    "ds_read_b64_tr_b16 v[208:209], v245 offset:35840\n"
    "ds_read_b64_tr_b16 v[210:211], v245 offset:44032\n"
    "v_mfma_f32_16x16x32_bf16 a[128:131], v[212:215], v[176:179], a[128:131]\n"
    "v_add_f32 v237, v237, v234\n"
    "v_cvt_pk_bf16_f32 v167, v234, v235\n"
    "v_mfma_f32_16x16x32_bf16 a[132:135], v[212:215], v[180:183], a[132:135]\n"
    "v_add_f32 v237, v237, v235\n"
    "v_mfma_f32_16x16x32_bf16 a[136:139], v[212:215], v[184:187], a[136:139]\n"
    "v_exp_f32 v232, v152\n"
    "v_mfma_f32_16x16x32_bf16 a[140:143], v[212:215], v[188:191], a[140:143]\n"
    "s_waitcnt lgkmcnt(6)\n"
    "ds_read_b64_tr_b16 v[220:221], v246 offset:35840\n"
    "ds_read_b64_tr_b16 v[222:223], v246 offset:44032\n"
    "v_mfma_f32_16x16x32_bf16 a[144:147], v[228:231], v[176:179], a[144:147]\n"
    "v_exp_f32 v233, v153\n"
    "v_mfma_f32_16x16x32_bf16 a[148:151], v[228:231], v[180:183], a[148:151]\n"
    "v_add_f32 v238, v238, v232\n"
    "v_cvt_pk_bf16_f32 v170, v232, v233\n"
    "v_mfma_f32_16x16x32_bf16 a[152:155], v[228:231], v[184:187], a[152:155]\n"
    "v_add_f32 v238, v238, v233\n"
    "v_mfma_f32_16x16x32_bf16 a[156:159], v[228:231], v[188:191], a[156:159]\n"
    "s_waitcnt lgkmcnt(6)\n"
    "ds_read_b64_tr_b16 v[228:229], v245 offset:36352\n"
    "ds_read_b64_tr_b16 v[230:231], v245 offset:44544\n"
    "v_mfma_f32_16x16x32_bf16 a[160:163], v[224:227], v[176:179], a[160:163]\n"
    "v_exp_f32 v234, v154\n"
    "v_mfma_f32_16x16x32_bf16 a[164:167], v[224:227], v[180:183], a[164:167]\n"
    "v_exp_f32 v235, v155\n"
    "v_mfma_f32_16x16x32_bf16 a[168:171], v[224:227], v[184:187], a[168:171]\n"
    "v_add_f32 v238, v238, v234\n"
    "v_cvt_pk_bf16_f32 v171, v234, v235\n"
    "v_mfma_f32_16x16x32_bf16 a[172:175], v[224:227], v[188:191], a[172:175]\n"
    "s_waitcnt lgkmcnt(6)\n"
    "ds_read_b64_tr_b16 v[224:225], v246 offset:36352\n"
    "ds_read_b64_tr_b16 v[226:227], v246 offset:44544\n"
    "v_mfma_f32_16x16x32_bf16 a[176:179], v[216:219], v[176:179], a[176:179]\n"
    "v_add_f32 v238, v238, v235\n"
    "v_mfma_f32_16x16x32_bf16 a[180:183], v[216:219], v[180:183], a[180:183]\n"
    "v_exp_f32 v232, v156\n"
    "v_mfma_f32_16x16x32_bf16 a[184:187], v[216:219], v[184:187], a[184:187]\n"
    "v_exp_f32 v233, v157\n"
    "v_mfma_f32_16x16x32_bf16 a[188:191], v[216:219], v[188:191], a[188:191]\n"
    "s_waitcnt lgkmcnt(6)\n"
    "v_add_f32 v239, v239, v232\n"
    "v_cvt_pk_bf16_f32 v174, v232, v233\n"
    "v_mfma_f32_16x16x32_bf16 a[192:195], v[208:211], v[176:179], a[192:195]\n"
    "v_add_f32 v239, v239, v233\n"
    "v_mfma_f32_16x16x32_bf16 a[196:199], v[208:211], v[180:183], a[196:199]\n"
    "v_exp_f32 v234, v158\n"
    "v_mfma_f32_16x16x32_bf16 a[200:203], v[208:211], v[184:187], a[200:203]\n"
    "s_waitcnt lgkmcnt(0) vmcnt(2)\n"
    "s_barrier\n"
    "v_exp_f32 v235, v159\n"
    "v_mfma_f32_16x16x32_bf16 a[204:207], v[208:211], v[188:191], a[204:207]\n"
    "s_nop 0\n"
    "ds_read_b128 v[208:211], v244 offset:32768\n"
    "ds_read_b128 v[212:215], v248 offset:33280\n"
    "v_mfma_f32_16x16x32_bf16 a[208:211], v[220:223], v[176:179], a[208:211]\n"
    "buffer_load_dwordx4 v247, s[36:39], s44 offen offset:0 lds\n"
    "v_mfma_f32_16x16x32_bf16 a[212:215], v[220:223], v[180:183], a[212:215]\n"
    "v_add_f32 v239, v239, v234\n"
    "v_cvt_pk_bf16_f32 v175, v234, v235\n"
    "v_mfma_f32_16x16x32_bf16 a[216:219], v[220:223], v[184:187], a[216:219]\n"
    "v_add_f32 v239, v239, v235\n"
    "v_max3_f32 v232, v236, v237, v238\n"
    "v_mfma_f32_16x16x32_bf16 a[220:223], v[220:223], v[188:191], a[220:223]\n"
    "s_nop 0\n"
    "v_max_f32 v232, v232, v239\n"
    "v_cmp_nge_f32 vcc, 0x4e800000, v232\n"
    "v_mfma_f32_16x16x32_bf16 a[224:227], v[228:231], v[176:179], a[224:227]\n"
    "buffer_load_dwordx4 v247, s[36:39], s45 offen offset:1024 lds\n"
    "v_mfma_f32_16x16x32_bf16 a[228:231], v[228:231], v[180:183], a[228:231]\n"
    "s_nop 0\n"
    "ds_read_b128 v[216:219], v244 offset:33792\n"
    "ds_read_b128 v[220:223], v248 offset:34304\n"
    "v_mfma_f32_16x16x32_bf16 a[232:235], v[228:231], v[184:187], a[232:235]\n"
    "buffer_load_dwordx4 v247, s[36:39], s46 offen offset:2048 lds\n"
    "v_mfma_f32_16x16x32_bf16 a[236:239], v[228:231], v[188:191], a[236:239]\n"
    "s_nop 0\n"
    "v_mfma_f32_16x16x32_bf16 a[240:243], v[224:227], v[176:179], a[240:243]\n"
    "v_mfma_f32_16x16x32_bf16 a[244:247], v[224:227], v[180:183], a[244:247]\n"
    "v_mfma_f32_16x16x32_bf16 a[248:251], v[224:227], v[184:187], a[248:251]\n"
    "v_mfma_f32_16x16x32_bf16 a[252:255], v[224:227], v[188:191], a[252:255]\n"
    "s_cbranch_vccnz STUB0_m\n"
    "RET0_m:\n"
    "s_sub_u32 s54, s54, 1\n"
    "s_waitcnt lgkmcnt(2)\n"
    "s_cbranch_scc1 LAST_m\n"
    "v_mfma_f32_16x16x32_bf16 v[128:131], v[208:211], v[0:3], v[192:195]\n"
    "buffer_load_dwordx4 v247, s[36:39], s47 offen offset:3072 lds\n"
    "v_mfma_f32_16x16x32_bf16 v[128:131], v[212:215], v[4:7], v[128:131]\n"
    "s_add_u32 m0, s52, 4096\n"
    "ds_read_b128 v[224:227], v244 offset:34816\n"
    "ds_read_b128 v[228:231], v248 offset:35328\n"
    "v_mfma_f32_16x16x32_bf16 v[132:135], v[208:211], v[32:35], v[196:199]\n"
    "v_add_f32 v240, v240, v236\n"
    "v_add_f32 v241, v241, v237\n"
    "v_mfma_f32_16x16x32_bf16 v[132:135], v[212:215], v[36:39], v[132:135]\n"
    "v_add_f32 v242, v242, v238\n"
    "v_add_f32 v243, v243, v239\n"
    "v_mfma_f32_16x16x32_bf16 v[136:139], v[208:211], v[64:67], v[200:203]\n"
    "buffer_load_dwordx4 v247, s[36:39], s48 offen offset:0 lds\n"
    "v_mfma_f32_16x16x32_bf16 v[136:139], v[212:215], v[68:71], v[136:139]\n"
    "v_mfma_f32_16x16x32_bf16 v[140:143], v[208:211], v[96:99], v[204:207]\n"
    "buffer_load_dwordx4 v247, s[36:39], s49 offen offset:1024 lds\n"
    "v_mfma_f32_16x16x32_bf16 v[140:143], v[212:215], v[100:103], v[140:143]\n"
    "s_waitcnt lgkmcnt(2)\n"
    "v_mfma_f32_16x16x32_bf16 v[128:131], v[216:219], v[8:11], v[128:131]\n"
    "v_mfma_f32_16x16x32_bf16 v[128:131], v[220:223], v[12:15], v[128:131]\n"
    "s_nop 0\n"
    "ds_read_b128 v[208:211], v244 offset:35840\n"
    "ds_read_b128 v[212:215], v248 offset:36352\n"
    "v_mfma_f32_16x16x32_bf16 v[132:135], v[216:219], v[40:43], v[132:135]\n"
    "buffer_load_dwordx4 v247, s[36:39], s50 offen offset:2048 lds\n"
    "v_mfma_f32_16x16x32_bf16 v[132:135], v[220:223], v[44:47], v[132:135]\n"
    "v_mfma_f32_16x16x32_bf16 v[136:139], v[216:219], v[72:75], v[136:139]\n"
    "v_mfma_f32_16x16x32_bf16 v[136:139], v[220:223], v[76:79], v[136:139]\n"
    "v_mfma_f32_16x16x32_bf16 v[140:143], v[216:219], v[104:107], v[140:143]\n"
    "buffer_load_dwordx4 v247, s[36:39], s51 offen offset:3072 lds\n"
    "v_mfma_f32_16x16x32_bf16 v[140:143], v[220:223], v[108:111], v[140:143]\n"
    "s_waitcnt lgkmcnt(2)\n"
    "v_mfma_f32_16x16x32_bf16 v[128:131], v[224:227], v[16:19], v[128:131]\n"
    "s_add_u32 m0, s53, 32768\n"
    "v_mfma_f32_16x16x32_bf16 v[128:131], v[228:231], v[20:23], v[128:131]\n"
    "s_nop 0\n"
    "ds_read_b128 v[216:219], v244 offset:40960\n"
    "ds_read_b128 v[220:223], v248 offset:41472\n"
    "v_mfma_f32_16x16x32_bf16 v[132:135], v[224:227], v[48:51], v[132:135]\n"
    "buffer_load_dwordx4 v247, s[40:43], s44 offen offset:0 lds\n"
    "v_mfma_f32_16x16x32_bf16 v[132:135], v[228:231], v[52:55], v[132:135]\n"
    "v_mfma_f32_16x16x32_bf16 v[136:139], v[224:227], v[80:83], v[136:139]\n"
    "v_mfma_f32_16x16x32_bf16 v[136:139], v[228:231], v[84:87], v[136:139]\n"
    "v_mfma_f32_16x16x32_bf16 v[140:143], v[224:227], v[112:115], v[140:143]\n"
    "buffer_load_dwordx4 v247, s[40:43], s45 offen offset:1024 lds\n"
    "v_mfma_f32_16x16x32_bf16 v[140:143], v[228:231], v[116:119], v[140:143]\n"
    "s_waitcnt lgkmcnt(2)\n"
    "v_mfma_f32_16x16x32_bf16 v[128:131], v[208:211], v[24:27], v[128:131]\n"
    "v_mfma_f32_16x16x32_bf16 v[128:131], v[212:215], v[28:31], v[128:131]\n"
    "s_nop 0\n"
    "ds_read_b128 v[224:227], v244 offset:41984\n"
    "ds_read_b128 v[228:231], v248 offset:42496\n"
    "v_mfma_f32_16x16x32_bf16 v[132:135], v[208:211], v[56:59], v[132:135]\n"
    "buffer_load_dwordx4 v247, s[40:43], s46 offen offset:2048 lds\n"
    "v_mfma_f32_16x16x32_bf16 v[132:135], v[212:215], v[60:63], v[132:135]\n"
    "v_mfma_f32_16x16x32_bf16 v[136:139], v[208:211], v[88:91], v[136:139]\n"
    "v_mfma_f32_16x16x32_bf16 v[136:139], v[212:215], v[92:95], v[136:139]\n"
    "v_mfma_f32_16x16x32_bf16 v[140:143], v[208:211], v[120:123], v[140:143]\n"
    "buffer_load_dwordx4 v247, s[40:43], s47 offen offset:3072 lds\n"
    "v_mfma_f32_16x16x32_bf16 v[140:143], v[212:215], v[124:127], v[140:143]\n"
    "s_waitcnt lgkmcnt(2)\n"
    "v_mfma_f32_16x16x32_bf16 v[144:147], v[216:219], v[0:3], v[192:195]\n"
    "s_add_u32 m0, s53, 36864\n"
    "v_mfma_f32_16x16x32_bf16 v[144:147], v[220:223], v[4:7], v[144:147]\n"
    "s_nop 0\n"
    "ds_read_b128 v[208:211], v244 offset:43008\n"
    "ds_read_b128 v[212:215], v248 offset:43520\n"
    "v_mfma_f32_16x16x32_bf16 v[148:151], v[216:219], v[32:35], v[196:199]\n"
    "v_exp_f32 v232, v128\n"
    "v_mfma_f32_16x16x32_bf16 v[148:151], v[220:223], v[36:39], v[148:151]\n"
    "v_exp_f32 v233, v129\n"
    "v_mfma_f32_16x16x32_bf16 v[152:155], v[216:219], v[64:67], v[200:203]\n"
    "buffer_load_dwordx4 v247, s[40:43], s48 offen offset:0 lds\n"
    "v_mfma_f32_16x16x32_bf16 v[152:155], v[220:223], v[68:71], v[152:155]\n"
    "v_add_f32 v236, v232, v233\n"
    "v_cvt_pk_bf16_f32 v176, v232, v233\n"
    "v_mfma_f32_16x16x32_bf16 v[156:159], v[216:219], v[96:99], v[204:207]\n"
    "v_exp_f32 v234, v130\n"
    "v_mfma_f32_16x16x32_bf16 v[156:159], v[220:223], v[100:103], v[156:159]\n"
    "s_waitcnt lgkmcnt(2)\n"
    "v_exp_f32 v235, v131\n"
    "v_mfma_f32_16x16x32_bf16 v[144:147], v[224:227], v[8:11], v[144:147]\n"
    "buffer_load_dwordx4 v247, s[40:43], s49 offen offset:1024 lds\n"
    "v_mfma_f32_16x16x32_bf16 v[144:147], v[228:231], v[12:15], v[144:147]\n"
    "s_nop 0\n"
    "ds_read_b128 v[216:219], v244 offset:44032\n"
    "ds_read_b128 v[220:223], v248 offset:44544\n"
    "v_mfma_f32_16x16x32_bf16 v[148:151], v[224:227], v[40:43], v[148:151]\n"
    "v_add_f32 v236, v236, v234\n"
    "v_cvt_pk_bf16_f32 v177, v234, v235\n"
    "v_mfma_f32_16x16x32_bf16 v[148:151], v[228:231], v[44:47], v[148:151]\n"
    "v_add_f32 v236, v236, v235\n"
    "v_mfma_f32_16x16x32_bf16 v[152:155], v[224:227], v[72:75], v[152:155]\n"
    "buffer_load_dwordx4 v247, s[40:43], s50 offen offset:2048 lds\n"
    "v_mfma_f32_16x16x32_bf16 v[152:155], v[228:231], v[76:79], v[152:155]\n"
    "v_exp_f32 v232, v132\n"
    "v_mfma_f32_16x16x32_bf16 v[156:159], v[224:227], v[104:107], v[156:159]\n"
    "v_exp_f32 v233, v133\n"
    "v_mfma_f32_16x16x32_bf16 v[156:159], v[228:231], v[108:111], v[156:159]\n"
    "s_waitcnt lgkmcnt(2)\n"
    "ds_read_b64_tr_b16 v[224:225], v245 offset:0\n"
    "ds_read_b64_tr_b16 v[226:227], v245 offset:8192\n"
    "v_mfma_f32_16x16x32_bf16 v[144:147], v[208:211], v[16:19], v[144:147]\n"
    "buffer_load_dwordx4 v247, s[40:43], s51 offen offset:3072 lds\n"
    "v_mfma_f32_16x16x32_bf16 v[144:147], v[212:215], v[20:23], v[144:147]\n"
    "s_mov_b32 m0, s55\n"
    "v_add_f32 v237, v232, v233\n"
    "v_cvt_pk_bf16_f32 v180, v232, v233\n"
    "v_mfma_f32_16x16x32_bf16 v[148:151], v[208:211], v[48:51], v[148:151]\n"
    "v_exp_f32 v234, v134\n"
    "v_mfma_f32_16x16x32_bf16 v[148:151], v[212:215], v[52:55], v[148:151]\n"
    "s_nop 0\n"
    "ds_read_b64_tr_b16 v[228:229], v246 offset:0\n"
    "ds_read_b64_tr_b16 v[230:231], v246 offset:8192\n"
    "v_mfma_f32_16x16x32_bf16 v[152:155], v[208:211], v[80:83], v[152:155]\n"
    "buffer_load_dword v247, s[64:67], s68 offen lds\n"
    "v_mfma_f32_16x16x32_bf16 v[152:155], v[212:215], v[84:87], v[152:155]\n"
    "v_add_u32 v247, 32768, v247\n"
    "v_mfma_f32_16x16x32_bf16 v[156:159], v[208:211], v[112:115], v[156:159]\n"
    "s_add_u32 m0, s52, 32768\n"
    "v_exp_f32 v235, v135\n"
    "v_mfma_f32_16x16x32_bf16 v[156:159], v[212:215], v[116:119], v[156:159]\n"
    "s_waitcnt lgkmcnt(4)\n"
    "ds_read_b64_tr_b16 v[208:209], v245 offset:512\n"
    "ds_read_b64_tr_b16 v[210:211], v245 offset:8704\n"
    "v_mfma_f32_16x16x32_bf16 v[144:147], v[216:219], v[24:27], v[144:147]\n"
    "v_add_f32 v237, v237, v234\n"
    "v_cvt_pk_bf16_f32 v181, v234, v235\n"
    "v_mfma_f32_16x16x32_bf16 v[144:147], v[220:223], v[28:31], v[144:147]\n"
    "v_add_f32 v237, v237, v235\n"
    "v_mfma_f32_16x16x32_bf16 v[148:151], v[216:219], v[56:59], v[148:151]\n"
    "v_exp_f32 v232, v136\n"
    "v_mfma_f32_16x16x32_bf16 v[148:151], v[220:223], v[60:63], v[148:151]\n"
    "s_nop 0\n"
    "ds_read_b64_tr_b16 v[212:213], v246 offset:512\n"
    "ds_read_b64_tr_b16 v[214:215], v246 offset:8704\n"
    "v_mfma_f32_16x16x32_bf16 v[152:155], v[216:219], v[88:91], v[152:155]\n"
    "v_exp_f32 v233, v137\n"
    "v_mfma_f32_16x16x32_bf16 v[152:155], v[220:223], v[92:95], v[152:155]\n"
    "v_add_f32 v238, v232, v233\n"
    "v_cvt_pk_bf16_f32 v184, v232, v233\n"
    "v_mfma_f32_16x16x32_bf16 v[156:159], v[216:219], v[120:123], v[156:159]\n"
    "v_exp_f32 v234, v138\n"
    "v_mfma_f32_16x16x32_bf16 v[156:159], v[220:223], v[124:127], v[156:159]\n"
    "s_waitcnt lgkmcnt(6)\n"
    "ds_read_b64_tr_b16 v[216:217], v245 offset:1024\n"
    "ds_read_b64_tr_b16 v[218:219], v245 offset:9216\n"
    "v_mfma_f32_16x16x32_bf16 a[0:3], v[224:227], v[160:163], a[0:3]\n"
    "v_exp_f32 v235, v139\n"
    "v_mfma_f32_16x16x32_bf16 a[4:7], v[224:227], v[164:167], a[4:7]\n"
    "v_add_f32 v238, v238, v234\n"
    "v_cvt_pk_bf16_f32 v185, v234, v235\n"
    "v_mfma_f32_16x16x32_bf16 a[8:11], v[224:227], v[168:171], a[8:11]\n"
    "v_add_f32 v238, v238, v235\n"
    "v_mfma_f32_16x16x32_bf16 a[12:15], v[224:227], v[172:175], a[12:15]\n"
    "s_waitcnt lgkmcnt(6)\n"
    "ds_read_b64_tr_b16 v[224:225], v246 offset:1024\n"
    "ds_read_b64_tr_b16 v[226:227], v246 offset:9216\n"
    "v_mfma_f32_16x16x32_bf16 a[16:19], v[228:231], v[160:163], a[16:19]\n"
    "v_exp_f32 v232, v140\n"
    "v_mfma_f32_16x16x32_bf16 a[20:23], v[228:231], v[164:167], a[20:23]\n"
    "v_exp_f32 v233, v141\n"
    "v_mfma_f32_16x16x32_bf16 a[24:27], v[228:231], v[168:171], a[24:27]\n"
    "v_add_f32 v239, v232, v233\n"
    "v_cvt_pk_bf16_f32 v188, v232, v233\n"
    "v_mfma_f32_16x16x32_bf16 a[28:31], v[228:231], v[172:175], a[28:31]\n"
    "s_waitcnt lgkmcnt(6)\n"
    "ds_read_b64_tr_b16 v[220:221], v245 offset:1536\n"
    "ds_read_b64_tr_b16 v[222:223], v245 offset:9728\n"
    "v_mfma_f32_16x16x32_bf16 a[32:35], v[208:211], v[160:163], a[32:35]\n"
    "v_exp_f32 v234, v142\n"
    "v_mfma_f32_16x16x32_bf16 a[36:39], v[208:211], v[164:167], a[36:39]\n"
    "v_exp_f32 v235, v143\n"
    "v_mfma_f32_16x16x32_bf16 a[40:43], v[208:211], v[168:171], a[40:43]\n"
    "v_add_f32 v239, v239, v234\n"
    "v_cvt_pk_bf16_f32 v189, v234, v235\n"
    "v_mfma_f32_16x16x32_bf16 a[44:47], v[208:211], v[172:175], a[44:47]\n"
    "s_waitcnt lgkmcnt(6)\n"
    "ds_read_b64_tr_b16 v[208:209], v246 offset:1536\n"
    "ds_read_b64_tr_b16 v[210:211], v246 offset:9728\n"
    "v_mfma_f32_16x16x32_bf16 a[48:51], v[212:215], v[160:163], a[48:51]\n"
    "v_add_f32 v239, v239, v235\n"
    "v_mfma_f32_16x16x32_bf16 a[52:55], v[212:215], v[164:167], a[52:55]\n"
    "v_exp_f32 v232, v144\n"
    "v_mfma_f32_16x16x32_bf16 a[56:59], v[212:215], v[168:171], a[56:59]\n"
    "v_exp_f32 v233, v145\n"
    "v_mfma_f32_16x16x32_bf16 a[60:63], v[212:215], v[172:175], a[60:63]\n"
    "s_waitcnt lgkmcnt(6)\n"
    "ds_read_b64_tr_b16 v[212:213], v245 offset:2048\n"
    "ds_read_b64_tr_b16 v[214:215], v245 offset:10240\n"
    "v_mfma_f32_16x16x32_bf16 a[64:67], v[216:219], v[160:163], a[64:67]\n"
    "v_add_f32 v236, v236, v232\n"
    "v_cvt_pk_bf16_f32 v178, v232, v233\n"
    "v_mfma_f32_16x16x32_bf16 a[68:71], v[216:219], v[164:167], a[68:71]\n"
    "v_add_f32 v236, v236, v233\n"
    "v_mfma_f32_16x16x32_bf16 a[72:75], v[216:219], v[168:171], a[72:75]\n"
    "v_exp_f32 v234, v146\n"
    "v_mfma_f32_16x16x32_bf16 a[76:79], v[216:219], v[172:175], a[76:79]\n"
    "s_waitcnt lgkmcnt(6)\n"
    "ds_read_b64_tr_b16 v[228:229], v246 offset:2048\n"
    "ds_read_b64_tr_b16 v[230:231], v246 offset:10240\n"
    "v_mfma_f32_16x16x32_bf16 a[80:83], v[224:227], v[160:163], a[80:83]\n"
    "v_exp_f32 v235, v147\n"
    "v_mfma_f32_16x16x32_bf16 a[84:87], v[224:227], v[164:167], a[84:87]\n"
    "v_add_f32 v236, v236, v234\n"
    "v_cvt_pk_bf16_f32 v179, v234, v235\n"
    "v_mfma_f32_16x16x32_bf16 a[88:91], v[224:227], v[168:171], a[88:91]\n"
    "v_add_f32 v236, v236, v235\n"
    "v_mfma_f32_16x16x32_bf16 a[92:95], v[224:227], v[172:175], a[92:95]\n"
    "s_waitcnt lgkmcnt(6)\n"
    "ds_read_b64_tr_b16 v[224:225], v245 offset:2560\n"
    "ds_read_b64_tr_b16 v[226:227], v245 offset:10752\n"
    "v_mfma_f32_16x16x32_bf16 a[96:99], v[220:223], v[160:163], a[96:99]\n"
    "v_exp_f32 v232, v148\n"
    "v_mfma_f32_16x16x32_bf16 a[100:103], v[220:223], v[164:167], a[100:103]\n"
    "v_exp_f32 v233, v149\n"
    "v_mfma_f32_16x16x32_bf16 a[104:107], v[220:223], v[168:171], a[104:107]\n"
    "v_add_f32 v237, v237, v232\n"
    "v_cvt_pk_bf16_f32 v182, v232, v233\n"
    "v_mfma_f32_16x16x32_bf16 a[108:111], v[220:223], v[172:175], a[108:111]\n"
    "s_waitcnt lgkmcnt(6)\n"
    "ds_read_b64_tr_b16 v[216:217], v246 offset:2560\n"
    "ds_read_b64_tr_b16 v[218:219], v246 offset:10752\n"
    "v_mfma_f32_16x16x32_bf16 a[112:115], v[208:211], v[160:163], a[112:115]\n"
    "v_add_f32 v237, v237, v233\n"
    "v_mfma_f32_16x16x32_bf16 a[116:119], v[208:211], v[164:167], a[116:119]\n"
    "v_exp_f32 v234, v150\n"
    "v_mfma_f32_16x16x32_bf16 a[120:123], v[208:211], v[168:171], a[120:123]\n"
    "v_exp_f32 v235, v151\n"
    "v_mfma_f32_16x16x32_bf16 a[124:127], v[208:211], v[172:175], a[124:127]\n"
    "s_waitcnt lgkmcnt(6)\n"
    "ds_read_b64_tr_b16 v[208:209], v245 offset:3072\n"
    "ds_read_b64_tr_b16 v[210:211], v245 offset:11264\n"
    "v_mfma_f32_16x16x32_bf16 a[128:131], v[212:215], v[160:163], a[128:131]\n"
    "v_add_f32 v237, v237, v234\n"
    "v_cvt_pk_bf16_f32 v183, v234, v235\n"
    "v_mfma_f32_16x16x32_bf16 a[132:135], v[212:215], v[164:167], a[132:135]\n"
    "v_add_f32 v237, v237, v235\n"
    "v_mfma_f32_16x16x32_bf16 a[136:139], v[212:215], v[168:171], a[136:139]\n"
    "v_exp_f32 v232, v152\n"
    "v_mfma_f32_16x16x32_bf16 a[140:143], v[212:215], v[172:175], a[140:143]\n"
    "s_waitcnt lgkmcnt(6)\n"
    "ds_read_b64_tr_b16 v[220:221], v246 offset:3072\n"
    "ds_read_b64_tr_b16 v[222:223], v246 offset:11264\n"
    "v_mfma_f32_16x16x32_bf16 a[144:147], v[228:231], v[160:163], a[144:147]\n"
    "v_exp_f32 v233, v153\n"
    "v_mfma_f32_16x16x32_bf16 a[148:151], v[228:231], v[164:167], a[148:151]\n"
    "v_add_f32 v238, v238, v232\n"
    "v_cvt_pk_bf16_f32 v186, v232, v233\n"
    "v_mfma_f32_16x16x32_bf16 a[152:155], v[228:231], v[168:171], a[152:155]\n"
    "v_add_f32 v238, v238, v233\n"
    "v_mfma_f32_16x16x32_bf16 a[156:159], v[228:231], v[172:175], a[156:159]\n"
    "s_waitcnt lgkmcnt(6)\n"
    "ds_read_b64_tr_b16 v[228:229], v245 offset:3584\n"
    "ds_read_b64_tr_b16 v[230:231], v245 offset:11776\n"
    "v_mfma_f32_16x16x32_bf16 a[160:163], v[224:227], v[160:163], a[160:163]\n"
    "v_exp_f32 v234, v154\n"
    "v_mfma_f32_16x16x32_bf16 a[164:167], v[224:227], v[164:167], a[164:167]\n"
    "v_exp_f32 v235, v155\n"
    "v_mfma_f32_16x16x32_bf16 a[168:171], v[224:227], v[168:171], a[168:171]\n"
    "v_add_f32 v238, v238, v234\n"
    "v_cvt_pk_bf16_f32 v187, v234, v235\n"
    "v_mfma_f32_16x16x32_bf16 a[172:175], v[224:227], v[172:175], a[172:175]\n"
    "s_waitcnt lgkmcnt(6)\n"
    "ds_read_b64_tr_b16 v[224:225], v246 offset:3584\n"
    "ds_read_b64_tr_b16 v[226:227], v246 offset:11776\n"
    "v_mfma_f32_16x16x32_bf16 a[176:179], v[216:219], v[160:163], a[176:179]\n"
    "v_add_f32 v238, v238, v235\n"
    "v_mfma_f32_16x16x32_bf16 a[180:183], v[216:219], v[164:167], a[180:183]\n"
    "v_exp_f32 v232, v156\n"
    "v_mfma_f32_16x16x32_bf16 a[184:187], v[216:219], v[168:171], a[184:187]\n"
    "v_exp_f32 v233, v157\n"
    "v_mfma_f32_16x16x32_bf16 a[188:191], v[216:219], v[172:175], a[188:191]\n"
    "s_waitcnt lgkmcnt(6)\n"
    "v_add_f32 v239, v239, v232\n"
    "v_cvt_pk_bf16_f32 v190, v232, v233\n"
    "v_mfma_f32_16x16x32_bf16 a[192:195], v[208:211], v[160:163], a[192:195]\n"
    "v_add_f32 v239, v239, v233\n"
    "v_mfma_f32_16x16x32_bf16 a[196:199], v[208:211], v[164:167], a[196:199]\n"
    "v_exp_f32 v234, v158\n"
    "v_mfma_f32_16x16x32_bf16 a[200:203], v[208:211], v[168:171], a[200:203]\n"
    "s_waitcnt lgkmcnt(0) vmcnt(1)\n"
    "s_barrier\n"
    "v_exp_f32 v235, v159\n"
    "v_mfma_f32_16x16x32_bf16 a[204:207], v[208:211], v[172:175], a[204:207]\n"
    "s_nop 0\n"
    "ds_read_b128 v[208:211], v244 offset:0\n"
    "ds_read_b128 v[212:215], v248 offset:512\n"
    "v_mfma_f32_16x16x32_bf16 a[208:211], v[220:223], v[160:163], a[208:211]\n"
    "buffer_load_dwordx4 v247, s[36:39], s44 offen offset:0 lds\n"
    "v_mfma_f32_16x16x32_bf16 a[212:215], v[220:223], v[164:167], a[212:215]\n"
    "v_add_f32 v239, v239, v234\n"
    "v_cvt_pk_bf16_f32 v191, v234, v235\n"
    "v_mfma_f32_16x16x32_bf16 a[216:219], v[220:223], v[168:171], a[216:219]\n"
    "v_add_f32 v239, v239, v235\n"
    "v_max3_f32 v232, v236, v237, v238\n"
    "v_mfma_f32_16x16x32_bf16 a[220:223], v[220:223], v[172:175], a[220:223]\n"
    "s_nop 0\n"
    "v_max_f32 v232, v232, v239\n"
    "v_cmp_nge_f32 vcc, 0x4e800000, v232\n"
    "v_mfma_f32_16x16x32_bf16 a[224:227], v[228:231], v[160:163], a[224:227]\n"
    "buffer_load_dwordx4 v247, s[36:39], s45 offen offset:1024 lds\n"
    "v_mfma_f32_16x16x32_bf16 a[228:231], v[228:231], v[164:167], a[228:231]\n"
    "s_nop 0\n"
    "ds_read_b128 v[216:219], v244 offset:1024\n"
    "ds_read_b128 v[220:223], v248 offset:1536\n"
    "v_mfma_f32_16x16x32_bf16 a[232:235], v[228:231], v[168:171], a[232:235]\n"
    "buffer_load_dwordx4 v247, s[36:39], s46 offen offset:2048 lds\n"
    "v_mfma_f32_16x16x32_bf16 a[236:239], v[228:231], v[172:175], a[236:239]\n"
    "s_nop 0\n"
    "v_mfma_f32_16x16x32_bf16 a[240:243], v[224:227], v[160:163], a[240:243]\n"
    "v_mfma_f32_16x16x32_bf16 a[244:247], v[224:227], v[164:167], a[244:247]\n"
    "v_mfma_f32_16x16x32_bf16 a[248:251], v[224:227], v[168:171], a[248:251]\n"
    "v_mfma_f32_16x16x32_bf16 a[252:255], v[224:227], v[172:175], a[252:255]\n"
    "s_cbranch_vccnz STUB1_m\n"
    "RET1_m:\n"
    "s_waitcnt lgkmcnt(2)\n"
    "s_branch LOOP_m\n"
    ".p2align 6\n"
    "LAST_m:\n"
    "v_mfma_f32_16x16x32_bf16 v[128:131], v[208:211], v[0:3], v[192:195]\n"
    "s_add_u32 m0, s53, 32768\n"
    "v_add_f32 v240, v240, v236\n"
    "v_add_f32 v241, v241, v237\n"
    "v_mfma_f32_16x16x32_bf16 v[128:131], v[212:215], v[4:7], v[128:131]\n"
    "s_nop 0\n"
    "ds_read_b128 v[224:227], v244 offset:34816\n"
    "ds_read_b128 v[228:231], v248 offset:35328\n"
    "v_mfma_f32_16x16x32_bf16 v[132:135], v[208:211], v[32:35], v[196:199]\n"
    "buffer_load_dwordx4 v247, s[40:43], s44 offen offset:0 lds\n"
    "v_mfma_f32_16x16x32_bf16 v[132:135], v[212:215], v[36:39], v[132:135]\n"
    "v_add_f32 v242, v242, v238\n"
    "v_add_f32 v243, v243, v239\n"
    "v_mfma_f32_16x16x32_bf16 v[136:139], v[208:211], v[64:67], v[200:203]\n"
    "v_mbcnt_lo_u32_b32 v251, -1, 0\n"
    "v_mbcnt_hi_u32_b32 v251, -1, v251\n"
    "v_mfma_f32_16x16x32_bf16 v[136:139], v[212:215], v[68:71], v[136:139]\n"
    "s_mov_b32 s11, 0\n"
    "s_mov_b32 s12, 6144\n"
    "s_mov_b32 s13, 12288\n"
    "v_mfma_f32_16x16x32_bf16 v[140:143], v[208:211], v[96:99], v[204:207]\n"
    "s_mov_b32 s16, 18432\n"
    "buffer_load_dwordx4 v247, s[40:43], s45 offen offset:1024 lds\n"
    "v_mfma_f32_16x16x32_bf16 v[140:143], v[212:215], v[100:103], v[140:143]\n"
    "s_waitcnt lgkmcnt(2)\n"
    "s_lshl_b32 s10, s30, 13\n"
    "s_add_u32 s10, s10, 98304\n"
    "v_mfma_f32_16x16x32_bf16 v[128:131], v[216:219], v[8:11], v[128:131]\n"
    "s_lshl_b32 s31, s30, 6\n"
    "v_bfe_u32 v249, v251, 2, 3\n"
    "v_add_u32 v249, s31, v249\n"
    "v_mfma_f32_16x16x32_bf16 v[128:131], v[220:223], v[12:15], v[128:131]\n"
    "s_nop 0\n"
    "ds_read_b128 v[208:211], v244 offset:35840\n"
    "ds_read_b128 v[212:215], v248 offset:36352\n"
    "v_mfma_f32_16x16x32_bf16 v[132:135], v[216:219], v[40:43], v[132:135]\n"
    "buffer_load_dwordx4 v247, s[40:43], s46 offen offset:2048 lds\n"
    "v_mfma_f32_16x16x32_bf16 v[132:135], v[220:223], v[44:47], v[132:135]\n"
    "v_mul_u32_u24 v253, 896, v249\n"
    "v_lshrrev_b32 v249, 5, v251\n"
    "v_mfma_f32_16x16x32_bf16 v[136:139], v[216:219], v[72:75], v[136:139]\n"
    "v_lshl_add_u32 v253, v249, 6, v253\n"
    "v_bfe_u32 v249, v251, 4, 1\n"
    "v_mfma_f32_16x16x32_bf16 v[136:139], v[220:223], v[76:79], v[136:139]\n"
    "v_mul_u32_u24 v249, 3, v249\n"
    "v_and_b32 v250, 3, v251\n"
    "v_mfma_f32_16x16x32_bf16 v[140:143], v[216:219], v[104:107], v[140:143]\n"
    "buffer_load_dwordx4 v247, s[40:43], s47 offen offset:3072 lds\n"
    "v_mfma_f32_16x16x32_bf16 v[140:143], v[220:223], v[108:111], v[140:143]\n"
    "s_waitcnt lgkmcnt(2)\n"
    "v_xor_b32 v249, v249, v250\n"
    "v_lshl_add_u32 v253, v249, 4, v253\n"
    "v_mfma_f32_16x16x32_bf16 v[128:131], v[224:227], v[16:19], v[128:131]\n"
    "s_add_u32 m0, s53, 36864\n"
    "s_mul_i32 s57, s28, 37\n"
    "s_lshr_b32 s57, s57, 8\n"
    "v_mfma_f32_16x16x32_bf16 v[128:131], v[228:231], v[20:23], v[128:131]\n"
    "s_mul_i32 s57, s57, 7\n"
    "ds_read_b128 v[216:219], v244 offset:40960\n"
    "ds_read_b128 v[220:223], v248 offset:41472\n"
    "v_mfma_f32_16x16x32_bf16 v[132:135], v[224:227], v[48:51], v[132:135]\n"
    "s_sub_u32 s58, s28, s57\n"
    "buffer_load_dwordx4 v247, s[40:43], s48 offen offset:0 lds\n"
    "v_mfma_f32_16x16x32_bf16 v[132:135], v[228:231], v[52:55], v[132:135]\n"
    "s_lshl_b32 s31, s58, 7\n"
    "v_add_u32 v253, s31, v253\n"
    "v_mfma_f32_16x16x32_bf16 v[136:139], v[224:227], v[80:83], v[136:139]\n"
    "s_cmp_eq_u32 s58, 6\n"
    "s_cselect_b32 s31, -6, 1\n"
    "v_mfma_f32_16x16x32_bf16 v[136:139], v[228:231], v[84:87], v[136:139]\n"
    "s_lshl_b32 s69, s31, 7\n"
    "v_bfe_u32 v249, v251, 2, 3\n"
    "v_mul_u32_u24 v252, 896, v249\n"
    "v_mfma_f32_16x16x32_bf16 v[140:143], v[224:227], v[112:115], v[140:143]\n"
    "buffer_load_dwordx4 v247, s[40:43], s49 offen offset:1024 lds\n"
    "v_mfma_f32_16x16x32_bf16 v[140:143], v[228:231], v[116:119], v[140:143]\n"
    "s_waitcnt lgkmcnt(2)\n"
    "v_lshrrev_b32 v249, 5, v251\n"
    "v_lshl_add_u32 v252, v249, 6, v252\n"
    "v_mfma_f32_16x16x32_bf16 v[128:131], v[208:211], v[24:27], v[128:131]\n"
    "v_bfe_u32 v249, v251, 4, 1\n"
    "v_mul_u32_u24 v249, 3, v249\n"
    "v_mfma_f32_16x16x32_bf16 v[128:131], v[212:215], v[28:31], v[128:131]\n"
    "s_nop 0\n"
    "ds_read_b128 v[224:227], v244 offset:41984\n"
    "ds_read_b128 v[228:231], v248 offset:42496\n"
    "v_mfma_f32_16x16x32_bf16 v[132:135], v[208:211], v[56:59], v[132:135]\n"
    "buffer_load_dwordx4 v247, s[40:43], s50 offen offset:2048 lds\n"
    "v_mfma_f32_16x16x32_bf16 v[132:135], v[212:215], v[60:63], v[132:135]\n"
    "v_and_b32 v250, 3, v251\n"
    "v_xor_b32 v249, v249, v250\n"
    "v_mfma_f32_16x16x32_bf16 v[136:139], v[208:211], v[88:91], v[136:139]\n"
    "v_lshl_add_u32 v252, v249, 4, v252\n"
    "v_mfma_f32_16x16x32_bf16 v[136:139], v[212:215], v[92:95], v[136:139]\n"
    "buffer_load_dwordx4 v247, s[40:43], s51 offen offset:3072 lds\n"
    "v_mfma_f32_16x16x32_bf16 v[140:143], v[208:211], v[120:123], v[140:143]\n"
    "s_mov_b32 s44, 0\n"
    "v_mfma_f32_16x16x32_bf16 v[140:143], v[212:215], v[124:127], v[140:143]\n"
    "s_waitcnt lgkmcnt(2)\n"
    "v_mfma_f32_16x16x32_bf16 v[144:147], v[216:219], v[0:3], v[192:195]\n"
    "s_mov_b32 s45, 6144\n"
    "v_mfma_f32_16x16x32_bf16 v[144:147], v[220:223], v[4:7], v[144:147]\n"
    "s_mov_b32 s46, 12288\n"
    "ds_read_b128 v[208:211], v244 offset:43008\n"
    "ds_read_b128 v[212:215], v248 offset:43520\n"
    "v_mfma_f32_16x16x32_bf16 v[148:151], v[216:219], v[32:35], v[196:199]\n"
    "s_mov_b32 s47, 18432\n"
    "v_exp_f32 v232, v128\n"
    "v_mfma_f32_16x16x32_bf16 v[148:151], v[220:223], v[36:39], v[148:151]\n"
    "s_mov_b32 s48, 28672\n"
    "v_exp_f32 v233, v129\n"
    "v_mfma_f32_16x16x32_bf16 v[152:155], v[216:219], v[64:67], v[200:203]\n"
    "s_mov_b32 s49, 34816\n"
    "v_add_f32 v236, v232, v233\n"
    "v_cvt_pk_bf16_f32 v176, v232, v233\n"
    "v_mfma_f32_16x16x32_bf16 v[152:155], v[220:223], v[68:71], v[152:155]\n"
    "s_mov_b32 s50, 40960\n"
    "v_exp_f32 v234, v130\n"
    "v_mfma_f32_16x16x32_bf16 v[156:159], v[216:219], v[96:99], v[204:207]\n"
    "s_mov_b32 s51, 47104\n"
    "v_exp_f32 v235, v131\n"
    "v_mfma_f32_16x16x32_bf16 v[156:159], v[220:223], v[100:103], v[156:159]\n"
    "s_waitcnt lgkmcnt(2)\n"
    "v_add_f32 v236, v236, v234\n"
    "v_cvt_pk_bf16_f32 v177, v234, v235\n"
    "v_mfma_f32_16x16x32_bf16 v[144:147], v[224:227], v[8:11], v[144:147]\n"
    "s_add_u32 m0, s52, 0\n"
    "v_add_f32 v236, v236, v235\n"
    "v_mfma_f32_16x16x32_bf16 v[144:147], v[228:231], v[12:15], v[144:147]\n"
    "s_nop 0\n"
    "ds_read_b128 v[216:219], v244 offset:44032\n"
    "ds_read_b128 v[220:223], v248 offset:44544\n"
    "v_mfma_f32_16x16x32_bf16 v[148:151], v[224:227], v[40:43], v[148:151]\n"
    "buffer_load_dwordx4 v253, s[72:75], s44 offen offset:0 lds\n"
    "v_mfma_f32_16x16x32_bf16 v[148:151], v[228:231], v[44:47], v[148:151]\n"
    "buffer_load_dwordx4 v253, s[72:75], s45 offen offset:1024 lds\n"
    "v_mfma_f32_16x16x32_bf16 v[152:155], v[224:227], v[72:75], v[152:155]\n"
    "buffer_load_dwordx4 v253, s[72:75], s46 offen offset:2048 lds\n"
    "v_mfma_f32_16x16x32_bf16 v[152:155], v[228:231], v[76:79], v[152:155]\n"
    "v_exp_f32 v232, v132\n"
    "v_mfma_f32_16x16x32_bf16 v[156:159], v[224:227], v[104:107], v[156:159]\n"
    "buffer_load_dwordx4 v253, s[72:75], s47 offen offset:3072 lds\n"
    "v_mfma_f32_16x16x32_bf16 v[156:159], v[228:231], v[108:111], v[156:159]\n"
    "s_waitcnt lgkmcnt(2)\n"
    "ds_read_b64_tr_b16 v[224:225], v245 offset:0\n"
    "ds_read_b64_tr_b16 v[226:227], v245 offset:8192\n"
    "v_mfma_f32_16x16x32_bf16 v[144:147], v[208:211], v[16:19], v[144:147]\n"
    "s_add_u32 m0, s52, 4096\n"
    "v_exp_f32 v233, v133\n"
    "v_mfma_f32_16x16x32_bf16 v[144:147], v[212:215], v[20:23], v[144:147]\n"
    "v_add_f32 v237, v232, v233\n"
    "v_cvt_pk_bf16_f32 v180, v232, v233\n"
    "v_mfma_f32_16x16x32_bf16 v[148:151], v[208:211], v[48:51], v[148:151]\n"
    "buffer_load_dwordx4 v253, s[72:75], s48 offen offset:0 lds\n"
    "v_mfma_f32_16x16x32_bf16 v[148:151], v[212:215], v[52:55], v[148:151]\n"
    "s_nop 0\n"
    "ds_read_b64_tr_b16 v[228:229], v246 offset:0\n"
    "ds_read_b64_tr_b16 v[230:231], v246 offset:8192\n"
    "v_mfma_f32_16x16x32_bf16 v[152:155], v[208:211], v[80:83], v[152:155]\n"
    "v_exp_f32 v234, v134\n"
    "v_mfma_f32_16x16x32_bf16 v[152:155], v[212:215], v[84:87], v[152:155]\n"
    "v_exp_f32 v235, v135\n"
    "v_mfma_f32_16x16x32_bf16 v[156:159], v[208:211], v[112:115], v[156:159]\n"
    "buffer_load_dwordx4 v253, s[72:75], s49 offen offset:1024 lds\n"
    "v_mfma_f32_16x16x32_bf16 v[156:159], v[212:215], v[116:119], v[156:159]\n"
    "s_waitcnt lgkmcnt(4)\n"
    "ds_read_b64_tr_b16 v[208:209], v245 offset:512\n"
    "ds_read_b64_tr_b16 v[210:211], v245 offset:8704\n"
    "v_mfma_f32_16x16x32_bf16 v[144:147], v[216:219], v[24:27], v[144:147]\n"
    "v_add_f32 v237, v237, v234\n"
    "v_cvt_pk_bf16_f32 v181, v234, v235\n"
    "v_mfma_f32_16x16x32_bf16 v[144:147], v[220:223], v[28:31], v[144:147]\n"
    "v_add_f32 v237, v237, v235\n"
    "v_mfma_f32_16x16x32_bf16 v[148:151], v[216:219], v[56:59], v[148:151]\n"
    "buffer_load_dwordx4 v253, s[72:75], s50 offen offset:2048 lds\n"
    "v_mfma_f32_16x16x32_bf16 v[148:151], v[220:223], v[60:63], v[148:151]\n"
    "s_nop 0\n"
    "ds_read_b64_tr_b16 v[212:213], v246 offset:512\n"
    "ds_read_b64_tr_b16 v[214:215], v246 offset:8704\n"
    "v_mfma_f32_16x16x32_bf16 v[152:155], v[216:219], v[88:91], v[152:155]\n"
    "v_exp_f32 v232, v136\n"
    "v_mfma_f32_16x16x32_bf16 v[152:155], v[220:223], v[92:95], v[152:155]\n"
    "v_exp_f32 v233, v137\n"
    "v_mfma_f32_16x16x32_bf16 v[156:159], v[216:219], v[120:123], v[156:159]\n"
    "buffer_load_dwordx4 v253, s[72:75], s51 offen offset:3072 lds\n"
    "v_mfma_f32_16x16x32_bf16 v[156:159], v[220:223], v[124:127], v[156:159]\n"
    "s_waitcnt lgkmcnt(6)\n"
    "ds_read_b64_tr_b16 v[216:217], v245 offset:1024\n"
    "ds_read_b64_tr_b16 v[218:219], v245 offset:9216\n"
    "v_mfma_f32_16x16x32_bf16 a[0:3], v[224:227], v[160:163], a[0:3]\n"
    "v_add_f32 v238, v232, v233\n"
    "v_cvt_pk_bf16_f32 v184, v232, v233\n"
    "v_mfma_f32_16x16x32_bf16 a[4:7], v[224:227], v[164:167], a[4:7]\n"
    "v_exp_f32 v234, v138\n"
    "v_mfma_f32_16x16x32_bf16 a[8:11], v[224:227], v[168:171], a[8:11]\n"
    "v_exp_f32 v235, v139\n"
    "v_mfma_f32_16x16x32_bf16 a[12:15], v[224:227], v[172:175], a[12:15]\n"
    "s_waitcnt lgkmcnt(6)\n"
    "ds_read_b64_tr_b16 v[224:225], v246 offset:1024\n"
    "ds_read_b64_tr_b16 v[226:227], v246 offset:9216\n"
    "v_mfma_f32_16x16x32_bf16 a[16:19], v[228:231], v[160:163], a[16:19]\n"
    "v_add_f32 v238, v238, v234\n"
    "v_cvt_pk_bf16_f32 v185, v234, v235\n"
    "v_mfma_f32_16x16x32_bf16 a[20:23], v[228:231], v[164:167], a[20:23]\n"
    "v_add_f32 v238, v238, v235\n"
    "v_mfma_f32_16x16x32_bf16 a[24:27], v[228:231], v[168:171], a[24:27]\n"
    "v_exp_f32 v232, v140\n"
    "v_mfma_f32_16x16x32_bf16 a[28:31], v[228:231], v[172:175], a[28:31]\n"
    "s_waitcnt lgkmcnt(6)\n"
    "ds_read_b64_tr_b16 v[220:221], v245 offset:1536\n"
    "ds_read_b64_tr_b16 v[222:223], v245 offset:9728\n"
    "v_mfma_f32_16x16x32_bf16 a[32:35], v[208:211], v[160:163], a[32:35]\n"
    "v_exp_f32 v233, v141\n"
    "v_mfma_f32_16x16x32_bf16 a[36:39], v[208:211], v[164:167], a[36:39]\n"
    "v_add_f32 v239, v232, v233\n"
    "v_cvt_pk_bf16_f32 v188, v232, v233\n"
    "v_mfma_f32_16x16x32_bf16 a[40:43], v[208:211], v[168:171], a[40:43]\n"
    "v_exp_f32 v234, v142\n"
    "v_mfma_f32_16x16x32_bf16 a[44:47], v[208:211], v[172:175], a[44:47]\n"
    "s_waitcnt lgkmcnt(6)\n"
    "ds_read_b64_tr_b16 v[208:209], v246 offset:1536\n"
    "ds_read_b64_tr_b16 v[210:211], v246 offset:9728\n"
    "v_mfma_f32_16x16x32_bf16 a[48:51], v[212:215], v[160:163], a[48:51]\n"
    "v_exp_f32 v235, v143\n"
    "v_mfma_f32_16x16x32_bf16 a[52:55], v[212:215], v[164:167], a[52:55]\n"
    "v_add_f32 v239, v239, v234\n"
    "v_cvt_pk_bf16_f32 v189, v234, v235\n"
    "v_mfma_f32_16x16x32_bf16 a[56:59], v[212:215], v[168:171], a[56:59]\n"
    "v_add_f32 v239, v239, v235\n"
    "v_mfma_f32_16x16x32_bf16 a[60:63], v[212:215], v[172:175], a[60:63]\n"
    "s_waitcnt lgkmcnt(6)\n"
    "ds_read_b64_tr_b16 v[212:213], v245 offset:2048\n"
    "ds_read_b64_tr_b16 v[214:215], v245 offset:10240\n"
    "v_mfma_f32_16x16x32_bf16 a[64:67], v[216:219], v[160:163], a[64:67]\n"
    "v_exp_f32 v232, v144\n"
    "v_mfma_f32_16x16x32_bf16 a[68:71], v[216:219], v[164:167], a[68:71]\n"
    "v_exp_f32 v233, v145\n"
    "v_mfma_f32_16x16x32_bf16 a[72:75], v[216:219], v[168:171], a[72:75]\n"
    "v_add_f32 v236, v236, v232\n"
    "v_cvt_pk_bf16_f32 v178, v232, v233\n"
    "v_mfma_f32_16x16x32_bf16 a[76:79], v[216:219], v[172:175], a[76:79]\n"
    "s_waitcnt lgkmcnt(6)\n"
    "ds_read_b64_tr_b16 v[228:229], v246 offset:2048\n"
    "ds_read_b64_tr_b16 v[230:231], v246 offset:10240\n"
    "v_mfma_f32_16x16x32_bf16 a[80:83], v[224:227], v[160:163], a[80:83]\n"
    "v_add_f32 v236, v236, v233\n"
    "v_mfma_f32_16x16x32_bf16 a[84:87], v[224:227], v[164:167], a[84:87]\n"
    "v_exp_f32 v234, v146\n"
    "v_mfma_f32_16x16x32_bf16 a[88:91], v[224:227], v[168:171], a[88:91]\n"
    "v_exp_f32 v235, v147\n"
    "v_mfma_f32_16x16x32_bf16 a[92:95], v[224:227], v[172:175], a[92:95]\n"
    "s_waitcnt lgkmcnt(6)\n"
    "ds_read_b64_tr_b16 v[224:225], v245 offset:2560\n"
    "ds_read_b64_tr_b16 v[226:227], v245 offset:10752\n"
    "v_mfma_f32_16x16x32_bf16 a[96:99], v[220:223], v[160:163], a[96:99]\n"
    "v_add_f32 v236, v236, v234\n"
    "v_cvt_pk_bf16_f32 v179, v234, v235\n"
    "v_mfma_f32_16x16x32_bf16 a[100:103], v[220:223], v[164:167], a[100:103]\n"
    "v_add_f32 v236, v236, v235\n"
    "v_mfma_f32_16x16x32_bf16 a[104:107], v[220:223], v[168:171], a[104:107]\n"
    "v_exp_f32 v232, v148\n"
    "v_mfma_f32_16x16x32_bf16 a[108:111], v[220:223], v[172:175], a[108:111]\n"
    "s_waitcnt lgkmcnt(6)\n"
    "ds_read_b64_tr_b16 v[216:217], v246 offset:2560\n"
    "ds_read_b64_tr_b16 v[218:219], v246 offset:10752\n"
    "v_mfma_f32_16x16x32_bf16 a[112:115], v[208:211], v[160:163], a[112:115]\n"
    "v_exp_f32 v233, v149\n"
    "v_mfma_f32_16x16x32_bf16 a[116:119], v[208:211], v[164:167], a[116:119]\n"
    "v_add_f32 v237, v237, v232\n"
    "v_cvt_pk_bf16_f32 v182, v232, v233\n"
    "v_mfma_f32_16x16x32_bf16 a[120:123], v[208:211], v[168:171], a[120:123]\n"
    "v_add_f32 v237, v237, v233\n"
    "v_mfma_f32_16x16x32_bf16 a[124:127], v[208:211], v[172:175], a[124:127]\n"
    "s_waitcnt lgkmcnt(6)\n"
    "ds_read_b64_tr_b16 v[208:209], v245 offset:3072\n"
    "ds_read_b64_tr_b16 v[210:211], v245 offset:11264\n"
    "v_mfma_f32_16x16x32_bf16 a[128:131], v[212:215], v[160:163], a[128:131]\n"
    "v_exp_f32 v234, v150\n"
    "v_mfma_f32_16x16x32_bf16 a[132:135], v[212:215], v[164:167], a[132:135]\n"
    "v_exp_f32 v235, v151\n"
    "v_mfma_f32_16x16x32_bf16 a[136:139], v[212:215], v[168:171], a[136:139]\n"
    "v_add_f32 v237, v237, v234\n"
    "v_cvt_pk_bf16_f32 v183, v234, v235\n"
    "v_mfma_f32_16x16x32_bf16 a[140:143], v[212:215], v[172:175], a[140:143]\n"
    "s_waitcnt lgkmcnt(6)\n"
    "ds_read_b64_tr_b16 v[220:221], v246 offset:3072\n"
    "ds_read_b64_tr_b16 v[222:223], v246 offset:11264\n"
    "v_mfma_f32_16x16x32_bf16 a[144:147], v[228:231], v[160:163], a[144:147]\n"
    "v_add_f32 v237, v237, v235\n"
    "v_mfma_f32_16x16x32_bf16 a[148:151], v[228:231], v[164:167], a[148:151]\n"
    "v_exp_f32 v232, v152\n"
    "v_mfma_f32_16x16x32_bf16 a[152:155], v[228:231], v[168:171], a[152:155]\n"
    "v_exp_f32 v233, v153\n"
    "v_mfma_f32_16x16x32_bf16 a[156:159], v[228:231], v[172:175], a[156:159]\n"
    "s_waitcnt lgkmcnt(6)\n"
    "ds_read_b64_tr_b16 v[228:229], v245 offset:3584\n"
    "ds_read_b64_tr_b16 v[230:231], v245 offset:11776\n"
    "v_mfma_f32_16x16x32_bf16 a[160:163], v[224:227], v[160:163], a[160:163]\n"
    "v_add_f32 v238, v238, v232\n"
    "v_cvt_pk_bf16_f32 v186, v232, v233\n"
    "v_mfma_f32_16x16x32_bf16 a[164:167], v[224:227], v[164:167], a[164:167]\n"
    "v_add_f32 v238, v238, v233\n"
    "v_mfma_f32_16x16x32_bf16 a[168:171], v[224:227], v[168:171], a[168:171]\n"
    "v_exp_f32 v234, v154\n"
    "v_mfma_f32_16x16x32_bf16 a[172:175], v[224:227], v[172:175], a[172:175]\n"
    "s_waitcnt lgkmcnt(6)\n"
    "ds_read_b64_tr_b16 v[224:225], v246 offset:3584\n"
    "ds_read_b64_tr_b16 v[226:227], v246 offset:11776\n"
    "v_mfma_f32_16x16x32_bf16 a[176:179], v[216:219], v[160:163], a[176:179]\n"
    "v_exp_f32 v235, v155\n"
    "v_mfma_f32_16x16x32_bf16 a[180:183], v[216:219], v[164:167], a[180:183]\n"
    "v_add_f32 v238, v238, v234\n"
    "v_cvt_pk_bf16_f32 v187, v234, v235\n"
    "v_mfma_f32_16x16x32_bf16 a[184:187], v[216:219], v[168:171], a[184:187]\n"
    "v_add_f32 v238, v238, v235\n"
    "v_mfma_f32_16x16x32_bf16 a[188:191], v[216:219], v[172:175], a[188:191]\n"
    "s_waitcnt lgkmcnt(6)\n"
    "v_exp_f32 v232, v156\n"
    "v_mfma_f32_16x16x32_bf16 a[192:195], v[208:211], v[160:163], a[192:195]\n"
    "v_exp_f32 v233, v157\n"
    "v_mfma_f32_16x16x32_bf16 a[196:199], v[208:211], v[164:167], a[196:199]\n"
    "v_add_f32 v239, v239, v232\n"
    "v_cvt_pk_bf16_f32 v190, v232, v233\n"
    "v_mfma_f32_16x16x32_bf16 a[200:203], v[208:211], v[168:171], a[200:203]\n"
    "s_waitcnt lgkmcnt(0) vmcnt(8)\n"
    "s_barrier\n"
    "v_add_f32 v239, v239, v233\n"
    "v_mfma_f32_16x16x32_bf16 a[204:207], v[208:211], v[172:175], a[204:207]\n"
    "s_nop 0\n"
    "v_add_u32 v253, s69, v253\n"
    "v_mfma_f32_16x16x32_bf16 a[208:211], v[220:223], v[160:163], a[208:211]\n"
    "s_add_u32 m0, s52, 32768\n"
    "v_exp_f32 v234, v158\n"
    "v_mfma_f32_16x16x32_bf16 a[212:215], v[220:223], v[164:167], a[212:215]\n"
    "buffer_load_dwordx4 v253, s[72:75], s44 offen offset:0 lds\n"
    "v_mfma_f32_16x16x32_bf16 a[216:219], v[220:223], v[168:171], a[216:219]\n"
    "v_exp_f32 v235, v159\n"
    "v_mfma_f32_16x16x32_bf16 a[220:223], v[220:223], v[172:175], a[220:223]\n"
    "s_nop 0\n"
    "v_add_f32 v239, v239, v234\n"
    "v_cvt_pk_bf16_f32 v191, v234, v235\n"
    "v_mfma_f32_16x16x32_bf16 a[224:227], v[228:231], v[160:163], a[224:227]\n"
    "buffer_load_dwordx4 v253, s[72:75], s45 offen offset:1024 lds\n"
    "v_mfma_f32_16x16x32_bf16 a[228:231], v[228:231], v[164:167], a[228:231]\n"
    "v_add_f32 v239, v239, v235\n"
    "v_max3_f32 v232, v236, v237, v238\n"
    "v_mfma_f32_16x16x32_bf16 a[232:235], v[228:231], v[168:171], a[232:235]\n"
    "v_max_f32 v232, v232, v239\n"
    "v_cmp_nge_f32 vcc, 0x4e800000, v232\n"
    "v_mfma_f32_16x16x32_bf16 a[236:239], v[228:231], v[172:175], a[236:239]\n"
    "s_nop 0\n"
    "buffer_load_dwordx4 v253, s[72:75], s46 offen offset:2048 lds\n"
    "v_mfma_f32_16x16x32_bf16 a[240:243], v[224:227], v[160:163], a[240:243]\n"
    "v_mfma_f32_16x16x32_bf16 a[244:247], v[224:227], v[164:167], a[244:247]\n"
    "v_mfma_f32_16x16x32_bf16 a[248:251], v[224:227], v[168:171], a[248:251]\n"
    "v_mfma_f32_16x16x32_bf16 a[252:255], v[224:227], v[172:175], a[252:255]\n"
    "s_cbranch_vccnz STUB1l_m\n"
    "RET1l_m:\n"
    "ds_read_b64_tr_b16 v[208:209], v245 offset:32768\n"
    "ds_read_b64_tr_b16 v[210:211], v245 offset:40960\n"
    "ds_read_b64_tr_b16 v[212:213], v246 offset:32768\n"
    "ds_read_b64_tr_b16 v[214:215], v246 offset:40960\n"
    "ds_read_b64_tr_b16 v[216:217], v245 offset:33280\n"
    "ds_read_b64_tr_b16 v[218:219], v245 offset:41472\n"
    "ds_read_b64_tr_b16 v[220:221], v246 offset:33280\n"
    "ds_read_b64_tr_b16 v[222:223], v246 offset:41472\n"
    "v_add_f32 v240, v240, v236\n"
    "v_add_f32 v241, v241, v237\n"
    "v_add_f32 v242, v242, v238\n"
    "v_add_f32 v243, v243, v239\n"
    "v_mov_b32 v232, v240\n"
    "v_mov_b32 v233, v241\n"
    "v_mov_b32 v234, v242\n"
    "v_mov_b32 v235, v243\n"
    "v_permlane32_swap_b32 v240, v232\n"
    "v_permlane32_swap_b32 v241, v233\n"
    "v_permlane32_swap_b32 v242, v234\n"
    "v_permlane32_swap_b32 v243, v235\n"
    "s_waitcnt lgkmcnt(6)\n"
    "v_mfma_f32_16x16x32_bf16 a[0:3], v[208:211], v[176:179], a[0:3]\n"
    "s_nop 0\n"
    "ds_read_b64_tr_b16 v[224:225], v245 offset:33792\n"
    "ds_read_b64_tr_b16 v[226:227], v245 offset:41984\n"
    "v_mfma_f32_16x16x32_bf16 a[4:7], v[208:211], v[180:183], a[4:7]\n"
    "buffer_load_dwordx4 v253, s[72:75], s47 offen offset:3072 lds\n"
    "v_mfma_f32_16x16x32_bf16 a[8:11], v[208:211], v[184:187], a[8:11]\n"
    "s_add_u32 m0, s52, 36864\n"
    "v_add_f32 v240, v240, v232\n"
    "v_add_f32 v241, v241, v233\n"
    "v_mfma_f32_16x16x32_bf16 a[12:15], v[208:211], v[188:191], a[12:15]\n"
    "s_waitcnt lgkmcnt(6)\n"
    "v_add_f32 v242, v242, v234\n"
    "v_add_f32 v243, v243, v235\n"
    "v_mfma_f32_16x16x32_bf16 a[16:19], v[212:215], v[176:179], a[16:19]\n"
    "s_nop 0\n"
    "ds_read_b64_tr_b16 v[228:229], v246 offset:33792\n"
    "ds_read_b64_tr_b16 v[230:231], v246 offset:41984\n"
    "v_mfma_f32_16x16x32_bf16 a[20:23], v[212:215], v[180:183], a[20:23]\n"
    "buffer_load_dwordx4 v253, s[72:75], s48 offen offset:0 lds\n"
    "v_mfma_f32_16x16x32_bf16 a[24:27], v[212:215], v[184:187], a[24:27]\n"
    "v_mov_b32 v232, v240\n"
    "v_mov_b32 v233, v241\n"
    "v_mfma_f32_16x16x32_bf16 a[28:31], v[212:215], v[188:191], a[28:31]\n"
    "s_waitcnt lgkmcnt(6)\n"
    "v_mov_b32 v234, v242\n"
    "v_mov_b32 v235, v243\n"
    "v_mfma_f32_16x16x32_bf16 a[32:35], v[216:219], v[176:179], a[32:35]\n"
    "s_nop 0\n"
    "ds_read_b64_tr_b16 v[208:209], v245 offset:34304\n"
    "ds_read_b64_tr_b16 v[210:211], v245 offset:42496\n"
    "v_mfma_f32_16x16x32_bf16 a[36:39], v[216:219], v[180:183], a[36:39]\n"
    "v_permlane16_swap_b32 v240, v232\n"
    "v_permlane16_swap_b32 v241, v233\n"
    "v_mfma_f32_16x16x32_bf16 a[40:43], v[216:219], v[184:187], a[40:43]\n"
    "buffer_load_dwordx4 v253, s[72:75], s49 offen offset:1024 lds\n"
    "v_mfma_f32_16x16x32_bf16 a[44:47], v[216:219], v[188:191], a[44:47]\n"
    "s_waitcnt lgkmcnt(6)\n"
    "v_permlane16_swap_b32 v242, v234\n"
    "v_permlane16_swap_b32 v243, v235\n"
    "v_mfma_f32_16x16x32_bf16 a[48:51], v[220:223], v[176:179], a[48:51]\n"
    "s_nop 0\n"
    "ds_read_b64_tr_b16 v[212:213], v246 offset:34304\n"
    "ds_read_b64_tr_b16 v[214:215], v246 offset:42496\n"
    "v_mfma_f32_16x16x32_bf16 a[52:55], v[220:223], v[180:183], a[52:55]\n"
    "v_add_f32 v240, v240, v232\n"
    "v_add_f32 v241, v241, v233\n"
    "v_mfma_f32_16x16x32_bf16 a[56:59], v[220:223], v[184:187], a[56:59]\n"
    "v_add_f32 v242, v242, v234\n"
    "v_add_f32 v243, v243, v235\n"
    "v_mfma_f32_16x16x32_bf16 a[60:63], v[220:223], v[188:191], a[60:63]\n"
    "s_waitcnt lgkmcnt(6)\n"
    "buffer_load_dwordx4 v253, s[72:75], s50 offen offset:2048 lds\n"
    "v_mfma_f32_16x16x32_bf16 a[64:67], v[224:227], v[176:179], a[64:67]\n"
    "s_nop 0\n"
    "ds_read_b64_tr_b16 v[216:217], v245 offset:34816\n"
    "ds_read_b64_tr_b16 v[218:219], v245 offset:43008\n"
    "v_mfma_f32_16x16x32_bf16 a[68:71], v[224:227], v[180:183], a[68:71]\n"
    "v_mbcnt_lo_u32_b32 v255, -1, 0\n"
    "v_mbcnt_hi_u32_b32 v255, -1, v255\n"
    "v_mfma_f32_16x16x32_bf16 a[72:75], v[224:227], v[184:187], a[72:75]\n"
    "s_mov_b32 s40, s62\n"
    "s_and_b32 s41, s63, 65535\n"
    "s_mov_b32 s42, 1835008\n"
    "v_mfma_f32_16x16x32_bf16 a[76:79], v[224:227], v[188:191], a[76:79]\n"
    "s_waitcnt lgkmcnt(6)\n"
    "s_mov_b32 s43, 131072\n"
    "v_and_b32 v130, 15, v255\n"
    "v_mfma_f32_16x16x32_bf16 a[80:83], v[228:231], v[176:179], a[80:83]\n"
    "s_nop 0\n"
    "ds_read_b64_tr_b16 v[220:221], v246 offset:34816\n"
    "ds_read_b64_tr_b16 v[222:223], v246 offset:43008\n"
    "v_mfma_f32_16x16x32_bf16 a[84:87], v[228:231], v[180:183], a[84:87]\n"
    "buffer_load_dwordx4 v253, s[72:75], s51 offen offset:3072 lds\n"
    "v_mfma_f32_16x16x32_bf16 a[88:91], v[228:231], v[184:187], a[88:91]\n"
    "s_add_u32 m0, s10, 32768\n"
    "v_lshlrev_b32 v130, 3, v130\n"
    "s_lshl_b32 s31, s61, 7\n"
    "v_mfma_f32_16x16x32_bf16 a[92:95], v[228:231], v[188:191], a[92:95]\n"
    "s_waitcnt lgkmcnt(6)\n"
    "s_add_u32 s31, s31, 4096\n"
    "v_add_u32 v130, s31, v130\n"
    "v_mfma_f32_16x16x32_bf16 a[96:99], v[208:211], v[176:179], a[96:99]\n"
    "s_nop 0\n"
    "ds_read_b64_tr_b16 v[224:225], v245 offset:35328\n"
    "ds_read_b64_tr_b16 v[226:227], v245 offset:43520\n"
    "v_mfma_f32_16x16x32_bf16 a[100:103], v[208:211], v[180:183], a[100:103]\n"
    "buffer_load_dwordx4 v252, s[76:79], s11 offen offset:0 lds\n"
    "v_mfma_f32_16x16x32_bf16 a[104:107], v[208:211], v[184:187], a[104:107]\n"
    "global_load_dwordx2 v[128:129], v130, s[20:21] sc1\n"
    "v_mov_b32 v193, v240\n"
    "v_mfma_f32_16x16x32_bf16 a[108:111], v[208:211], v[188:191], a[108:111]\n"
    "s_waitcnt lgkmcnt(6)\n"
    "v_mov_b32 v197, v241\n"
    "v_mov_b32 v201, v242\n"
    "v_mfma_f32_16x16x32_bf16 a[112:115], v[212:215], v[176:179], a[112:115]\n"
    "s_nop 0\n"
    "ds_read_b64_tr_b16 v[228:229], v246 offset:35328\n"
    "ds_read_b64_tr_b16 v[230:231], v246 offset:43520\n"
    "v_mfma_f32_16x16x32_bf16 a[116:119], v[212:215], v[180:183], a[116:119]\n"
    "v_mov_b32 v205, v243\n"
    "v_lshlrev_b32 v242, 4, v255\n"
    "v_mfma_f32_16x16x32_bf16 a[120:123], v[212:215], v[184:187], a[120:123]\n"
    "s_lshl_b32 s56, s30, 14\n"
    "buffer_load_dwordx4 v252, s[76:79], s12 offen offset:1024 lds\n"
    "v_mfma_f32_16x16x32_bf16 a[124:127], v[212:215], v[188:191], a[124:127]\n"
    "s_waitcnt lgkmcnt(6)\n"
    "s_add_u32 s56, s56, 65536\n"
    "s_xor_b32 s58, s56, 16384\n"
    "v_mfma_f32_16x16x32_bf16 a[128:131], v[216:219], v[176:179], a[128:131]\n"
    "s_nop 0\n"
    "ds_read_b64_tr_b16 v[208:209], v245 offset:35840\n"
    "ds_read_b64_tr_b16 v[210:211], v245 offset:44032\n"
    "v_mfma_f32_16x16x32_bf16 a[132:135], v[216:219], v[180:183], a[132:135]\n"
    "v_add_u32 v247, s58, v242\n"
    "v_add_u32 v254, s56, v242\n"
    "v_mfma_f32_16x16x32_bf16 a[136:139], v[216:219], v[184:187], a[136:139]\n"
    "v_lshlrev_b32 v131, 3, v255\n"
    "s_lshl_b32 s56, s30, 13\n"
    "v_mfma_f32_16x16x32_bf16 a[140:143], v[216:219], v[188:191], a[140:143]\n"
    "s_waitcnt lgkmcnt(6)\n"
    "buffer_load_dwordx4 v252, s[76:79], s13 offen offset:2048 lds\n"
    "v_mfma_f32_16x16x32_bf16 a[144:147], v[220:223], v[176:179], a[144:147]\n"
    "s_add_u32 s56, s56, 135168\n"
    "ds_read_b64_tr_b16 v[212:213], v246 offset:35840\n"
    "ds_read_b64_tr_b16 v[214:215], v246 offset:44032\n"
    "v_mfma_f32_16x16x32_bf16 a[148:151], v[220:223], v[180:183], a[148:151]\n"
    "s_xor_b32 s57, s56, 8192\n"
    "v_add_u32 v134, s57, v131\n"
    "v_add_u32 v131, s56, v131\n"
    "v_mfma_f32_16x16x32_bf16 a[152:155], v[220:223], v[184:187], a[152:155]\n"
    "ds_write_b64 v131, v[200:201] offset:0\n"
    "v_mfma_f32_16x16x32_bf16 a[156:159], v[220:223], v[188:191], a[156:159]\n"
    "s_waitcnt lgkmcnt(7)\n"
    "ds_write_b64 v131, v[204:205] offset:512\n"
    "v_mfma_f32_16x16x32_bf16 a[160:163], v[224:227], v[176:179], a[160:163]\n"
    "s_mul_i32 s31, s8, 224\n"
    "ds_read_b64_tr_b16 v[216:217], v245 offset:36352\n"
    "ds_read_b64_tr_b16 v[218:219], v245 offset:44544\n"
    "v_mfma_f32_16x16x32_bf16 a[164:167], v[224:227], v[180:183], a[164:167]\n"
    "s_mul_i32 s59, s30, 14\n"
    "buffer_load_dwordx4 v252, s[76:79], s16 offen offset:3072 lds\n"
    "v_mfma_f32_16x16x32_bf16 a[168:171], v[224:227], v[184:187], a[168:171]\n"
    "s_add_u32 s31, s31, s59\n"
    "v_add_u32 v252, 28672, v252\n"
    "s_lshl_b32 s92, s31, 10\n"
    "v_mfma_f32_16x16x32_bf16 a[172:175], v[224:227], v[188:191], a[172:175]\n"
    "s_waitcnt lgkmcnt(8)\n"
    "s_add_u32 s93, s92, 57344\n"
    "s_add_u32 s94, s92, 114688\n"
    "v_mfma_f32_16x16x32_bf16 a[176:179], v[228:231], v[176:179], a[176:179]\n"
    "s_add_u32 s95, s92, 172032\n"
    "ds_read_b64_tr_b16 v[220:221], v246 offset:36352\n"
    "ds_read_b64_tr_b16 v[222:223], v246 offset:44544\n"
    "v_mfma_f32_16x16x32_bf16 a[180:183], v[228:231], v[180:183], a[180:183]\n"
    "s_mov_b32 s86, 7340032\n"
    "s_cmp_lt_u32 %1, 224\n"
    "s_cselect_b32 s86, s86, 0\n"
    "v_mfma_f32_16x16x32_bf16 a[184:187], v[228:231], v[184:187], a[184:187]\n"
    "s_mul_i32 s57, s28, 37\n"
    "s_lshr_b32 s57, s57, 8\n"
    "s_mul_i32 s57, s57, 7\n"
    "v_mfma_f32_16x16x32_bf16 a[188:191], v[228:231], v[188:191], a[188:191]\n"
    "s_waitcnt lgkmcnt(8)\n"
    "s_sub_u32 s56, s28, s57\n"
    "s_lshl_b32 s64, s56, 16\n"
    "v_mfma_f32_16x16x32_bf16 a[192:195], v[208:211], v[176:179], a[192:195]\n"
    "s_add_u32 s56, s56, 1\n"
    "s_cmp_eq_u32 s56, 7\n"
    "s_cselect_b32 s56, 0, s56\n"
    "v_mfma_f32_16x16x32_bf16 a[196:199], v[208:211], v[180:183], a[196:199]\n"
    "s_lshl_b32 s65, s56, 16\n"
    "s_add_u32 s56, s56, 1\n"
    "s_cmp_eq_u32 s56, 7\n"
    "v_mfma_f32_16x16x32_bf16 a[200:203], v[208:211], v[184:187], a[200:203]\n"
    "s_cselect_b32 s56, 0, s56\n"
    "s_lshl_b32 s66, s56, 16\n"
    "s_add_u32 s56, s56, 1\n"
    "v_mfma_f32_16x16x32_bf16 a[204:207], v[208:211], v[188:191], a[204:207]\n"
    "s_waitcnt lgkmcnt(6)\n"
    "s_cmp_eq_u32 s56, 7\n"
    "s_cselect_b32 s56, 0, s56\n"
    "v_mfma_f32_16x16x32_bf16 a[208:211], v[212:215], v[176:179], a[208:211]\n"
    "s_lshl_b32 s67, s56, 16\n"
    "s_add_u32 s56, s56, 1\n"
    "s_cmp_eq_u32 s56, 7\n"
    "v_mfma_f32_16x16x32_bf16 a[212:215], v[212:215], v[180:183], a[212:215]\n"
    "s_cselect_b32 s56, 0, s56\n"
    "s_lshl_b32 s68, s56, 16\n"
    "s_add_u32 s56, s56, 1\n"
    "v_mfma_f32_16x16x32_bf16 a[216:219], v[212:215], v[184:187], a[216:219]\n"
    "s_cmp_eq_u32 s56, 7\n"
    "s_cselect_b32 s56, 0, s56\n"
    "s_lshl_b32 s53, s56, 16\n"
    "v_mfma_f32_16x16x32_bf16 a[220:223], v[212:215], v[188:191], a[220:223]\n"
    "s_waitcnt lgkmcnt(2)\n"
    "s_add_u32 s56, s56, 1\n"
    "s_cmp_eq_u32 s56, 7\n"
    "v_mfma_f32_16x16x32_bf16 a[224:227], v[216:219], v[176:179], a[224:227]\n"
    "s_cselect_b32 s56, 0, s56\n"
    "s_lshl_b32 s54, s56, 16\n"
    "s_sub_u32 s14, s66, s65\n"
    "v_mfma_f32_16x16x32_bf16 a[228:231], v[216:219], v[180:183], a[228:231]\n"
    "s_ashr_i32 s14, s14, 9\n"
    "s_sub_u32 s15, s67, s66\n"
    "s_ashr_i32 s15, s15, 9\n"
    "v_mfma_f32_16x16x32_bf16 a[232:235], v[216:219], v[184:187], a[232:235]\n"
    "s_sub_u32 s17, s68, s67\n"
    "s_ashr_i32 s17, s17, 9\n"
    "s_sub_u32 s98, s53, s68\n"
    "v_mfma_f32_16x16x32_bf16 a[236:239], v[216:219], v[188:191], a[236:239]\n"
    "s_waitcnt lgkmcnt(0)\n"
    "s_ashr_i32 s98, s98, 9\n"
    "s_sub_u32 s99, s54, s53\n"
    "v_mfma_f32_16x16x32_bf16 a[240:243], v[220:223], v[176:179], a[240:243]\n"
    "s_ashr_i32 s99, s99, 9\n"
    "v_mfma_f32_16x16x32_bf16 a[244:247], v[220:223], v[180:183], a[244:247]\n"
    "v_mfma_f32_16x16x32_bf16 a[248:251], v[220:223], v[184:187], a[248:251]\n"
    "v_mfma_f32_16x16x32_bf16 a[252:255], v[220:223], v[188:191], a[252:255]\n"
    "K1TAIL:\n"
    "s_waitcnt vmcnt(3)\n"
    "v_cmp_ne_u64 vcc, s[70:71], v[128:129]\n"
    "s_cbranch_vccnz SPINH\n"
    "SPINHD:\n"
    "s_waitcnt lgkmcnt(0)\n"
    "s_barrier\n"
    "ds_read_b64 v[216:217], v134 offset:0\n"
    "ds_read_b64 v[218:219], v134 offset:512\n"
    "v_lshrrev_b32 v232, 5, v255\n"
    "ds_write_b128 v254, a[8:11] offset:0\n"
    "v_lshlrev_b32 v245, 10, v232\n"
    "v_bfe_u32 v232, v255, 2, 3\n"
    "v_lshl_add_u32 v245, v232, 6, v245\n"
    "v_bfe_u32 v232, v255, 4, 1\n"
    "v_mul_u32_u24 v232, 3, v232\n"
    "v_bfe_u32 v233, v255, 1, 1\n"
    "ds_write_b128 v254, a[24:27] offset:1024\n"
    "buffer_load_dwordx4 a[8:11], v242, s[84:87], s92 offen offset:0 sc1\n"
    "v_xor_b32 v232, v232, v233\n"
    "v_lshl_add_u32 v245, v232, 4, v245\n"
    "v_and_b32 v232, 1, v255\n"
    "v_lshl_add_u32 v245, v232, 3, v245\n"
    "v_xor_b32 v246, 32, v245\n"
    "v_add_u32 v240, 65536, v245\n"
    "ds_write_b128 v254, a[40:43] offset:2048\n"
    "v_add_u32 v241, 65536, v246\n"
    "v_add_u32 v244, s10, v245\n"
    "v_add_u32 v251, s10, v246\n"
    "s_lshl_b32 s31, s29, 2\n"
    "s_add_u32 s31, s31, s30\n"
    "s_mul_i32 s31, s31, 448\n"
    "ds_write_b128 v254, a[56:59] offset:3072\n"
    "buffer_load_dwordx4 a[24:27], v242, s[84:87], s93 offen offset:0 sc1\n"
    "s_add_u32 s31, s31, s28\n"
    "s_lshl_b32 s31, s31, 10\n"
    "s_add_u32 s31, s31, 8388608\n"
    "s_add_u32 s88, s26, s31\n"
    "s_addc_u32 s89, s27, 0\n"
    "s_and_b32 s89, s89, 65535\n"
    "ds_write_b128 v254, a[72:75] offset:4096\n"
    "s_mov_b32 s90, 443392\n"
    "s_mov_b32 s91, 131072\n"
    "s_waitcnt lgkmcnt(5)\n"
    "v_min_f32 v228, v192, v216\n"
    "v_sub_f32 v192, v228, v192\n"
    "v_sub_f32 v216, v228, v216\n"
    "ds_write_b128 v254, a[88:91] offset:5120\n"
    "buffer_load_dwordx4 a[40:43], v242, s[84:87], s94 offen offset:0 sc1\n"
    "v_exp_f32 v192, v192\n"
    "v_exp_f32 v216, v216\n"
    "s_nop 0\n"
    "v_mul_f32 v193, v193, v192\n"
    "v_fma_f32 v193, v217, v216, v193\n"
    "v_rcp_f32 v193, v193\n"
    "ds_write_b128 v254, a[104:107] offset:6144\n"
    "s_nop 0\n"
    "v_mul_f32 v220, v192, v193\n"
    "v_mul_f32 v224, v216, v193\n"
    "v_min_f32 v229, v196, v218\n"
    "v_sub_f32 v196, v229, v196\n"
    "v_sub_f32 v218, v229, v218\n"
    "ds_write_b128 v254, a[120:123] offset:7168\n"
    "buffer_load_dwordx4 a[56:59], v242, s[84:87], s95 offen offset:0 sc1\n"
    "v_exp_f32 v196, v196\n"
    "v_exp_f32 v218, v218\n"
    "s_nop 0\n"
    "v_mul_f32 v197, v197, v196\n"
    "v_fma_f32 v197, v219, v218, v197\n"
    "v_rcp_f32 v197, v197\n"
    "ds_write_b128 v254, a[136:139] offset:8192\n"
    "s_nop 0\n"
    "v_mul_f32 v222, v196, v197\n"
    "v_mul_f32 v226, v218, v197\n"
    "v_mov_b32 v248, s70\n"
    "v_mov_b32 v249, s71\n"
    "s_mul_i32 s31, s29, 224\n"
    "ds_write_b128 v254, a[152:155] offset:9216\n"
    "buffer_load_dwordx4 a[72:75], v242, s[84:87], s92 offen offset:1024 sc1\n"
    "s_add_u32 s31, s31, s28\n"
    "s_lshl_b32 s31, s31, 3\n"
    "s_add_u32 s31, s31, 8192\n"
    "s_add_u32 s36, s20, s31\n"
    "s_addc_u32 s37, s21, 0\n"
    "s_and_b32 s37, s37, 65535\n"
    "ds_write_b128 v254, a[168:171] offset:10240\n"
    "s_mov_b32 s38, 1544\n"
    "s_mov_b32 s39, 131072\n"
    "v_and_b32 v232, 15, v255\n"
    "v_lshlrev_b32 v250, 3, v232\n"
    "s_mul_i32 s31, s61, 7\n"
    "s_add_u32 s31, s31, s9\n"
    "ds_write_b128 v254, a[184:187] offset:11264\n"
    "buffer_load_dwordx4 a[88:91], v242, s[84:87], s93 offen offset:1024 sc1\n"
    "s_lshl_b32 s31, s31, 8\n"
    "s_add_u32 s31, s31, 8192\n"
    "v_add_u32 v250, s31, v250\n"
    "s_lshl_b32 s31, s30, 6\n"
    "v_add_u32 v234, s31, v255\n"
    "v_lshlrev_b32 v234, 12, v234\n"
    "ds_write_b128 v254, a[200:203] offset:12288\n"
    "s_add_u32 s31, s9, 14\n"
    "v_sub_u32 v243, s31, v232\n"
    "v_mul_u32_u24 v233, 37, v243\n"
    "v_lshrrev_b32 v233, 8, v233\n"
    "v_mul_u32_u24 v233, 7, v233\n"
    "v_sub_u32 v243, v243, v233\n"
    "ds_write_b128 v254, a[216:219] offset:13312\n"
    "buffer_load_dwordx4 a[104:107], v242, s[84:87], s94 offen offset:1024 sc1\n"
    "v_add_u32 v243, 1, v243\n"
    "v_cmp_lt_u32 vcc, 13, v232\n"
    "s_nop 1\n"
    "v_cndmask_b32 v243, v243, v232, vcc\n"
    "s_lshl_b32 s58, s9, 2\n"
    "s_add_u32 s58, s58, s30\n"
    "ds_write_b128 v254, a[232:235] offset:14336\n"
    "s_mul_i32 s31, s8, 56\n"
    "s_add_u32 s31, s31, s58\n"
    "s_lshl_b32 s31, s31, 14\n"
    "s_add_u32 s31, s31, 8388608\n"
    "s_add_u32 s80, s26, s31\n"
    "s_addc_u32 s81, s27, 0\n"
    "ds_write_b128 v254, a[248:251] offset:15360\n"
    "buffer_load_dwordx4 a[120:123], v242, s[84:87], s95 offen offset:1024 sc1\n"
    "s_and_b32 s81, s81, 65535\n"
    "s_mov_b32 s82, 475136\n"
    "s_mov_b32 s83, 131072\n"
    "s_cmp_lt_u32 %1, 224\n"
    "s_cselect_b32 s82, s82, 0\n"
    "s_mul_i32 s57, s8, 57344\n"
    "s_lshl_b32 s58, s58, 5\n"
    "s_add_u32 s60, s57, s58\n"
    "s_add_u32 s61, s60, 14336\n"
    "s_add_u32 s62, s60, 28672\n"
    "s_add_u32 s63, s60, 43008\n"
    "s_add_u32 s31, s9, 1\n"
    "s_lshl_b32 s31, s31, 10\n"
    "s_mov_b32 s28, 0\n"
    "v_mov_b32 v230, v234\n"
    "s_waitcnt lgkmcnt(0)\n"
    "s_barrier\n"
    "ds_read_b128 v[112:115], v247 offset:0\n"
    "ds_read_b128 v[116:119], v247 offset:1024\n"
    "ds_read_b128 v[120:123], v247 offset:2048\n"
    "ds_read_b128 v[124:127], v247 offset:3072\n"
    "ds_read_b128 v[0:3], v247 offset:4096\n"
    "ds_read_b128 v[4:7], v247 offset:5120\n"
    "ds_read_b128 v[8:11], v247 offset:6144\n"
    "ds_read_b128 v[12:15], v247 offset:7168\n"
    "ds_read_b128 v[16:19], v247 offset:8192\n"
    "ds_read_b128 v[20:23], v247 offset:9216\n"
    "ds_read_b128 v[24:27], v247 offset:10240\n"
    "ds_read_b128 v[28:31], v247 offset:11264\n"
    "ds_read_b128 v[32:35], v247 offset:12288\n"
    "ds_read_b128 v[36:39], v247 offset:13312\n"
    "ds_read_b128 v[40:43], v247 offset:14336\n"
    "ds_read_b128 v[44:47], v247 offset:15360\n"
    "buffer_load_dwordx4 a[136:139], v242, s[84:87], s92 offen offset:2048 sc1\n"
    "buffer_load_dwordx4 a[152:155], v242, s[84:87], s93 offen offset:2048 sc1\n"
    "s_waitcnt lgkmcnt(0)\n"
    "s_barrier\n"
    "v_accvgpr_read_b32 v232, a0\n"
    "ds_write_b128 v254, a[12:15] offset:0\n"
    "v_accvgpr_read_b32 v233, a1\n"
    "v_accvgpr_read_b32 v234, a2\n"
    "v_accvgpr_read_b32 v235, a3\n"
    "v_pk_mul_f32 v[112:113], v[112:113], v[224:225] op_sel_hi:[1,0]\n"
    "v_pk_mul_f32 v[114:115], v[114:115], v[224:225] op_sel_hi:[1,0]\n"
    "v_pk_fma_f32 v[232:233], v[232:233], v[220:221], v[112:113] op_sel_hi:[1,0,1]\n"
    "v_pk_fma_f32 v[234:235], v[234:235], v[220:221], v[114:115] op_sel_hi:[1,0,1]\n"
    "ds_write_b128 v254, a[28:31] offset:1024\n"
    "buffer_load_dwordx4 a[168:171], v242, s[84:87], s94 offen offset:2048 sc1\n"
    "v_cvt_pk_bf16_f32 v112, v232, v233\n"
    "v_cvt_pk_bf16_f32 v113, v234, v235\n"
    "v_accvgpr_read_b32 v236, a16\n"
    "v_accvgpr_read_b32 v237, a17\n"
    "v_accvgpr_read_b32 v238, a18\n"
    "v_accvgpr_read_b32 v239, a19\n"
    "v_pk_mul_f32 v[116:117], v[116:117], v[224:225] op_sel_hi:[1,0]\n"
    "ds_write_b128 v254, a[44:47] offset:2048\n"
    "v_pk_mul_f32 v[118:119], v[118:119], v[224:225] op_sel_hi:[1,0]\n"
    "v_pk_fma_f32 v[236:237], v[236:237], v[220:221], v[116:117] op_sel_hi:[1,0,1]\n"
    "v_pk_fma_f32 v[238:239], v[238:239], v[220:221], v[118:119] op_sel_hi:[1,0,1]\n"
    "v_cvt_pk_bf16_f32 v114, v236, v237\n"
    "v_cvt_pk_bf16_f32 v115, v238, v239\n"
    "v_accvgpr_read_b32 v232, a32\n"
    "v_accvgpr_read_b32 v233, a33\n"
    "ds_write_b128 v254, a[60:63] offset:3072\n"
    "buffer_load_dwordx4 a[184:187], v242, s[84:87], s95 offen offset:2048 sc1\n"
    "v_accvgpr_read_b32 v234, a34\n"
    "v_accvgpr_read_b32 v235, a35\n"
    "v_pk_mul_f32 v[120:121], v[120:121], v[224:225] op_sel_hi:[1,0]\n"
    "v_pk_mul_f32 v[122:123], v[122:123], v[224:225] op_sel_hi:[1,0]\n"
    "v_pk_fma_f32 v[232:233], v[232:233], v[220:221], v[120:121] op_sel_hi:[1,0,1]\n"
    "v_pk_fma_f32 v[234:235], v[234:235], v[220:221], v[122:123] op_sel_hi:[1,0,1]\n"
    "v_cvt_pk_bf16_f32 v116, v232, v233\n"
    "ds_write_b128 v254, a[76:79] offset:4096\n"
    "v_cvt_pk_bf16_f32 v117, v234, v235\n"
    "v_accvgpr_read_b32 v236, a48\n"
    "v_accvgpr_read_b32 v237, a49\n"
    "v_accvgpr_read_b32 v238, a50\n"
    "v_accvgpr_read_b32 v239, a51\n"
    "v_pk_mul_f32 v[124:125], v[124:125], v[224:225] op_sel_hi:[1,0]\n"
    "v_pk_mul_f32 v[126:127], v[126:127], v[224:225] op_sel_hi:[1,0]\n"
    "ds_write_b128 v254, a[92:95] offset:5120\n"
    "buffer_load_dwordx4 a[200:203], v242, s[84:87], s92 offen offset:3072 sc1\n"
    "v_pk_fma_f32 v[236:237], v[236:237], v[220:221], v[124:125] op_sel_hi:[1,0,1]\n"
    "v_pk_fma_f32 v[238:239], v[238:239], v[220:221], v[126:127] op_sel_hi:[1,0,1]\n"
    "v_cvt_pk_bf16_f32 v118, v236, v237\n"
    "v_cvt_pk_bf16_f32 v119, v238, v239\n"
    "v_accvgpr_read_b32 v232, a64\n"
    "v_accvgpr_read_b32 v233, a65\n"
    "v_accvgpr_read_b32 v234, a66\n"
    "ds_write_b128 v254, a[108:111] offset:6144\n"
    "v_accvgpr_read_b32 v235, a67\n"
    "v_pk_mul_f32 v[0:1], v[0:1], v[224:225] op_sel_hi:[1,0]\n"
    "v_pk_mul_f32 v[2:3], v[2:3], v[224:225] op_sel_hi:[1,0]\n"
    "v_pk_fma_f32 v[232:233], v[232:233], v[220:221], v[0:1] op_sel_hi:[1,0,1]\n"
    "v_pk_fma_f32 v[234:235], v[234:235], v[220:221], v[2:3] op_sel_hi:[1,0,1]\n"
    "v_cvt_pk_bf16_f32 v120, v232, v233\n"
    "v_cvt_pk_bf16_f32 v121, v234, v235\n"
    "ds_write_b128 v254, a[124:127] offset:7168\n"
    "buffer_load_dwordx4 a[216:219], v242, s[84:87], s93 offen offset:3072 sc1\n"
    "v_accvgpr_read_b32 v236, a80\n"
    "v_accvgpr_read_b32 v237, a81\n"
    "v_accvgpr_read_b32 v238, a82\n"
    "v_accvgpr_read_b32 v239, a83\n"
    "v_pk_mul_f32 v[4:5], v[4:5], v[224:225] op_sel_hi:[1,0]\n"
    "v_pk_mul_f32 v[6:7], v[6:7], v[224:225] op_sel_hi:[1,0]\n"
    "v_pk_fma_f32 v[236:237], v[236:237], v[220:221], v[4:5] op_sel_hi:[1,0,1]\n"
    "ds_write_b128 v254, a[140:143] offset:8192\n"
    "v_pk_fma_f32 v[238:239], v[238:239], v[220:221], v[6:7] op_sel_hi:[1,0,1]\n"
    "v_cvt_pk_bf16_f32 v122, v236, v237\n"
    "v_cvt_pk_bf16_f32 v123, v238, v239\n"
    "v_accvgpr_read_b32 v232, a96\n"
    "v_accvgpr_read_b32 v233, a97\n"
    "v_accvgpr_read_b32 v234, a98\n"
    "v_accvgpr_read_b32 v235, a99\n"
    "ds_write_b128 v254, a[156:159] offset:9216\n"
    "buffer_load_dwordx4 a[232:235], v242, s[84:87], s94 offen offset:3072 sc1\n"
    "v_pk_mul_f32 v[8:9], v[8:9], v[224:225] op_sel_hi:[1,0]\n"
    "v_pk_mul_f32 v[10:11], v[10:11], v[224:225] op_sel_hi:[1,0]\n"
    "v_pk_fma_f32 v[232:233], v[232:233], v[220:221], v[8:9] op_sel_hi:[1,0,1]\n"
    "v_pk_fma_f32 v[234:235], v[234:235], v[220:221], v[10:11] op_sel_hi:[1,0,1]\n"
    "v_cvt_pk_bf16_f32 v124, v232, v233\n"
    "v_cvt_pk_bf16_f32 v125, v234, v235\n"
    "v_accvgpr_read_b32 v236, a112\n"
    "ds_write_b128 v254, a[172:175] offset:10240\n"
    "v_accvgpr_read_b32 v237, a113\n"
    "v_accvgpr_read_b32 v238, a114\n"
    "v_accvgpr_read_b32 v239, a115\n"
    "v_pk_mul_f32 v[12:13], v[12:13], v[224:225] op_sel_hi:[1,0]\n"
    "v_pk_mul_f32 v[14:15], v[14:15], v[224:225] op_sel_hi:[1,0]\n"
    "v_pk_fma_f32 v[236:237], v[236:237], v[220:221], v[12:13] op_sel_hi:[1,0,1]\n"
    "v_pk_fma_f32 v[238:239], v[238:239], v[220:221], v[14:15] op_sel_hi:[1,0,1]\n"
    "ds_write_b128 v254, a[188:191] offset:11264\n"
    "buffer_load_dwordx4 a[248:251], v242, s[84:87], s95 offen offset:3072 sc1\n"
    "s_add_u32 s92, s92, 4096\n"
    "s_add_u32 s93, s93, 4096\n"
    "s_add_u32 s94, s94, 4096\n"
    "s_add_u32 s95, s95, 4096\n"
    "v_cvt_pk_bf16_f32 v126, v236, v237\n"
    "v_cvt_pk_bf16_f32 v127, v238, v239\n"
    "v_accvgpr_read_b32 v232, a128\n"
    "v_accvgpr_read_b32 v233, a129\n"
    "v_accvgpr_read_b32 v234, a130\n"
    "v_accvgpr_read_b32 v235, a131\n"
    "v_pk_mul_f32 v[16:17], v[16:17], v[224:225] op_sel_hi:[1,0]\n"
    "ds_write_b128 v254, a[204:207] offset:12288\n"
    "v_pk_mul_f32 v[18:19], v[18:19], v[224:225] op_sel_hi:[1,0]\n"
    "v_pk_fma_f32 v[232:233], v[232:233], v[220:221], v[16:17] op_sel_hi:[1,0,1]\n"
    "v_pk_fma_f32 v[234:235], v[234:235], v[220:221], v[18:19] op_sel_hi:[1,0,1]\n"
    "v_cvt_pk_bf16_f32 v128, v232, v233\n"
    "v_cvt_pk_bf16_f32 v129, v234, v235\n"
    "v_accvgpr_read_b32 v236, a144\n"
    "v_accvgpr_read_b32 v237, a145\n"
    "ds_write_b128 v254, a[220:223] offset:13312\n"
    "buffer_load_dwordx4 a[12:15], v242, s[84:87], s92 offen offset:0 sc1\n"
    "v_accvgpr_read_b32 v238, a146\n"
    "v_accvgpr_read_b32 v239, a147\n"
    "v_pk_mul_f32 v[20:21], v[20:21], v[224:225] op_sel_hi:[1,0]\n"
    "v_pk_mul_f32 v[22:23], v[22:23], v[224:225] op_sel_hi:[1,0]\n"
    "v_pk_fma_f32 v[236:237], v[236:237], v[220:221], v[20:21] op_sel_hi:[1,0,1]\n"
    "v_pk_fma_f32 v[238:239], v[238:239], v[220:221], v[22:23] op_sel_hi:[1,0,1]\n"
    "v_cvt_pk_bf16_f32 v130, v236, v237\n"
    "ds_write_b128 v254, a[236:239] offset:14336\n"
    "v_cvt_pk_bf16_f32 v131, v238, v239\n"
    "v_accvgpr_read_b32 v232, a160\n"
    "v_accvgpr_read_b32 v233, a161\n"
    "v_accvgpr_read_b32 v234, a162\n"
    "v_accvgpr_read_b32 v235, a163\n"
    "v_pk_mul_f32 v[24:25], v[24:25], v[224:225] op_sel_hi:[1,0]\n"
    "v_pk_mul_f32 v[26:27], v[26:27], v[224:225] op_sel_hi:[1,0]\n"
    "ds_write_b128 v254, a[252:255] offset:15360\n"
    "buffer_load_dwordx4 a[28:31], v242, s[84:87], s93 offen offset:0 sc1\n"
    "v_pk_fma_f32 v[232:233], v[232:233], v[220:221], v[24:25] op_sel_hi:[1,0,1]\n"
    "v_pk_fma_f32 v[234:235], v[234:235], v[220:221], v[26:27] op_sel_hi:[1,0,1]\n"
    "v_cvt_pk_bf16_f32 v132, v232, v233\n"
    "v_cvt_pk_bf16_f32 v133, v234, v235\n"
    "v_accvgpr_read_b32 v236, a176\n"
    "v_accvgpr_read_b32 v237, a177\n"
    "v_accvgpr_read_b32 v238, a178\n"
    "v_accvgpr_read_b32 v239, a179\n"
    "v_pk_mul_f32 v[28:29], v[28:29], v[224:225] op_sel_hi:[1,0]\n"
    "v_pk_mul_f32 v[30:31], v[30:31], v[224:225] op_sel_hi:[1,0]\n"
    "v_pk_fma_f32 v[236:237], v[236:237], v[220:221], v[28:29] op_sel_hi:[1,0,1]\n"
    "v_pk_fma_f32 v[238:239], v[238:239], v[220:221], v[30:31] op_sel_hi:[1,0,1]\n"
    "v_cvt_pk_bf16_f32 v134, v236, v237\n"
    "v_cvt_pk_bf16_f32 v135, v238, v239\n"
    "s_waitcnt lgkmcnt(0)\n"
    "s_barrier\n"
    "ds_read_b128 v[48:51], v247 offset:0\n"
    "ds_read_b128 v[52:55], v247 offset:1024\n"
    "ds_read_b128 v[56:59], v247 offset:2048\n"
    "ds_read_b128 v[60:63], v247 offset:3072\n"
    "ds_read_b128 v[64:67], v247 offset:4096\n"
    "ds_read_b128 v[68:71], v247 offset:5120\n"
    "ds_read_b128 v[72:75], v247 offset:6144\n"
    "ds_read_b128 v[76:79], v247 offset:7168\n"
    "ds_read_b128 v[80:83], v247 offset:8192\n"
    "ds_read_b128 v[84:87], v247 offset:9216\n"
    "ds_read_b128 v[88:91], v247 offset:10240\n"
    "ds_read_b128 v[92:95], v247 offset:11264\n"
    "ds_read_b128 v[96:99], v247 offset:12288\n"
    "ds_read_b128 v[100:103], v247 offset:13312\n"
    "ds_read_b128 v[104:107], v247 offset:14336\n"
    "ds_read_b128 v[108:111], v247 offset:15360\n"
    "v_mov_b32 v247, v230\n"
    "v_accvgpr_read_b32 v232, a192\n"
    "v_accvgpr_read_b32 v233, a193\n"
    "v_accvgpr_read_b32 v234, a194\n"
    "buffer_load_dwordx4 a[44:47], v242, s[84:87], s94 offen offset:0 sc1\n"
    "v_accvgpr_read_b32 v235, a195\n"
    "v_pk_mul_f32 v[32:33], v[32:33], v[224:225] op_sel_hi:[1,0]\n"
    "v_pk_mul_f32 v[34:35], v[34:35], v[224:225] op_sel_hi:[1,0]\n"
    "v_pk_fma_f32 v[232:233], v[232:233], v[220:221], v[32:33] op_sel_hi:[1,0,1]\n"
    "v_pk_fma_f32 v[234:235], v[234:235], v[220:221], v[34:35] op_sel_hi:[1,0,1]\n"
    "v_cvt_pk_bf16_f32 v136, v232, v233\n"
    "v_cvt_pk_bf16_f32 v137, v234, v235\n"
    "v_accvgpr_read_b32 v236, a208\n"
    "v_accvgpr_read_b32 v237, a209\n"
    "v_accvgpr_read_b32 v238, a210\n"
    "v_accvgpr_read_b32 v239, a211\n"
    "v_pk_mul_f32 v[36:37], v[36:37], v[224:225] op_sel_hi:[1,0]\n"
    "v_pk_mul_f32 v[38:39], v[38:39], v[224:225] op_sel_hi:[1,0]\n"
    "v_pk_fma_f32 v[236:237], v[236:237], v[220:221], v[36:37] op_sel_hi:[1,0,1]\n"
    "buffer_load_dwordx4 a[60:63], v242, s[84:87], s95 offen offset:0 sc1\n"
    "v_pk_fma_f32 v[238:239], v[238:239], v[220:221], v[38:39] op_sel_hi:[1,0,1]\n"
    "v_cvt_pk_bf16_f32 v138, v236, v237\n"
    "v_cvt_pk_bf16_f32 v139, v238, v239\n"
    "v_accvgpr_read_b32 v232, a224\n"
    "v_accvgpr_read_b32 v233, a225\n"
    "v_accvgpr_read_b32 v234, a226\n"
    "v_accvgpr_read_b32 v235, a227\n"
    "v_pk_mul_f32 v[40:41], v[40:41], v[224:225] op_sel_hi:[1,0]\n"
    "v_pk_mul_f32 v[42:43], v[42:43], v[224:225] op_sel_hi:[1,0]\n"
    "v_pk_fma_f32 v[232:233], v[232:233], v[220:221], v[40:41] op_sel_hi:[1,0,1]\n"
    "v_pk_fma_f32 v[234:235], v[234:235], v[220:221], v[42:43] op_sel_hi:[1,0,1]\n"
    "v_cvt_pk_bf16_f32 v140, v232, v233\n"
    "v_cvt_pk_bf16_f32 v141, v234, v235\n"
    "v_accvgpr_read_b32 v236, a240\n"
    "v_accvgpr_read_b32 v237, a241\n"
    "v_accvgpr_read_b32 v238, a242\n"
    "v_accvgpr_read_b32 v239, a243\n"
    "v_pk_mul_f32 v[44:45], v[44:45], v[224:225] op_sel_hi:[1,0]\n"
    "v_pk_mul_f32 v[46:47], v[46:47], v[224:225] op_sel_hi:[1,0]\n"
    "v_pk_fma_f32 v[236:237], v[236:237], v[220:221], v[44:45] op_sel_hi:[1,0,1]\n"
    "v_pk_fma_f32 v[238:239], v[238:239], v[220:221], v[46:47] op_sel_hi:[1,0,1]\n"
    "v_cvt_pk_bf16_f32 v142, v236, v237\n"
    "v_cvt_pk_bf16_f32 v143, v238, v239\n"
    "s_waitcnt lgkmcnt(0)\n"
    "s_barrier\n"
    "v_accvgpr_read_b32 v232, a4\n"
    "s_add_u32 m0, s10, 36864\n"
    "s_nop 0\n"
    "buffer_load_dwordx4 v252, s[76:79], s11 offen offset:0 lds\n"
    "v_accvgpr_read_b32 v233, a5\n"
    "v_accvgpr_read_b32 v234, a6\n"
    "v_accvgpr_read_b32 v235, a7\n"
    "v_pk_mul_f32 v[48:49], v[48:49], v[226:227] op_sel_hi:[1,0]\n"
    "v_pk_mul_f32 v[50:51], v[50:51], v[226:227] op_sel_hi:[1,0]\n"
    "v_pk_fma_f32 v[232:233], v[232:233], v[222:223], v[48:49] op_sel_hi:[1,0,1]\n"
    "v_pk_fma_f32 v[234:235], v[234:235], v[222:223], v[50:51] op_sel_hi:[1,0,1]\n"
    "v_cvt_pk_bf16_f32 v144, v232, v233\n"
    "v_cvt_pk_bf16_f32 v145, v234, v235\n"
    "v_accvgpr_read_b32 v236, a20\n"
    "v_accvgpr_read_b32 v237, a21\n"
    "v_accvgpr_read_b32 v238, a22\n"
    "buffer_load_dwordx4 v252, s[76:79], s12 offen offset:1024 lds\n"
    "v_accvgpr_read_b32 v239, a23\n"
    "v_pk_mul_f32 v[52:53], v[52:53], v[226:227] op_sel_hi:[1,0]\n"
    "v_pk_mul_f32 v[54:55], v[54:55], v[226:227] op_sel_hi:[1,0]\n"
    "v_pk_fma_f32 v[236:237], v[236:237], v[222:223], v[52:53] op_sel_hi:[1,0,1]\n"
    "v_pk_fma_f32 v[238:239], v[238:239], v[222:223], v[54:55] op_sel_hi:[1,0,1]\n"
    "v_cvt_pk_bf16_f32 v146, v236, v237\n"
    "v_cvt_pk_bf16_f32 v147, v238, v239\n"
    "v_accvgpr_read_b32 v232, a36\n"
    "v_accvgpr_read_b32 v233, a37\n"
    "v_accvgpr_read_b32 v234, a38\n"
    "v_accvgpr_read_b32 v235, a39\n"
    "v_pk_mul_f32 v[56:57], v[56:57], v[226:227] op_sel_hi:[1,0]\n"
    "buffer_load_dwordx4 v252, s[76:79], s13 offen offset:2048 lds\n"
    "v_pk_mul_f32 v[58:59], v[58:59], v[226:227] op_sel_hi:[1,0]\n"
    "v_pk_fma_f32 v[232:233], v[232:233], v[222:223], v[56:57] op_sel_hi:[1,0,1]\n"
    "v_pk_fma_f32 v[234:235], v[234:235], v[222:223], v[58:59] op_sel_hi:[1,0,1]\n"
    "v_cvt_pk_bf16_f32 v148, v232, v233\n"
    "v_cvt_pk_bf16_f32 v149, v234, v235\n"
    "v_accvgpr_read_b32 v236, a52\n"
    "v_accvgpr_read_b32 v237, a53\n"
    "v_accvgpr_read_b32 v238, a54\n"
    "v_accvgpr_read_b32 v239, a55\n"
    "v_pk_mul_f32 v[60:61], v[60:61], v[226:227] op_sel_hi:[1,0]\n"
    "v_pk_mul_f32 v[62:63], v[62:63], v[226:227] op_sel_hi:[1,0]\n"
    "v_pk_fma_f32 v[236:237], v[236:237], v[222:223], v[60:61] op_sel_hi:[1,0,1]\n"
    "buffer_load_dwordx4 v252, s[76:79], s16 offen offset:3072 lds\n"
    "v_pk_fma_f32 v[238:239], v[238:239], v[222:223], v[62:63] op_sel_hi:[1,0,1]\n"
    "v_cvt_pk_bf16_f32 v150, v236, v237\n"
    "v_cvt_pk_bf16_f32 v151, v238, v239\n"
    "v_accvgpr_read_b32 v232, a68\n"
    "v_accvgpr_read_b32 v233, a69\n"
    "v_accvgpr_read_b32 v234, a70\n"
    "v_accvgpr_read_b32 v235, a71\n"
    "v_pk_mul_f32 v[64:65], v[64:65], v[226:227] op_sel_hi:[1,0]\n"
    "v_pk_mul_f32 v[66:67], v[66:67], v[226:227] op_sel_hi:[1,0]\n"
    "v_pk_fma_f32 v[232:233], v[232:233], v[222:223], v[64:65] op_sel_hi:[1,0,1]\n"
    "v_pk_fma_f32 v[234:235], v[234:235], v[222:223], v[66:67] op_sel_hi:[1,0,1]\n"
    "v_cvt_pk_bf16_f32 v152, v232, v233\n"
    "v_add_u32 v252, 28672, v252\n"
    "v_cvt_pk_bf16_f32 v153, v234, v235\n"
    "v_accvgpr_read_b32 v236, a84\n"
    "v_accvgpr_read_b32 v237, a85\n"
    "v_accvgpr_read_b32 v238, a86\n"
    "v_accvgpr_read_b32 v239, a87\n"
    "v_pk_mul_f32 v[68:69], v[68:69], v[226:227] op_sel_hi:[1,0]\n"
    "v_pk_mul_f32 v[70:71], v[70:71], v[226:227] op_sel_hi:[1,0]\n"
    "v_pk_fma_f32 v[236:237], v[236:237], v[222:223], v[68:69] op_sel_hi:[1,0,1]\n"
    "v_pk_fma_f32 v[238:239], v[238:239], v[222:223], v[70:71] op_sel_hi:[1,0,1]\n"
    "v_cvt_pk_bf16_f32 v154, v236, v237\n"
    "v_cvt_pk_bf16_f32 v155, v238, v239\n"
    "v_accvgpr_read_b32 v232, a100\n"
    "v_add_u32 v253, s14, v253\n"
    "v_accvgpr_read_b32 v233, a101\n"
    "v_accvgpr_read_b32 v234, a102\n"
    "v_accvgpr_read_b32 v235, a103\n"
    "v_pk_mul_f32 v[72:73], v[72:73], v[226:227] op_sel_hi:[1,0]\n"
    "v_pk_mul_f32 v[74:75], v[74:75], v[226:227] op_sel_hi:[1,0]\n"
    "v_pk_fma_f32 v[232:233], v[232:233], v[222:223], v[72:73] op_sel_hi:[1,0,1]\n"
    "v_pk_fma_f32 v[234:235], v[234:235], v[222:223], v[74:75] op_sel_hi:[1,0,1]\n"
    "v_cvt_pk_bf16_f32 v156, v232, v233\n"
    "v_cvt_pk_bf16_f32 v157, v234, v235\n"
    "v_accvgpr_read_b32 v236, a116\n"
    "v_accvgpr_read_b32 v237, a117\n"
    "v_accvgpr_read_b32 v238, a118\n"
    "s_add_u32 m0, s52, 65536\n"
    "s_nop 0\n"
    "buffer_load_dwordx4 v253, s[72:75], s44 offen offset:0 lds\n"
    "v_accvgpr_read_b32 v239, a119\n"
    "v_pk_mul_f32 v[76:77], v[76:77], v[226:227] op_sel_hi:[1,0]\n"
    "v_pk_mul_f32 v[78:79], v[78:79], v[226:227] op_sel_hi:[1,0]\n"
    "v_pk_fma_f32 v[236:237], v[236:237], v[222:223], v[76:77] op_sel_hi:[1,0,1]\n"
    "v_pk_fma_f32 v[238:239], v[238:239], v[222:223], v[78:79] op_sel_hi:[1,0,1]\n"
    "v_cvt_pk_bf16_f32 v158, v236, v237\n"
    "v_cvt_pk_bf16_f32 v159, v238, v239\n"
    "v_accvgpr_read_b32 v232, a132\n"
    "v_accvgpr_read_b32 v233, a133\n"
    "v_accvgpr_read_b32 v234, a134\n"
    "v_accvgpr_read_b32 v235, a135\n"
    "v_pk_mul_f32 v[80:81], v[80:81], v[226:227] op_sel_hi:[1,0]\n"
    "buffer_load_dwordx4 v253, s[72:75], s45 offen offset:1024 lds\n"
    "v_pk_mul_f32 v[82:83], v[82:83], v[226:227] op_sel_hi:[1,0]\n"
    "v_pk_fma_f32 v[232:233], v[232:233], v[222:223], v[80:81] op_sel_hi:[1,0,1]\n"
    "v_pk_fma_f32 v[234:235], v[234:235], v[222:223], v[82:83] op_sel_hi:[1,0,1]\n"
    "v_cvt_pk_bf16_f32 v160, v232, v233\n"
    "v_cvt_pk_bf16_f32 v161, v234, v235\n"
    "v_accvgpr_read_b32 v236, a148\n"
    "v_accvgpr_read_b32 v237, a149\n"
    "v_accvgpr_read_b32 v238, a150\n"
    "v_accvgpr_read_b32 v239, a151\n"
    "v_pk_mul_f32 v[84:85], v[84:85], v[226:227] op_sel_hi:[1,0]\n"
    "v_pk_mul_f32 v[86:87], v[86:87], v[226:227] op_sel_hi:[1,0]\n"
    "v_pk_fma_f32 v[236:237], v[236:237], v[222:223], v[84:85] op_sel_hi:[1,0,1]\n"
    "buffer_load_dwordx4 v253, s[72:75], s46 offen offset:2048 lds\n"
    "v_pk_fma_f32 v[238:239], v[238:239], v[222:223], v[86:87] op_sel_hi:[1,0,1]\n"
    "v_cvt_pk_bf16_f32 v162, v236, v237\n"
    "v_cvt_pk_bf16_f32 v163, v238, v239\n"
    "v_accvgpr_read_b32 v232, a164\n"
    "v_accvgpr_read_b32 v233, a165\n"
    "v_accvgpr_read_b32 v234, a166\n"
    "v_accvgpr_read_b32 v235, a167\n"
    "v_pk_mul_f32 v[88:89], v[88:89], v[226:227] op_sel_hi:[1,0]\n"
    "v_pk_mul_f32 v[90:91], v[90:91], v[226:227] op_sel_hi:[1,0]\n"
    "v_pk_fma_f32 v[232:233], v[232:233], v[222:223], v[88:89] op_sel_hi:[1,0,1]\n"
    "v_pk_fma_f32 v[234:235], v[234:235], v[222:223], v[90:91] op_sel_hi:[1,0,1]\n"
    "v_cvt_pk_bf16_f32 v164, v232, v233\n"
    "buffer_load_dwordx4 v253, s[72:75], s47 offen offset:3072 lds\n"
    "v_cvt_pk_bf16_f32 v165, v234, v235\n"
    "v_accvgpr_read_b32 v236, a180\n"
    "v_accvgpr_read_b32 v237, a181\n"
    "v_accvgpr_read_b32 v238, a182\n"
    "v_accvgpr_read_b32 v239, a183\n"
    "v_pk_mul_f32 v[92:93], v[92:93], v[226:227] op_sel_hi:[1,0]\n"
    "v_pk_mul_f32 v[94:95], v[94:95], v[226:227] op_sel_hi:[1,0]\n"
    "v_pk_fma_f32 v[236:237], v[236:237], v[222:223], v[92:93] op_sel_hi:[1,0,1]\n"
    "v_pk_fma_f32 v[238:239], v[238:239], v[222:223], v[94:95] op_sel_hi:[1,0,1]\n"
    "v_cvt_pk_bf16_f32 v166, v236, v237\n"
    "v_cvt_pk_bf16_f32 v167, v238, v239\n"
    "v_accvgpr_read_b32 v232, a196\n"
    "s_add_u32 m0, s52, 69632\n"
    "s_nop 0\n"
    "buffer_load_dwordx4 v253, s[72:75], s48 offen offset:0 lds\n"
    "v_accvgpr_read_b32 v233, a197\n"
    "v_accvgpr_read_b32 v234, a198\n"
    "v_accvgpr_read_b32 v235, a199\n"
    "v_pk_mul_f32 v[96:97], v[96:97], v[226:227] op_sel_hi:[1,0]\n"
    "v_pk_mul_f32 v[98:99], v[98:99], v[226:227] op_sel_hi:[1,0]\n"
    "v_pk_fma_f32 v[232:233], v[232:233], v[222:223], v[96:97] op_sel_hi:[1,0,1]\n"
    "v_pk_fma_f32 v[234:235], v[234:235], v[222:223], v[98:99] op_sel_hi:[1,0,1]\n"
    "v_cvt_pk_bf16_f32 v168, v232, v233\n"
    "v_cvt_pk_bf16_f32 v169, v234, v235\n"
    "v_accvgpr_read_b32 v236, a212\n"
    "v_accvgpr_read_b32 v237, a213\n"
    "v_accvgpr_read_b32 v238, a214\n"
    "ds_read_b64_tr_b16 v[0:1], v245 offset:0\n"
    "ds_read_b64_tr_b16 v[2:3], v245 offset:2048\n"
    "ds_read_b64_tr_b16 v[4:5], v245 offset:4096\n"
    "ds_read_b64_tr_b16 v[6:7], v245 offset:6144\n"
    "ds_read_b64_tr_b16 v[8:9], v245 offset:8192\n"
    "ds_read_b64_tr_b16 v[10:11], v245 offset:10240\n"
    "ds_read_b64_tr_b16 v[12:13], v245 offset:12288\n"
    "ds_read_b64_tr_b16 v[14:15], v245 offset:14336\n"
    "ds_read_b64_tr_b16 v[16:17], v245 offset:16384\n"
    "ds_read_b64_tr_b16 v[18:19], v245 offset:18432\n"
    "ds_read_b64_tr_b16 v[20:21], v245 offset:20480\n"
    "ds_read_b64_tr_b16 v[22:23], v245 offset:22528\n"
    "ds_read_b64_tr_b16 v[24:25], v245 offset:24576\n"
    "ds_read_b64_tr_b16 v[26:27], v245 offset:26624\n"
    "ds_read_b64_tr_b16 v[28:29], v245 offset:28672\n"
    "ds_read_b64_tr_b16 v[30:31], v245 offset:30720\n"
    "ds_read_b64_tr_b16 v[32:33], v246 offset:0\n"
    "ds_read_b64_tr_b16 v[34:35], v246 offset:2048\n"
    "buffer_load_dwordx4 v253, s[72:75], s49 offen offset:1024 lds\n"
    "v_accvgpr_read_b32 v239, a215\n"
    "v_pk_mul_f32 v[100:101], v[100:101], v[226:227] op_sel_hi:[1,0]\n"
    "v_pk_mul_f32 v[102:103], v[102:103], v[226:227] op_sel_hi:[1,0]\n"
    "v_pk_fma_f32 v[236:237], v[236:237], v[222:223], v[100:101] op_sel_hi:[1,0,1]\n"
    "v_pk_fma_f32 v[238:239], v[238:239], v[222:223], v[102:103] op_sel_hi:[1,0,1]\n"
    "v_cvt_pk_bf16_f32 v170, v236, v237\n"
    "v_cvt_pk_bf16_f32 v171, v238, v239\n"
    "v_accvgpr_read_b32 v232, a228\n"
    "v_accvgpr_read_b32 v233, a229\n"
    "v_accvgpr_read_b32 v234, a230\n"
    "v_accvgpr_read_b32 v235, a231\n"
    "v_pk_mul_f32 v[104:105], v[104:105], v[226:227] op_sel_hi:[1,0]\n"
    "buffer_load_dwordx4 v253, s[72:75], s50 offen offset:2048 lds\n"
    "v_pk_mul_f32 v[106:107], v[106:107], v[226:227] op_sel_hi:[1,0]\n"
    "v_pk_fma_f32 v[232:233], v[232:233], v[222:223], v[104:105] op_sel_hi:[1,0,1]\n"
    "v_pk_fma_f32 v[234:235], v[234:235], v[222:223], v[106:107] op_sel_hi:[1,0,1]\n"
    "v_cvt_pk_bf16_f32 v172, v232, v233\n"
    "v_cvt_pk_bf16_f32 v173, v234, v235\n"
    "v_accvgpr_read_b32 v236, a244\n"
    "v_accvgpr_read_b32 v237, a245\n"
    "v_accvgpr_read_b32 v238, a246\n"
    "v_accvgpr_read_b32 v239, a247\n"
    "v_pk_mul_f32 v[108:109], v[108:109], v[226:227] op_sel_hi:[1,0]\n"
    "v_pk_mul_f32 v[110:111], v[110:111], v[226:227] op_sel_hi:[1,0]\n"
    "v_pk_fma_f32 v[236:237], v[236:237], v[222:223], v[108:109] op_sel_hi:[1,0,1]\n"
    "buffer_load_dwordx4 v253, s[72:75], s51 offen offset:3072 lds\n"
    "v_pk_fma_f32 v[238:239], v[238:239], v[222:223], v[110:111] op_sel_hi:[1,0,1]\n"
    "v_cvt_pk_bf16_f32 v174, v236, v237\n"
    "v_cvt_pk_bf16_f32 v175, v238, v239\n"
    "v_mul_u32_u24 v228, 56, v242\n"
    "buffer_load_dwordx4 a[192:195], v228, s[40:43], s60 offen offset:0\n"
    "buffer_load_dwordx4 a[196:199], v228, s[40:43], s60 offen offset:16\n"
    "s_waitcnt lgkmcnt(14)\n"
    "v_mfma_f32_16x16x32_bf16 v[64:67], v[0:3], v[112:115], 0\n"
    "s_nop 0\n"
    "ds_read_b64_tr_b16 v[36:37], v246 offset:4096\n"
    "ds_read_b64_tr_b16 v[38:39], v246 offset:6144\n"
    "v_mfma_f32_16x16x32_bf16 v[64:67], v[4:7], v[116:119], v[64:67]\n"
    "s_waitcnt lgkmcnt(12)\n"
    "buffer_load_dwordx4 a[76:79], v242, s[84:87], s92 offen offset:1024 sc1\n"
    "v_mfma_f32_16x16x32_bf16 v[64:67], v[8:11], v[120:123], v[64:67]\n"
    "s_nop 0\n"
    "ds_read_b64_tr_b16 v[40:41], v246 offset:8192\n"
    "ds_read_b64_tr_b16 v[42:43], v246 offset:10240\n"
    "v_mfma_f32_16x16x32_bf16 v[64:67], v[12:15], v[124:127], v[64:67]\n"
    "s_waitcnt lgkmcnt(10)\n"
    "buffer_load_dwordx4 a[92:95], v242, s[84:87], s93 offen offset:1024 sc1\n"
    "v_mfma_f32_16x16x32_bf16 v[64:67], v[16:19], v[128:131], v[64:67]\n"
    "s_nop 0\n"
    "ds_read_b64_tr_b16 v[44:45], v246 offset:12288\n"
    "ds_read_b64_tr_b16 v[46:47], v246 offset:14336\n"
    "v_mfma_f32_16x16x32_bf16 v[64:67], v[20:23], v[132:135], v[64:67]\n"
    "s_waitcnt lgkmcnt(8)\n"
    "v_mfma_f32_16x16x32_bf16 v[64:67], v[24:27], v[136:139], v[64:67]\n"
    "s_nop 0\n"
    "ds_read_b64_tr_b16 v[48:49], v246 offset:16384\n"
    "ds_read_b64_tr_b16 v[50:51], v246 offset:18432\n"
    "v_mfma_f32_16x16x32_bf16 v[64:67], v[28:31], v[140:143], v[64:67]\n"
    "buffer_load_dwordx4 a[108:111], v242, s[84:87], s94 offen offset:1024 sc1\n"
    "v_mfma_f32_16x16x32_bf16 v[68:71], v[0:3], v[144:147], 0\n"
    "s_nop 0\n"
    "ds_read_b64_tr_b16 v[52:53], v246 offset:20480\n"
    "ds_read_b64_tr_b16 v[54:55], v246 offset:22528\n"
    "v_mfma_f32_16x16x32_bf16 v[68:71], v[4:7], v[148:151], v[68:71]\n"
    "v_mfma_f32_16x16x32_bf16 v[68:71], v[8:11], v[152:155], v[68:71]\n"
    "s_nop 0\n"
    "ds_read_b64_tr_b16 v[56:57], v246 offset:24576\n"
    "ds_read_b64_tr_b16 v[58:59], v246 offset:26624\n"
    "v_mfma_f32_16x16x32_bf16 v[68:71], v[12:15], v[156:159], v[68:71]\n"
    "buffer_load_dwordx4 a[124:127], v242, s[84:87], s95 offen offset:1024 sc1\n"
    "v_mfma_f32_16x16x32_bf16 v[68:71], v[16:19], v[160:163], v[68:71]\n"
    "s_nop 0\n"
    "ds_read_b64_tr_b16 v[60:61], v246 offset:28672\n"
    "ds_read_b64_tr_b16 v[62:63], v246 offset:30720\n"
    "v_mfma_f32_16x16x32_bf16 v[68:71], v[20:23], v[164:167], v[68:71]\n"
    "v_mfma_f32_16x16x32_bf16 v[68:71], v[24:27], v[168:171], v[68:71]\n"
    "s_nop 0\n"
    "ds_read_b64_tr_b16 v[0:1], v245 offset:512\n"
    "ds_read_b64_tr_b16 v[2:3], v245 offset:2560\n"
    "v_mfma_f32_16x16x32_bf16 v[68:71], v[28:31], v[172:175], v[68:71]\n"
    "s_waitcnt lgkmcnt(14)\n"
    "buffer_load_dwordx4 a[140:143], v242, s[84:87], s92 offen offset:2048 sc1\n"
    "v_mfma_f32_16x16x32_bf16 v[72:75], v[32:35], v[112:115], 0\n"
    "s_nop 0\n"
    "ds_read_b64_tr_b16 v[4:5], v245 offset:4608\n"
    "ds_read_b64_tr_b16 v[6:7], v245 offset:6656\n"
    "v_mfma_f32_16x16x32_bf16 v[72:75], v[36:39], v[116:119], v[72:75]\n"
    "s_waitcnt lgkmcnt(12)\n"
    "v_cvt_pk_bf16_f32 v80, v64, v65\n"
    "v_cvt_pk_bf16_f32 v81, v66, v67\n"
    "v_mfma_f32_16x16x32_bf16 v[72:75], v[40:43], v[120:123], v[72:75]\n"
    "s_nop 0\n"
    "ds_read_b64_tr_b16 v[8:9], v245 offset:8704\n"
    "ds_read_b64_tr_b16 v[10:11], v245 offset:10752\n"
    "v_mfma_f32_16x16x32_bf16 v[72:75], v[44:47], v[124:127], v[72:75]\n"
    "s_waitcnt lgkmcnt(10)\n"
    "buffer_load_dwordx4 a[156:159], v242, s[84:87], s93 offen offset:2048 sc1\n"
    "v_mfma_f32_16x16x32_bf16 v[72:75], v[48:51], v[128:131], v[72:75]\n"
    "s_nop 0\n"
    "ds_read_b64_tr_b16 v[12:13], v245 offset:12800\n"
    "ds_read_b64_tr_b16 v[14:15], v245 offset:14848\n"
    "v_mfma_f32_16x16x32_bf16 v[72:75], v[52:55], v[132:135], v[72:75]\n"
    "s_waitcnt lgkmcnt(8)\n"
    "v_mfma_f32_16x16x32_bf16 v[72:75], v[56:59], v[136:139], v[72:75]\n"
    "s_add_u32 s96, s64, 0\n"
    "ds_read_b64_tr_b16 v[16:17], v245 offset:16896\n"
    "ds_read_b64_tr_b16 v[18:19], v245 offset:18944\n"
    "v_mfma_f32_16x16x32_bf16 v[72:75], v[60:63], v[140:143], v[72:75]\n"
    "buffer_load_dwordx4 a[172:175], v242, s[84:87], s94 offen offset:2048 sc1\n"
    "v_mfma_f32_16x16x32_bf16 v[76:79], v[32:35], v[144:147], 0\n"
    "s_nop 0\n"
    "ds_read_b64_tr_b16 v[20:21], v245 offset:20992\n"
    "ds_read_b64_tr_b16 v[22:23], v245 offset:23040\n"
    "v_mfma_f32_16x16x32_bf16 v[76:79], v[36:39], v[148:151], v[76:79]\n"
    "v_cvt_pk_bf16_f32 v82, v68, v69\n"
    "v_cvt_pk_bf16_f32 v83, v70, v71\n"
    "v_mfma_f32_16x16x32_bf16 v[76:79], v[40:43], v[152:155], v[76:79]\n"
    "s_nop 0\n"
    "ds_read_b64_tr_b16 v[24:25], v245 offset:25088\n"
    "ds_read_b64_tr_b16 v[26:27], v245 offset:27136\n"
    "v_mfma_f32_16x16x32_bf16 v[76:79], v[44:47], v[156:159], v[76:79]\n"
    "buffer_store_dwordx4 v[80:83], v242, s[88:91], s96 offen offset:0 sc1\n"
    "v_mfma_f32_16x16x32_bf16 v[76:79], v[48:51], v[160:163], v[76:79]\n"
    "s_nop 0\n"
    "ds_read_b64_tr_b16 v[28:29], v245 offset:29184\n"
    "ds_read_b64_tr_b16 v[30:31], v245 offset:31232\n"
    "v_mfma_f32_16x16x32_bf16 v[76:79], v[52:55], v[164:167], v[76:79]\n"
    "buffer_load_dwordx4 a[188:191], v242, s[84:87], s95 offen offset:2048 sc1\n"
    "v_mfma_f32_16x16x32_bf16 v[76:79], v[56:59], v[168:171], v[76:79]\n"
    "s_nop 0\n"
    "ds_read_b64_tr_b16 v[32:33], v246 offset:512\n"
    "ds_read_b64_tr_b16 v[34:35], v246 offset:2560\n"
    "v_mfma_f32_16x16x32_bf16 v[76:79], v[60:63], v[172:175], v[76:79]\n"
    "s_waitcnt lgkmcnt(14)\n"
    "buffer_load_dwordx4 a[204:207], v242, s[84:87], s92 offen offset:3072 sc1\n"
    "v_mfma_f32_16x16x32_bf16 v[64:67], v[0:3], v[112:115], 0\n"
    "s_nop 0\n"
    "ds_read_b64_tr_b16 v[36:37], v246 offset:4608\n"
    "ds_read_b64_tr_b16 v[38:39], v246 offset:6656\n"
    "v_mfma_f32_16x16x32_bf16 v[64:67], v[4:7], v[116:119], v[64:67]\n"
    "s_waitcnt lgkmcnt(12)\n"
    "v_cvt_pk_bf16_f32 v84, v72, v73\n"
    "v_cvt_pk_bf16_f32 v85, v74, v75\n"
    "v_mfma_f32_16x16x32_bf16 v[64:67], v[8:11], v[120:123], v[64:67]\n"
    "s_nop 0\n"
    "ds_read_b64_tr_b16 v[40:41], v246 offset:8704\n"
    "ds_read_b64_tr_b16 v[42:43], v246 offset:10752\n"
    "v_mfma_f32_16x16x32_bf16 v[64:67], v[12:15], v[124:127], v[64:67]\n"
    "s_waitcnt lgkmcnt(10)\n"
    "buffer_load_dwordx4 a[220:223], v242, s[84:87], s93 offen offset:3072 sc1\n"
    "v_mfma_f32_16x16x32_bf16 v[64:67], v[16:19], v[128:131], v[64:67]\n"
    "s_nop 0\n"
    "ds_read_b64_tr_b16 v[44:45], v246 offset:12800\n"
    "ds_read_b64_tr_b16 v[46:47], v246 offset:14848\n"
    "v_mfma_f32_16x16x32_bf16 v[64:67], v[20:23], v[132:135], v[64:67]\n"
    "s_waitcnt lgkmcnt(8)\n"
    "v_mfma_f32_16x16x32_bf16 v[64:67], v[24:27], v[136:139], v[64:67]\n"
    "s_add_u32 s97, s64, 16384\n"
    "ds_read_b64_tr_b16 v[48:49], v246 offset:16896\n"
    "ds_read_b64_tr_b16 v[50:51], v246 offset:18944\n"
    "v_mfma_f32_16x16x32_bf16 v[64:67], v[28:31], v[140:143], v[64:67]\n"
    "buffer_load_dwordx4 a[236:239], v242, s[84:87], s94 offen offset:3072 sc1\n"
    "v_mfma_f32_16x16x32_bf16 v[68:71], v[0:3], v[144:147], 0\n"
    "s_nop 0\n"
    "ds_read_b64_tr_b16 v[52:53], v246 offset:20992\n"
    "ds_read_b64_tr_b16 v[54:55], v246 offset:23040\n"
    "v_mfma_f32_16x16x32_bf16 v[68:71], v[4:7], v[148:151], v[68:71]\n"
    "v_cvt_pk_bf16_f32 v86, v76, v77\n"
    "v_cvt_pk_bf16_f32 v87, v78, v79\n"
    "v_mfma_f32_16x16x32_bf16 v[68:71], v[8:11], v[152:155], v[68:71]\n"
    "s_nop 0\n"
    "ds_read_b64_tr_b16 v[56:57], v246 offset:25088\n"
    "ds_read_b64_tr_b16 v[58:59], v246 offset:27136\n"
    "v_mfma_f32_16x16x32_bf16 v[68:71], v[12:15], v[156:159], v[68:71]\n"
    "buffer_store_dwordx4 v[84:87], v242, s[88:91], s97 offen offset:0 sc1\n"
    "v_mfma_f32_16x16x32_bf16 v[68:71], v[16:19], v[160:163], v[68:71]\n"
    "s_nop 0\n"
    "ds_read_b64_tr_b16 v[60:61], v246 offset:29184\n"
    "ds_read_b64_tr_b16 v[62:63], v246 offset:31232\n"
    "v_mfma_f32_16x16x32_bf16 v[68:71], v[20:23], v[164:167], v[68:71]\n"
    "s_nop 0\n"
    "s_barrier\n"
    "buffer_load_dwordx4 a[252:255], v242, s[84:87], s95 offen offset:3072 sc1\n"
    "v_mfma_f32_16x16x32_bf16 v[68:71], v[24:27], v[168:171], v[68:71]\n"
    "s_add_u32 s92, s92, 4096\n"
    "ds_read_b64_tr_b16 v[0:1], v245 offset:32768\n"
    "ds_read_b64_tr_b16 v[2:3], v245 offset:34816\n"
    "v_mfma_f32_16x16x32_bf16 v[68:71], v[28:31], v[172:175], v[68:71]\n"
    "s_waitcnt lgkmcnt(14)\n"
    "v_mfma_f32_16x16x32_bf16 v[72:75], v[32:35], v[112:115], 0\n"
    "s_add_u32 s93, s93, 4096\n"
    "ds_read_b64_tr_b16 v[4:5], v245 offset:36864\n"
    "ds_read_b64_tr_b16 v[6:7], v245 offset:38912\n"
    "v_mfma_f32_16x16x32_bf16 v[72:75], v[36:39], v[116:119], v[72:75]\n"
    "s_waitcnt lgkmcnt(12)\n"
    "v_cvt_pk_bf16_f32 v80, v64, v65\n"
    "v_cvt_pk_bf16_f32 v81, v66, v67\n"
    "v_mfma_f32_16x16x32_bf16 v[72:75], v[40:43], v[120:123], v[72:75]\n"
    "s_add_u32 s94, s94, 4096\n"
    "ds_read_b64_tr_b16 v[8:9], v245 offset:40960\n"
    "ds_read_b64_tr_b16 v[10:11], v245 offset:43008\n"
    "v_mfma_f32_16x16x32_bf16 v[72:75], v[44:47], v[124:127], v[72:75]\n"
    "s_waitcnt lgkmcnt(10)\n"
    "v_mfma_f32_16x16x32_bf16 v[72:75], v[48:51], v[128:131], v[72:75]\n"
    "s_add_u32 s95, s95, 4096\n"
    "ds_read_b64_tr_b16 v[12:13], v245 offset:45056\n"
    "ds_read_b64_tr_b16 v[14:15], v245 offset:47104\n"
    "v_mfma_f32_16x16x32_bf16 v[72:75], v[52:55], v[132:135], v[72:75]\n"
    "s_waitcnt lgkmcnt(8)\n"
    "v_mfma_f32_16x16x32_bf16 v[72:75], v[56:59], v[136:139], v[72:75]\n"
    "s_add_u32 s96, s64, 32768\n"
    "ds_read_b64_tr_b16 v[16:17], v245 offset:49152\n"
    "ds_read_b64_tr_b16 v[18:19], v245 offset:51200\n"
    "v_mfma_f32_16x16x32_bf16 v[72:75], v[60:63], v[140:143], v[72:75]\n"
    "s_add_u32 m0, s10, 0\n"
    "v_mfma_f32_16x16x32_bf16 v[76:79], v[32:35], v[144:147], 0\n"
    "s_nop 0\n"
    "ds_read_b64_tr_b16 v[20:21], v245 offset:53248\n"
    "ds_read_b64_tr_b16 v[22:23], v245 offset:55296\n"
    "v_mfma_f32_16x16x32_bf16 v[76:79], v[36:39], v[148:151], v[76:79]\n"
    "buffer_load_dwordx4 v252, s[76:79], s11 offen offset:0 lds\n"
    "v_mfma_f32_16x16x32_bf16 v[76:79], v[40:43], v[152:155], v[76:79]\n"
    "s_nop 0\n"
    "ds_read_b64_tr_b16 v[24:25], v245 offset:57344\n"
    "ds_read_b64_tr_b16 v[26:27], v245 offset:59392\n"
    "v_mfma_f32_16x16x32_bf16 v[76:79], v[44:47], v[156:159], v[76:79]\n"
    "v_cvt_pk_bf16_f32 v82, v68, v69\n"
    "v_cvt_pk_bf16_f32 v83, v70, v71\n"
    "v_mfma_f32_16x16x32_bf16 v[76:79], v[48:51], v[160:163], v[76:79]\n"
    "s_nop 0\n"
    "ds_read_b64_tr_b16 v[28:29], v245 offset:61440\n"
    "ds_read_b64_tr_b16 v[30:31], v245 offset:63488\n"
    "v_mfma_f32_16x16x32_bf16 v[76:79], v[52:55], v[164:167], v[76:79]\n"
    "buffer_store_dwordx4 v[80:83], v242, s[88:91], s96 offen offset:0 sc1\n"
    "v_mfma_f32_16x16x32_bf16 v[76:79], v[56:59], v[168:171], v[76:79]\n"
    "s_nop 0\n"
    "ds_read_b64_tr_b16 v[32:33], v246 offset:32768\n"
    "ds_read_b64_tr_b16 v[34:35], v246 offset:34816\n"
    "v_mfma_f32_16x16x32_bf16 v[76:79], v[60:63], v[172:175], v[76:79]\n"
    "s_waitcnt lgkmcnt(14)\n"
    "buffer_load_dwordx4 v252, s[76:79], s12 offen offset:1024 lds\n"
    "v_mfma_f32_16x16x32_bf16 v[64:67], v[0:3], v[112:115], 0\n"
    "s_nop 0\n"
    "ds_read_b64_tr_b16 v[36:37], v246 offset:36864\n"
    "ds_read_b64_tr_b16 v[38:39], v246 offset:38912\n"
    "v_mfma_f32_16x16x32_bf16 v[64:67], v[4:7], v[116:119], v[64:67]\n"
    "s_waitcnt lgkmcnt(12)\n"
    "buffer_load_dwordx4 v252, s[76:79], s13 offen offset:2048 lds\n"
    "v_mfma_f32_16x16x32_bf16 v[64:67], v[8:11], v[120:123], v[64:67]\n"
    "s_nop 0\n"
    "ds_read_b64_tr_b16 v[40:41], v246 offset:40960\n"
    "ds_read_b64_tr_b16 v[42:43], v246 offset:43008\n"
    "v_mfma_f32_16x16x32_bf16 v[64:67], v[12:15], v[124:127], v[64:67]\n"
    "s_waitcnt lgkmcnt(10)\n"
    "v_cvt_pk_bf16_f32 v84, v72, v73\n"
    "v_cvt_pk_bf16_f32 v85, v74, v75\n"
    "v_mfma_f32_16x16x32_bf16 v[64:67], v[16:19], v[128:131], v[64:67]\n"
    "s_nop 0\n"
    "ds_read_b64_tr_b16 v[44:45], v246 offset:45056\n"
    "ds_read_b64_tr_b16 v[46:47], v246 offset:47104\n"
    "v_mfma_f32_16x16x32_bf16 v[64:67], v[20:23], v[132:135], v[64:67]\n"
    "s_waitcnt lgkmcnt(8)\n"
    "buffer_load_dwordx4 v252, s[76:79], s16 offen offset:3072 lds\n"
    "v_mfma_f32_16x16x32_bf16 v[64:67], v[24:27], v[136:139], v[64:67]\n"
    "s_add_u32 s97, s64, 49152\n"
    "ds_read_b64_tr_b16 v[48:49], v246 offset:49152\n"
    "ds_read_b64_tr_b16 v[50:51], v246 offset:51200\n"
    "v_mfma_f32_16x16x32_bf16 v[64:67], v[28:31], v[140:143], v[64:67]\n"
    "v_add_u32 v252, 28672, v252\n"
    "v_mfma_f32_16x16x32_bf16 v[68:71], v[0:3], v[144:147], 0\n"
    "s_add_u32 m0, s10, 4096\n"
    "ds_read_b64_tr_b16 v[52:53], v246 offset:53248\n"
    "ds_read_b64_tr_b16 v[54:55], v246 offset:55296\n"
    "v_mfma_f32_16x16x32_bf16 v[68:71], v[4:7], v[148:151], v[68:71]\n"
    "buffer_load_dwordx4 v252, s[76:79], s11 offen offset:0 lds\n"
    "v_mfma_f32_16x16x32_bf16 v[68:71], v[8:11], v[152:155], v[68:71]\n"
    "s_nop 0\n"
    "ds_read_b64_tr_b16 v[56:57], v246 offset:57344\n"
    "ds_read_b64_tr_b16 v[58:59], v246 offset:59392\n"
    "v_mfma_f32_16x16x32_bf16 v[68:71], v[12:15], v[156:159], v[68:71]\n"
    "v_cvt_pk_bf16_f32 v86, v76, v77\n"
    "v_cvt_pk_bf16_f32 v87, v78, v79\n"
    "v_mfma_f32_16x16x32_bf16 v[68:71], v[16:19], v[160:163], v[68:71]\n"
    "s_nop 0\n"
    "ds_read_b64_tr_b16 v[60:61], v246 offset:61440\n"
    "ds_read_b64_tr_b16 v[62:63], v246 offset:63488\n"
    "v_mfma_f32_16x16x32_bf16 v[68:71], v[20:23], v[164:167], v[68:71]\n"
    "buffer_store_dwordx4 v[84:87], v242, s[88:91], s97 offen offset:0 sc1\n"
    "v_mfma_f32_16x16x32_bf16 v[68:71], v[24:27], v[168:171], v[68:71]\n"
    "s_nop 0\n"
    "ds_read_b64_tr_b16 v[0:1], v245 offset:33280\n"
    "ds_read_b64_tr_b16 v[2:3], v245 offset:35328\n"
    "v_mfma_f32_16x16x32_bf16 v[68:71], v[28:31], v[172:175], v[68:71]\n"
    "s_waitcnt lgkmcnt(14)\n"
    "buffer_load_dwordx4 v252, s[76:79], s12 offen offset:1024 lds\n"
    "v_mfma_f32_16x16x32_bf16 v[72:75], v[32:35], v[112:115], 0\n"
    "s_nop 0\n"
    "ds_read_b64_tr_b16 v[4:5], v245 offset:37376\n"
    "ds_read_b64_tr_b16 v[6:7], v245 offset:39424\n"
    "v_mfma_f32_16x16x32_bf16 v[72:75], v[36:39], v[116:119], v[72:75]\n"
    "s_waitcnt lgkmcnt(12)\n"
    "buffer_load_dwordx4 v252, s[76:79], s13 offen offset:2048 lds\n"
    "v_mfma_f32_16x16x32_bf16 v[72:75], v[40:43], v[120:123], v[72:75]\n"
    "s_nop 0\n"
    "ds_read_b64_tr_b16 v[8:9], v245 offset:41472\n"
    "ds_read_b64_tr_b16 v[10:11], v245 offset:43520\n"
    "v_mfma_f32_16x16x32_bf16 v[72:75], v[44:47], v[124:127], v[72:75]\n"
    "s_waitcnt lgkmcnt(10)\n"
    "v_cvt_pk_bf16_f32 v80, v64, v65\n"
    "v_cvt_pk_bf16_f32 v81, v66, v67\n"
    "v_mfma_f32_16x16x32_bf16 v[72:75], v[48:51], v[128:131], v[72:75]\n"
    "s_nop 0\n"
    "ds_read_b64_tr_b16 v[12:13], v245 offset:45568\n"
    "ds_read_b64_tr_b16 v[14:15], v245 offset:47616\n"
    "v_mfma_f32_16x16x32_bf16 v[72:75], v[52:55], v[132:135], v[72:75]\n"
    "s_waitcnt lgkmcnt(8)\n"
    "buffer_load_dwordx4 v252, s[76:79], s16 offen offset:3072 lds\n"
    "v_mfma_f32_16x16x32_bf16 v[72:75], v[56:59], v[136:139], v[72:75]\n"
    "s_add_u32 s96, s65, 0\n"
    "ds_read_b64_tr_b16 v[16:17], v245 offset:49664\n"
    "ds_read_b64_tr_b16 v[18:19], v245 offset:51712\n"
    "v_mfma_f32_16x16x32_bf16 v[72:75], v[60:63], v[140:143], v[72:75]\n"
    "v_add_u32 v252, 28672, v252\n"
    "buffer_load_dwordx4 a[0:3], v242, s[84:87], s92 offen offset:0 sc1\n"
    "v_mfma_f32_16x16x32_bf16 v[76:79], v[32:35], v[144:147], 0\n"
    "s_nop 0\n"
    "ds_read_b64_tr_b16 v[20:21], v245 offset:53760\n"
    "ds_read_b64_tr_b16 v[22:23], v245 offset:55808\n"
    "v_mfma_f32_16x16x32_bf16 v[76:79], v[36:39], v[148:151], v[76:79]\n"
    "buffer_load_dwordx4 a[4:7], v242, s[84:87], s93 offen offset:0 sc1\n"
    "v_mfma_f32_16x16x32_bf16 v[76:79], v[40:43], v[152:155], v[76:79]\n"
    "s_nop 0\n"
    "ds_read_b64_tr_b16 v[24:25], v245 offset:57856\n"
    "ds_read_b64_tr_b16 v[26:27], v245 offset:59904\n"
    "v_mfma_f32_16x16x32_bf16 v[76:79], v[44:47], v[156:159], v[76:79]\n"
    "v_cvt_pk_bf16_f32 v82, v68, v69\n"
    "v_cvt_pk_bf16_f32 v83, v70, v71\n"
    "v_mfma_f32_16x16x32_bf16 v[76:79], v[48:51], v[160:163], v[76:79]\n"
    "s_nop 0\n"
    "ds_read_b64_tr_b16 v[28:29], v245 offset:61952\n"
    "ds_read_b64_tr_b16 v[30:31], v245 offset:64000\n"
    "v_mfma_f32_16x16x32_bf16 v[76:79], v[52:55], v[164:167], v[76:79]\n"
    "buffer_store_dwordx4 v[80:83], v242, s[88:91], s96 offen offset:0 sc1\n"
    "v_mfma_f32_16x16x32_bf16 v[76:79], v[56:59], v[168:171], v[76:79]\n"
    "s_nop 0\n"
    "ds_read_b64_tr_b16 v[32:33], v246 offset:33280\n"
    "ds_read_b64_tr_b16 v[34:35], v246 offset:35328\n"
    "v_mfma_f32_16x16x32_bf16 v[76:79], v[60:63], v[172:175], v[76:79]\n"
    "s_waitcnt lgkmcnt(14)\n"
    "buffer_load_dwordx4 a[16:19], v242, s[84:87], s94 offen offset:0 sc1\n"
    "v_mfma_f32_16x16x32_bf16 v[64:67], v[0:3], v[112:115], 0\n"
    "s_nop 0\n"
    "ds_read_b64_tr_b16 v[36:37], v246 offset:37376\n"
    "ds_read_b64_tr_b16 v[38:39], v246 offset:39424\n"
    "v_mfma_f32_16x16x32_bf16 v[64:67], v[4:7], v[116:119], v[64:67]\n"
    "s_waitcnt lgkmcnt(12)\n"
    "buffer_load_dwordx4 a[20:23], v242, s[84:87], s95 offen offset:0 sc1\n"
    "v_mfma_f32_16x16x32_bf16 v[64:67], v[8:11], v[120:123], v[64:67]\n"
    "s_nop 0\n"
    "ds_read_b64_tr_b16 v[40:41], v246 offset:41472\n"
    "ds_read_b64_tr_b16 v[42:43], v246 offset:43520\n"
    "v_mfma_f32_16x16x32_bf16 v[64:67], v[12:15], v[124:127], v[64:67]\n"
    "s_waitcnt lgkmcnt(10)\n"
    "v_cvt_pk_bf16_f32 v84, v72, v73\n"
    "v_cvt_pk_bf16_f32 v85, v74, v75\n"
    "v_mfma_f32_16x16x32_bf16 v[64:67], v[16:19], v[128:131], v[64:67]\n"
    "s_nop 0\n"
    "ds_read_b64_tr_b16 v[44:45], v246 offset:45568\n"
    "ds_read_b64_tr_b16 v[46:47], v246 offset:47616\n"
    "v_mfma_f32_16x16x32_bf16 v[64:67], v[20:23], v[132:135], v[64:67]\n"
    "s_waitcnt lgkmcnt(8)\n"
    "buffer_load_dwordx4 a[32:35], v242, s[84:87], s92 offen offset:1024 sc1\n"
    "v_mfma_f32_16x16x32_bf16 v[64:67], v[24:27], v[136:139], v[64:67]\n"
    "s_add_u32 s97, s65, 16384\n"
    "ds_read_b64_tr_b16 v[48:49], v246 offset:49664\n"
    "ds_read_b64_tr_b16 v[50:51], v246 offset:51712\n"
    "v_mfma_f32_16x16x32_bf16 v[64:67], v[28:31], v[140:143], v[64:67]\n"
    "buffer_load_dwordx4 a[36:39], v242, s[84:87], s93 offen offset:1024 sc1\n"
    "v_mfma_f32_16x16x32_bf16 v[68:71], v[0:3], v[144:147], 0\n"
    "s_nop 0\n"
    "ds_read_b64_tr_b16 v[52:53], v246 offset:53760\n"
    "ds_read_b64_tr_b16 v[54:55], v246 offset:55808\n"
    "v_mfma_f32_16x16x32_bf16 v[68:71], v[4:7], v[148:151], v[68:71]\n"
    "buffer_load_dwordx4 a[48:51], v242, s[84:87], s94 offen offset:1024 sc1\n"
    "v_mfma_f32_16x16x32_bf16 v[68:71], v[8:11], v[152:155], v[68:71]\n"
    "s_nop 0\n"
    "ds_read_b64_tr_b16 v[56:57], v246 offset:57856\n"
    "ds_read_b64_tr_b16 v[58:59], v246 offset:59904\n"
    "v_mfma_f32_16x16x32_bf16 v[68:71], v[12:15], v[156:159], v[68:71]\n"
    "v_cvt_pk_bf16_f32 v86, v76, v77\n"
    "v_cvt_pk_bf16_f32 v87, v78, v79\n"
    "v_mfma_f32_16x16x32_bf16 v[68:71], v[16:19], v[160:163], v[68:71]\n"
    "s_nop 0\n"
    "ds_read_b64_tr_b16 v[60:61], v246 offset:61952\n"
    "ds_read_b64_tr_b16 v[62:63], v246 offset:64000\n"
    "v_mfma_f32_16x16x32_bf16 v[68:71], v[20:23], v[164:167], v[68:71]\n"
    "buffer_store_dwordx4 v[84:87], v242, s[88:91], s97 offen offset:0 sc1\n"
    "v_mfma_f32_16x16x32_bf16 v[68:71], v[24:27], v[168:171], v[68:71]\n"
    "buffer_load_dwordx4 a[52:55], v242, s[84:87], s95 offen offset:1024 sc1\n"
    "v_mfma_f32_16x16x32_bf16 v[68:71], v[28:31], v[172:175], v[68:71]\n"
    "s_waitcnt lgkmcnt(12)\n"
    "v_mfma_f32_16x16x32_bf16 v[72:75], v[32:35], v[112:115], 0\n"
    "s_lshr_b32 s18, s64, 8\n"
    "v_cvt_pk_bf16_f32 v80, v64, v65\n"
    "v_cvt_pk_bf16_f32 v81, v66, v67\n"
    "v_mfma_f32_16x16x32_bf16 v[72:75], v[36:39], v[116:119], v[72:75]\n"
    "s_waitcnt lgkmcnt(8)\n"
    "v_mfma_f32_16x16x32_bf16 v[72:75], v[40:43], v[120:123], v[72:75]\n"
    "s_waitcnt vmcnt(44)\n"
    "v_mfma_f32_16x16x32_bf16 v[72:75], v[44:47], v[124:127], v[72:75]\n"
    "s_waitcnt lgkmcnt(4)\n"
    "ds_read_b64_tr_b16 v[88:89], v244 offset:32768\n"
    "ds_read_b64_tr_b16 v[90:91], v244 offset:34816\n"
    "v_mfma_f32_16x16x32_bf16 v[72:75], v[48:51], v[128:131], v[72:75]\n"
    "s_nop 0\n"
    "ds_read_b64_tr_b16 v[92:93], v244 offset:36864\n"
    "ds_read_b64_tr_b16 v[94:95], v244 offset:38912\n"
    "v_mfma_f32_16x16x32_bf16 v[72:75], v[52:55], v[132:135], v[72:75]\n"
    "s_waitcnt lgkmcnt(4)\n"
    "v_mfma_f32_16x16x32_bf16 v[72:75], v[56:59], v[136:139], v[72:75]\n"
    "s_add_u32 s96, s65, 32768\n"
    "v_mfma_f32_16x16x32_bf16 v[72:75], v[60:63], v[140:143], v[72:75]\n"
    "v_mfma_f32_16x16x32_bf16 v[76:79], v[32:35], v[144:147], 0\n"
    "v_cvt_pk_bf16_f32 v82, v68, v69\n"
    "v_cvt_pk_bf16_f32 v83, v70, v71\n"
    "v_mfma_f32_16x16x32_bf16 v[76:79], v[36:39], v[148:151], v[76:79]\n"
    "buffer_store_dwordx4 v[80:83], v242, s[88:91], s96 offen offset:0 sc1\n"
    "v_mfma_f32_16x16x32_bf16 v[76:79], v[40:43], v[152:155], v[76:79]\n"
    "v_mfma_f32_16x16x32_bf16 v[76:79], v[44:47], v[156:159], v[76:79]\n"
    "s_nop 0\n"
    "ds_read_b64_tr_b16 v[96:97], v251 offset:32768\n"
    "ds_read_b64_tr_b16 v[98:99], v251 offset:34816\n"
    "v_mfma_f32_16x16x32_bf16 v[76:79], v[48:51], v[160:163], v[76:79]\n"
    "s_nop 0\n"
    "ds_read_b64_tr_b16 v[100:101], v251 offset:36864\n"
    "ds_read_b64_tr_b16 v[102:103], v251 offset:38912\n"
    "v_mfma_f32_16x16x32_bf16 v[76:79], v[52:55], v[164:167], v[76:79]\n"
    "v_mfma_f32_16x16x32_bf16 v[76:79], v[56:59], v[168:171], v[76:79]\n"
    "v_mfma_f32_16x16x32_bf16 v[76:79], v[60:63], v[172:175], v[76:79]\n"
    "s_waitcnt lgkmcnt(4)\n"
    "v_mfma_f32_16x16x32_bf16 v[176:179], v[88:91], a[8:11], 0\n"
    "v_cvt_pk_bf16_f32 v84, v72, v73\n"
    "v_cvt_pk_bf16_f32 v85, v74, v75\n"
    "v_mfma_f32_16x16x32_bf16 v[176:179], v[92:95], a[72:75], v[176:179]\n"
    "v_mfma_f32_16x16x32_bf16 v[180:183], v[88:91], a[24:27], 0\n"
    "v_mfma_f32_16x16x32_bf16 v[180:183], v[92:95], a[88:91], v[180:183]\n"
    "s_nop 0\n"
    "ds_read_b64_tr_b16 v[104:105], v244 offset:33280\n"
    "ds_read_b64_tr_b16 v[106:107], v244 offset:35328\n"
    "v_mfma_f32_16x16x32_bf16 v[184:187], v[88:91], a[40:43], 0\n"
    "s_nop 0\n"
    "ds_read_b64_tr_b16 v[108:109], v244 offset:37376\n"
    "ds_read_b64_tr_b16 v[110:111], v244 offset:39424\n"
    "v_mfma_f32_16x16x32_bf16 v[184:187], v[92:95], a[104:107], v[184:187]\n"
    "v_mfma_f32_16x16x32_bf16 v[188:191], v[88:91], a[56:59], 0\n"
    "s_add_u32 s97, s65, 49152\n"
    "v_mfma_f32_16x16x32_bf16 v[188:191], v[92:95], a[120:123], v[188:191]\n"
    "s_waitcnt lgkmcnt(4)\n"
    "v_mfma_f32_16x16x32_bf16 v[192:195], v[96:99], a[8:11], 0\n"
    "v_cvt_pk_bf16_f32 v86, v76, v77\n"
    "v_cvt_pk_bf16_f32 v87, v78, v79\n"
    "v_mfma_f32_16x16x32_bf16 v[192:195], v[100:103], a[72:75], v[192:195]\n"
    "buffer_store_dwordx4 v[84:87], v242, s[88:91], s97 offen offset:0 sc1\n"
    "v_mfma_f32_16x16x32_bf16 v[196:199], v[96:99], a[24:27], 0\n"
    "v_mfma_f32_16x16x32_bf16 v[196:199], v[100:103], a[88:91], v[196:199]\n"
    "s_nop 0\n"
    "ds_read_b64_tr_b16 v[88:89], v251 offset:33280\n"
    "ds_read_b64_tr_b16 v[90:91], v251 offset:35328\n"
    "v_mfma_f32_16x16x32_bf16 v[200:203], v[96:99], a[40:43], 0\n"
    "s_nop 0\n"
    "ds_read_b64_tr_b16 v[92:93], v251 offset:37376\n"
    "ds_read_b64_tr_b16 v[94:95], v251 offset:39424\n"
    "v_mfma_f32_16x16x32_bf16 v[200:203], v[100:103], a[104:107], v[200:203]\n"
    "s_waitcnt vmcnt(15)\n"
    "s_barrier\n"
    "v_mfma_f32_16x16x32_bf16 v[204:207], v[96:99], a[56:59], 0\n"
    "s_nop 0\n"
    "ds_read_b64_tr_b16 v[0:1], v240 offset:0\n"
    "ds_read_b64_tr_b16 v[2:3], v240 offset:2048\n"
    "v_mfma_f32_16x16x32_bf16 v[204:207], v[100:103], a[120:123], v[204:207]\n"
    "s_waitcnt lgkmcnt(6)\n"
    "buffer_store_dwordx2 v[248:249], v247, s[36:39], s18 offen offset:0 sc1\n"
    "v_mfma_f32_16x16x32_bf16 v[208:211], v[104:107], a[8:11], 0\n"
    "s_nop 0\n"
    "ds_read_b64_tr_b16 v[4:5], v240 offset:4096\n"
    "ds_read_b64_tr_b16 v[6:7], v240 offset:6144\n"
    "v_mfma_f32_16x16x32_bf16 v[208:211], v[108:111], a[72:75], v[208:211]\n"
    "v_add_u32 v253, s15, v253\n"
    "v_mfma_f32_16x16x32_bf16 v[212:215], v[104:107], a[24:27], 0\n"
    "s_add_u32 m0, s52, 0\n"
    "ds_read_b64_tr_b16 v[8:9], v240 offset:8192\n"
    "ds_read_b64_tr_b16 v[10:11], v240 offset:10240\n"
    "v_mfma_f32_16x16x32_bf16 v[212:215], v[108:111], a[88:91], v[212:215]\n"
    "buffer_load_dwordx4 v253, s[72:75], s44 offen offset:0 lds\n"
    "v_mfma_f32_16x16x32_bf16 v[216:219], v[104:107], a[40:43], 0\n"
    "s_nop 0\n"
    "ds_read_b64_tr_b16 v[12:13], v240 offset:12288\n"
    "ds_read_b64_tr_b16 v[14:15], v240 offset:14336\n"
    "v_mfma_f32_16x16x32_bf16 v[216:219], v[108:111], a[104:107], v[216:219]\n"
    "buffer_load_dwordx4 v253, s[72:75], s45 offen offset:1024 lds\n"
    "v_mfma_f32_16x16x32_bf16 v[220:223], v[104:107], a[56:59], 0\n"
    "s_nop 0\n"
    "ds_read_b64_tr_b16 v[16:17], v240 offset:16384\n"
    "ds_read_b64_tr_b16 v[18:19], v240 offset:18432\n"
    "v_mfma_f32_16x16x32_bf16 v[220:223], v[108:111], a[120:123], v[220:223]\n"
    "s_waitcnt lgkmcnt(10)\n"
    "buffer_load_dwordx4 v253, s[72:75], s46 offen offset:2048 lds\n"
    "v_mfma_f32_16x16x32_bf16 v[224:227], v[88:91], a[8:11], 0\n"
    "s_nop 0\n"
    "ds_read_b64_tr_b16 v[20:21], v240 offset:20480\n"
    "ds_read_b64_tr_b16 v[22:23], v240 offset:22528\n"
    "v_mfma_f32_16x16x32_bf16 v[224:227], v[92:95], a[72:75], v[224:227]\n"
    "v_mfma_f32_16x16x32_bf16 v[228:231], v[88:91], a[24:27], 0\n"
    "s_nop 0\n"
    "ds_read_b64_tr_b16 v[24:25], v240 offset:24576\n"
    "ds_read_b64_tr_b16 v[26:27], v240 offset:26624\n"
    "v_mfma_f32_16x16x32_bf16 v[228:231], v[92:95], a[88:91], v[228:231]\n"
    "buffer_load_dwordx4 v253, s[72:75], s47 offen offset:3072 lds\n"
    "v_mfma_f32_16x16x32_bf16 v[232:235], v[88:91], a[40:43], 0\n"
    "s_add_u32 m0, s52, 4096\n"
    "ds_read_b64_tr_b16 v[28:29], v240 offset:28672\n"
    "ds_read_b64_tr_b16 v[30:31], v240 offset:30720\n"
    "v_mfma_f32_16x16x32_bf16 v[232:235], v[92:95], a[104:107], v[232:235]\n"
    "v_mfma_f32_16x16x32_bf16 v[236:239], v[88:91], a[56:59], 0\n"
    "s_nop 0\n"
    "ds_read_b64_tr_b16 v[32:33], v241 offset:0\n"
    "ds_read_b64_tr_b16 v[34:35], v241 offset:2048\n"
    "v_mfma_f32_16x16x32_bf16 v[236:239], v[92:95], a[120:123], v[236:239]\n"
    "s_waitcnt lgkmcnt(14)\n"
    "buffer_load_dwordx4 v253, s[72:75], s48 offen offset:0 lds\n"
    "v_mfma_f32_16x16x32_bf16 v[64:67], v[0:3], v[112:115], 0\n"
    "s_nop 0\n"
    "ds_read_b64_tr_b16 v[36:37], v241 offset:4096\n"
    "ds_read_b64_tr_b16 v[38:39], v241 offset:6144\n"
    "v_mfma_f32_16x16x32_bf16 v[64:67], v[4:7], v[116:119], v[64:67]\n"
    "s_waitcnt lgkmcnt(12)\n"
    "v_mfma_f32_16x16x32_bf16 v[64:67], v[8:11], v[120:123], v[64:67]\n"
    "s_nop 0\n"
    "ds_read_b64_tr_b16 v[40:41], v241 offset:8192\n"
    "ds_read_b64_tr_b16 v[42:43], v241 offset:10240\n"
    "v_mfma_f32_16x16x32_bf16 v[64:67], v[12:15], v[124:127], v[64:67]\n"
    "s_waitcnt lgkmcnt(10)\n"
    "buffer_load_dwordx4 v253, s[72:75], s49 offen offset:1024 lds\n"
    "v_mfma_f32_16x16x32_bf16 v[64:67], v[16:19], v[128:131], v[64:67]\n"
    "s_nop 0\n"
    "ds_read_b64_tr_b16 v[44:45], v241 offset:12288\n"
    "ds_read_b64_tr_b16 v[46:47], v241 offset:14336\n"
    "v_mfma_f32_16x16x32_bf16 v[64:67], v[20:23], v[132:135], v[64:67]\n"
    "s_waitcnt lgkmcnt(8)\n"
    "v_mfma_f32_16x16x32_bf16 v[64:67], v[24:27], v[136:139], v[64:67]\n"
    "s_nop 0\n"
    "ds_read_b64_tr_b16 v[48:49], v241 offset:16384\n"
    "ds_read_b64_tr_b16 v[50:51], v241 offset:18432\n"
    "v_mfma_f32_16x16x32_bf16 v[64:67], v[28:31], v[140:143], v[64:67]\n"
    "buffer_load_dwordx4 v253, s[72:75], s50 offen offset:2048 lds\n"
    "v_mfma_f32_16x16x32_bf16 v[68:71], v[0:3], v[144:147], 0\n"
    "s_nop 0\n"
    "ds_read_b64_tr_b16 v[52:53], v241 offset:20480\n"
    "ds_read_b64_tr_b16 v[54:55], v241 offset:22528\n"
    "v_mfma_f32_16x16x32_bf16 v[68:71], v[4:7], v[148:151], v[68:71]\n"
    "v_mfma_f32_16x16x32_bf16 v[68:71], v[8:11], v[152:155], v[68:71]\n"
    "s_nop 0\n"
    "ds_read_b64_tr_b16 v[56:57], v241 offset:24576\n"
    "ds_read_b64_tr_b16 v[58:59], v241 offset:26624\n"
    "v_mfma_f32_16x16x32_bf16 v[68:71], v[12:15], v[156:159], v[68:71]\n"
    "buffer_load_dwordx4 v253, s[72:75], s51 offen offset:3072 lds\n"
    "v_mfma_f32_16x16x32_bf16 v[68:71], v[16:19], v[160:163], v[68:71]\n"
    "s_nop 0\n"
    "ds_read_b64_tr_b16 v[60:61], v241 offset:28672\n"
    "ds_read_b64_tr_b16 v[62:63], v241 offset:30720\n"
    "v_mfma_f32_16x16x32_bf16 v[68:71], v[20:23], v[164:167], v[68:71]\n"
    "v_add_u32 v253, s17, v253\n"
    "v_mfma_f32_16x16x32_bf16 v[68:71], v[24:27], v[168:171], v[68:71]\n"
    "s_add_u32 m0, s52, 32768\n"
    "ds_read_b64_tr_b16 v[0:1], v240 offset:512\n"
    "ds_read_b64_tr_b16 v[2:3], v240 offset:2560\n"
    "v_mfma_f32_16x16x32_bf16 v[68:71], v[28:31], v[172:175], v[68:71]\n"
    "s_waitcnt lgkmcnt(14)\n"
    "buffer_load_dwordx4 v253, s[72:75], s44 offen offset:0 lds\n"
    "v_mfma_f32_16x16x32_bf16 v[72:75], v[32:35], v[112:115], 0\n"
    "s_nop 0\n"
    "ds_read_b64_tr_b16 v[4:5], v240 offset:4608\n"
    "ds_read_b64_tr_b16 v[6:7], v240 offset:6656\n"
    "v_mfma_f32_16x16x32_bf16 v[72:75], v[36:39], v[116:119], v[72:75]\n"
    "s_waitcnt lgkmcnt(12)\n"
    "v_cvt_pk_bf16_f32 v80, v64, v65\n"
    "v_cvt_pk_bf16_f32 v81, v66, v67\n"
    "v_mfma_f32_16x16x32_bf16 v[72:75], v[40:43], v[120:123], v[72:75]\n"
    "s_nop 0\n"
    "ds_read_b64_tr_b16 v[8:9], v240 offset:8704\n"
    "ds_read_b64_tr_b16 v[10:11], v240 offset:10752\n"
    "v_mfma_f32_16x16x32_bf16 v[72:75], v[44:47], v[124:127], v[72:75]\n"
    "s_waitcnt lgkmcnt(10)\n"
    "buffer_load_dwordx4 v253, s[72:75], s45 offen offset:1024 lds\n"
    "v_mfma_f32_16x16x32_bf16 v[72:75], v[48:51], v[128:131], v[72:75]\n"
    "s_nop 0\n"
    "ds_read_b64_tr_b16 v[12:13], v240 offset:12800\n"
    "ds_read_b64_tr_b16 v[14:15], v240 offset:14848\n"
    "v_mfma_f32_16x16x32_bf16 v[72:75], v[52:55], v[132:135], v[72:75]\n"
    "s_waitcnt lgkmcnt(8)\n"
    "v_mfma_f32_16x16x32_bf16 v[72:75], v[56:59], v[136:139], v[72:75]\n"
    "s_add_u32 s96, s66, 0\n"
    "ds_read_b64_tr_b16 v[16:17], v240 offset:16896\n"
    "ds_read_b64_tr_b16 v[18:19], v240 offset:18944\n"
    "v_mfma_f32_16x16x32_bf16 v[72:75], v[60:63], v[140:143], v[72:75]\n"
    "buffer_load_dwordx4 v253, s[72:75], s46 offen offset:2048 lds\n"
    "v_mfma_f32_16x16x32_bf16 v[76:79], v[32:35], v[144:147], 0\n"
    "s_nop 0\n"
    "ds_read_b64_tr_b16 v[20:21], v240 offset:20992\n"
    "ds_read_b64_tr_b16 v[22:23], v240 offset:23040\n"
    "v_mfma_f32_16x16x32_bf16 v[76:79], v[36:39], v[148:151], v[76:79]\n"
    "v_cvt_pk_bf16_f32 v82, v68, v69\n"
    "v_cvt_pk_bf16_f32 v83, v70, v71\n"
    "v_mfma_f32_16x16x32_bf16 v[76:79], v[40:43], v[152:155], v[76:79]\n"
    "s_nop 0\n"
    "ds_read_b64_tr_b16 v[24:25], v240 offset:25088\n"
    "ds_read_b64_tr_b16 v[26:27], v240 offset:27136\n"
    "v_mfma_f32_16x16x32_bf16 v[76:79], v[44:47], v[156:159], v[76:79]\n"
    "buffer_store_dwordx4 v[80:83], v242, s[88:91], s96 offen offset:0 sc1\n"
    "v_mfma_f32_16x16x32_bf16 v[76:79], v[48:51], v[160:163], v[76:79]\n"
    "s_nop 0\n"
    "ds_read_b64_tr_b16 v[28:29], v240 offset:29184\n"
    "ds_read_b64_tr_b16 v[30:31], v240 offset:31232\n"
    "v_mfma_f32_16x16x32_bf16 v[76:79], v[52:55], v[164:167], v[76:79]\n"
    "buffer_load_dwordx4 v253, s[72:75], s47 offen offset:3072 lds\n"
    "v_mfma_f32_16x16x32_bf16 v[76:79], v[56:59], v[168:171], v[76:79]\n"
    "s_add_u32 m0, s52, 36864\n"
    "ds_read_b64_tr_b16 v[32:33], v241 offset:512\n"
    "ds_read_b64_tr_b16 v[34:35], v241 offset:2560\n"
    "v_mfma_f32_16x16x32_bf16 v[76:79], v[60:63], v[172:175], v[76:79]\n"
    "s_waitcnt lgkmcnt(14)\n"
    "buffer_load_dwordx4 v253, s[72:75], s48 offen offset:0 lds\n"
    "v_mfma_f32_16x16x32_bf16 v[64:67], v[0:3], v[112:115], 0\n"
    "s_nop 0\n"
    "ds_read_b64_tr_b16 v[36:37], v241 offset:4608\n"
    "ds_read_b64_tr_b16 v[38:39], v241 offset:6656\n"
    "v_mfma_f32_16x16x32_bf16 v[64:67], v[4:7], v[116:119], v[64:67]\n"
    "s_waitcnt lgkmcnt(12)\n"
    "v_cvt_pk_bf16_f32 v84, v72, v73\n"
    "v_cvt_pk_bf16_f32 v85, v74, v75\n"
    "v_mfma_f32_16x16x32_bf16 v[64:67], v[8:11], v[120:123], v[64:67]\n"
    "s_nop 0\n"
    "ds_read_b64_tr_b16 v[40:41], v241 offset:8704\n"
    "ds_read_b64_tr_b16 v[42:43], v241 offset:10752\n"
    "v_mfma_f32_16x16x32_bf16 v[64:67], v[12:15], v[124:127], v[64:67]\n"
    "s_waitcnt lgkmcnt(10)\n"
    "buffer_load_dwordx4 v253, s[72:75], s49 offen offset:1024 lds\n"
    "v_mfma_f32_16x16x32_bf16 v[64:67], v[16:19], v[128:131], v[64:67]\n"
    "s_nop 0\n"
    "ds_read_b64_tr_b16 v[44:45], v241 offset:12800\n"
    "ds_read_b64_tr_b16 v[46:47], v241 offset:14848\n"
    "v_mfma_f32_16x16x32_bf16 v[64:67], v[20:23], v[132:135], v[64:67]\n"
    "s_waitcnt lgkmcnt(8)\n"
    "v_mfma_f32_16x16x32_bf16 v[64:67], v[24:27], v[136:139], v[64:67]\n"
    "s_add_u32 s97, s66, 16384\n"
    "ds_read_b64_tr_b16 v[48:49], v241 offset:16896\n"
    "ds_read_b64_tr_b16 v[50:51], v241 offset:18944\n"
    "v_mfma_f32_16x16x32_bf16 v[64:67], v[28:31], v[140:143], v[64:67]\n"
    "buffer_load_dwordx4 v253, s[72:75], s50 offen offset:2048 lds\n"
    "v_mfma_f32_16x16x32_bf16 v[68:71], v[0:3], v[144:147], 0\n"
    "s_nop 0\n"
    "ds_read_b64_tr_b16 v[52:53], v241 offset:20992\n"
    "ds_read_b64_tr_b16 v[54:55], v241 offset:23040\n"
    "v_mfma_f32_16x16x32_bf16 v[68:71], v[4:7], v[148:151], v[68:71]\n"
    "v_cvt_pk_bf16_f32 v86, v76, v77\n"
    "v_cvt_pk_bf16_f32 v87, v78, v79\n"
    "v_mfma_f32_16x16x32_bf16 v[68:71], v[8:11], v[152:155], v[68:71]\n"
    "s_nop 0\n"
    "ds_read_b64_tr_b16 v[56:57], v241 offset:25088\n"
    "ds_read_b64_tr_b16 v[58:59], v241 offset:27136\n"
    "v_mfma_f32_16x16x32_bf16 v[68:71], v[12:15], v[156:159], v[68:71]\n"
    "buffer_store_dwordx4 v[84:87], v242, s[88:91], s97 offen offset:0 sc1\n"
    "v_mfma_f32_16x16x32_bf16 v[68:71], v[16:19], v[160:163], v[68:71]\n"
    "s_nop 0\n"
    "ds_read_b64_tr_b16 v[60:61], v241 offset:29184\n"
    "ds_read_b64_tr_b16 v[62:63], v241 offset:31232\n"
    "v_mfma_f32_16x16x32_bf16 v[68:71], v[20:23], v[164:167], v[68:71]\n"
    "s_waitcnt vmcnt(9)\n"
    "s_barrier\n"
    "buffer_load_dwordx4 v253, s[72:75], s51 offen offset:3072 lds\n"
    "v_mfma_f32_16x16x32_bf16 v[68:71], v[24:27], v[168:171], v[68:71]\n"
    "s_nop 0\n"
    "ds_read_b64_tr_b16 v[0:1], v245 offset:0\n"
    "ds_read_b64_tr_b16 v[2:3], v245 offset:2048\n"
    "v_mfma_f32_16x16x32_bf16 v[68:71], v[28:31], v[172:175], v[68:71]\n"
    "s_waitcnt lgkmcnt(14)\n"
    "buffer_load_dwordx4 a[64:67], v242, s[84:87], s92 offen offset:2048 sc1\n"
    "v_mfma_f32_16x16x32_bf16 v[72:75], v[32:35], v[112:115], 0\n"
    "s_nop 0\n"
    "ds_read_b64_tr_b16 v[4:5], v245 offset:4096\n"
    "ds_read_b64_tr_b16 v[6:7], v245 offset:6144\n"
    "v_mfma_f32_16x16x32_bf16 v[72:75], v[36:39], v[116:119], v[72:75]\n"
    "s_waitcnt lgkmcnt(12)\n"
    "v_cvt_pk_bf16_f32 v80, v64, v65\n"
    "v_cvt_pk_bf16_f32 v81, v66, v67\n"
    "v_mfma_f32_16x16x32_bf16 v[72:75], v[40:43], v[120:123], v[72:75]\n"
    "s_nop 0\n"
    "ds_read_b64_tr_b16 v[8:9], v245 offset:8192\n"
    "ds_read_b64_tr_b16 v[10:11], v245 offset:10240\n"
    "v_mfma_f32_16x16x32_bf16 v[72:75], v[44:47], v[124:127], v[72:75]\n"
    "s_waitcnt lgkmcnt(10)\n"
    "buffer_load_dwordx4 a[68:71], v242, s[84:87], s93 offen offset:2048 sc1\n"
    "v_mfma_f32_16x16x32_bf16 v[72:75], v[48:51], v[128:131], v[72:75]\n"
    "s_nop 0\n"
    "ds_read_b64_tr_b16 v[12:13], v245 offset:12288\n"
    "ds_read_b64_tr_b16 v[14:15], v245 offset:14336\n"
    "v_mfma_f32_16x16x32_bf16 v[72:75], v[52:55], v[132:135], v[72:75]\n"
    "s_waitcnt lgkmcnt(8)\n"
    "v_mfma_f32_16x16x32_bf16 v[72:75], v[56:59], v[136:139], v[72:75]\n"
    "s_add_u32 s96, s66, 32768\n"
    "ds_read_b64_tr_b16 v[16:17], v245 offset:16384\n"
    "ds_read_b64_tr_b16 v[18:19], v245 offset:18432\n"
    "v_mfma_f32_16x16x32_bf16 v[72:75], v[60:63], v[140:143], v[72:75]\n"
    "buffer_load_dwordx4 a[80:83], v242, s[84:87], s94 offen offset:2048 sc1\n"
    "v_mfma_f32_16x16x32_bf16 v[76:79], v[32:35], v[144:147], 0\n"
    "s_nop 0\n"
    "ds_read_b64_tr_b16 v[20:21], v245 offset:20480\n"
    "ds_read_b64_tr_b16 v[22:23], v245 offset:22528\n"
    "v_mfma_f32_16x16x32_bf16 v[76:79], v[36:39], v[148:151], v[76:79]\n"
    "v_cvt_pk_bf16_f32 v82, v68, v69\n"
    "v_cvt_pk_bf16_f32 v83, v70, v71\n"
    "v_mfma_f32_16x16x32_bf16 v[76:79], v[40:43], v[152:155], v[76:79]\n"
    "s_nop 0\n"
    "ds_read_b64_tr_b16 v[24:25], v245 offset:24576\n"
    "ds_read_b64_tr_b16 v[26:27], v245 offset:26624\n"
    "v_mfma_f32_16x16x32_bf16 v[76:79], v[44:47], v[156:159], v[76:79]\n"
    "buffer_store_dwordx4 v[80:83], v242, s[88:91], s96 offen offset:0 sc1\n"
    "v_mfma_f32_16x16x32_bf16 v[76:79], v[48:51], v[160:163], v[76:79]\n"
    "s_nop 0\n"
    "ds_read_b64_tr_b16 v[28:29], v245 offset:28672\n"
    "ds_read_b64_tr_b16 v[30:31], v245 offset:30720\n"
    "v_mfma_f32_16x16x32_bf16 v[76:79], v[52:55], v[164:167], v[76:79]\n"
    "buffer_load_dwordx4 a[84:87], v242, s[84:87], s95 offen offset:2048 sc1\n"
    "v_mfma_f32_16x16x32_bf16 v[76:79], v[56:59], v[168:171], v[76:79]\n"
    "s_nop 0\n"
    "ds_read_b64_tr_b16 v[32:33], v246 offset:0\n"
    "ds_read_b64_tr_b16 v[34:35], v246 offset:2048\n"
    "v_mfma_f32_16x16x32_bf16 v[76:79], v[60:63], v[172:175], v[76:79]\n"
    "s_waitcnt lgkmcnt(14)\n"
    "buffer_load_dwordx4 a[96:99], v242, s[84:87], s92 offen offset:3072 sc1\n"
    "v_mfma_f32_16x16x32_bf16 v[64:67], v[0:3], v[112:115], 0\n"
    "s_nop 0\n"
    "ds_read_b64_tr_b16 v[36:37], v246 offset:4096\n"
    "ds_read_b64_tr_b16 v[38:39], v246 offset:6144\n"
    "v_mfma_f32_16x16x32_bf16 v[64:67], v[4:7], v[116:119], v[64:67]\n"
    "s_waitcnt lgkmcnt(12)\n"
    "v_cvt_pk_bf16_f32 v84, v72, v73\n"
    "v_cvt_pk_bf16_f32 v85, v74, v75\n"
    "v_mfma_f32_16x16x32_bf16 v[64:67], v[8:11], v[120:123], v[64:67]\n"
    "s_nop 0\n"
    "ds_read_b64_tr_b16 v[40:41], v246 offset:8192\n"
    "ds_read_b64_tr_b16 v[42:43], v246 offset:10240\n"
    "v_mfma_f32_16x16x32_bf16 v[64:67], v[12:15], v[124:127], v[64:67]\n"
    "s_waitcnt lgkmcnt(10)\n"
    "buffer_load_dwordx4 a[100:103], v242, s[84:87], s93 offen offset:3072 sc1\n"
    "v_mfma_f32_16x16x32_bf16 v[64:67], v[16:19], v[128:131], v[64:67]\n"
    "s_nop 0\n"
    "ds_read_b64_tr_b16 v[44:45], v246 offset:12288\n"
    "ds_read_b64_tr_b16 v[46:47], v246 offset:14336\n"
    "v_mfma_f32_16x16x32_bf16 v[64:67], v[20:23], v[132:135], v[64:67]\n"
    "s_waitcnt lgkmcnt(8)\n"
    "v_mfma_f32_16x16x32_bf16 v[64:67], v[24:27], v[136:139], v[64:67]\n"
    "s_add_u32 s97, s66, 49152\n"
    "ds_read_b64_tr_b16 v[48:49], v246 offset:16384\n"
    "ds_read_b64_tr_b16 v[50:51], v246 offset:18432\n"
    "v_mfma_f32_16x16x32_bf16 v[64:67], v[28:31], v[140:143], v[64:67]\n"
    "buffer_load_dwordx4 a[112:115], v242, s[84:87], s94 offen offset:3072 sc1\n"
    "v_mfma_f32_16x16x32_bf16 v[68:71], v[0:3], v[144:147], 0\n"
    "s_nop 0\n"
    "ds_read_b64_tr_b16 v[52:53], v246 offset:20480\n"
    "ds_read_b64_tr_b16 v[54:55], v246 offset:22528\n"
    "v_mfma_f32_16x16x32_bf16 v[68:71], v[4:7], v[148:151], v[68:71]\n"
    "v_cvt_pk_bf16_f32 v86, v76, v77\n"
    "v_cvt_pk_bf16_f32 v87, v78, v79\n"
    "v_mfma_f32_16x16x32_bf16 v[68:71], v[8:11], v[152:155], v[68:71]\n"
    "s_nop 0\n"
    "ds_read_b64_tr_b16 v[56:57], v246 offset:24576\n"
    "ds_read_b64_tr_b16 v[58:59], v246 offset:26624\n"
    "v_mfma_f32_16x16x32_bf16 v[68:71], v[12:15], v[156:159], v[68:71]\n"
    "buffer_store_dwordx4 v[84:87], v242, s[88:91], s97 offen offset:0 sc1\n"
    "v_mfma_f32_16x16x32_bf16 v[68:71], v[16:19], v[160:163], v[68:71]\n"
    "s_nop 0\n"
    "ds_read_b64_tr_b16 v[60:61], v246 offset:28672\n"
    "ds_read_b64_tr_b16 v[62:63], v246 offset:30720\n"
    "v_mfma_f32_16x16x32_bf16 v[68:71], v[20:23], v[164:167], v[68:71]\n"
    "buffer_load_dwordx4 a[116:119], v242, s[84:87], s95 offen offset:3072 sc1\n"
    "v_mfma_f32_16x16x32_bf16 v[68:71], v[24:27], v[168:171], v[68:71]\n"
    "s_add_u32 s92, s92, 4096\n"
    "ds_read_b64_tr_b16 v[0:1], v245 offset:512\n"
    "ds_read_b64_tr_b16 v[2:3], v245 offset:2560\n"
    "v_mfma_f32_16x16x32_bf16 v[68:71], v[28:31], v[172:175], v[68:71]\n"
    "s_waitcnt lgkmcnt(14)\n"
    "v_mfma_f32_16x16x32_bf16 v[72:75], v[32:35], v[112:115], 0\n"
    "s_add_u32 s93, s93, 4096\n"
    "ds_read_b64_tr_b16 v[4:5], v245 offset:4608\n"
    "ds_read_b64_tr_b16 v[6:7], v245 offset:6656\n"
    "v_mfma_f32_16x16x32_bf16 v[72:75], v[36:39], v[116:119], v[72:75]\n"
    "s_waitcnt lgkmcnt(12)\n"
    "v_cvt_pk_bf16_f32 v80, v64, v65\n"
    "v_cvt_pk_bf16_f32 v81, v66, v67\n"
    "v_mfma_f32_16x16x32_bf16 v[72:75], v[40:43], v[120:123], v[72:75]\n"
    "s_add_u32 s94, s94, 4096\n"
    "ds_read_b64_tr_b16 v[8:9], v245 offset:8704\n"
    "ds_read_b64_tr_b16 v[10:11], v245 offset:10752\n"
    "v_mfma_f32_16x16x32_bf16 v[72:75], v[44:47], v[124:127], v[72:75]\n"
    "s_waitcnt lgkmcnt(10)\n"
    "v_mfma_f32_16x16x32_bf16 v[72:75], v[48:51], v[128:131], v[72:75]\n"
    "s_add_u32 s95, s95, 4096\n"
    "ds_read_b64_tr_b16 v[12:13], v245 offset:12800\n"
    "ds_read_b64_tr_b16 v[14:15], v245 offset:14848\n"
    "v_mfma_f32_16x16x32_bf16 v[72:75], v[52:55], v[132:135], v[72:75]\n"
    "s_waitcnt lgkmcnt(8)\n"
    "v_mfma_f32_16x16x32_bf16 v[72:75], v[56:59], v[136:139], v[72:75]\n"
    "s_add_u32 s96, s67, 0\n"
    "ds_read_b64_tr_b16 v[16:17], v245 offset:16896\n"
    "ds_read_b64_tr_b16 v[18:19], v245 offset:18944\n"
    "v_mfma_f32_16x16x32_bf16 v[72:75], v[60:63], v[140:143], v[72:75]\n"
    "s_add_u32 m0, s10, 32768\n"
    "v_mfma_f32_16x16x32_bf16 v[76:79], v[32:35], v[144:147], 0\n"
    "s_nop 0\n"
    "ds_read_b64_tr_b16 v[20:21], v245 offset:20992\n"
    "ds_read_b64_tr_b16 v[22:23], v245 offset:23040\n"
    "v_mfma_f32_16x16x32_bf16 v[76:79], v[36:39], v[148:151], v[76:79]\n"
    "buffer_load_dwordx4 v252, s[76:79], s11 offen offset:0 lds\n"
    "v_mfma_f32_16x16x32_bf16 v[76:79], v[40:43], v[152:155], v[76:79]\n"
    "s_nop 0\n"
    "ds_read_b64_tr_b16 v[24:25], v245 offset:25088\n"
    "ds_read_b64_tr_b16 v[26:27], v245 offset:27136\n"
    "v_mfma_f32_16x16x32_bf16 v[76:79], v[44:47], v[156:159], v[76:79]\n"
    "v_cvt_pk_bf16_f32 v82, v68, v69\n"
    "v_cvt_pk_bf16_f32 v83, v70, v71\n"
    "v_mfma_f32_16x16x32_bf16 v[76:79], v[48:51], v[160:163], v[76:79]\n"
    "s_nop 0\n"
    "ds_read_b64_tr_b16 v[28:29], v245 offset:29184\n"
    "ds_read_b64_tr_b16 v[30:31], v245 offset:31232\n"
    "v_mfma_f32_16x16x32_bf16 v[76:79], v[52:55], v[164:167], v[76:79]\n"
    "buffer_store_dwordx4 v[80:83], v242, s[88:91], s96 offen offset:0 sc1\n"
    "v_mfma_f32_16x16x32_bf16 v[76:79], v[56:59], v[168:171], v[76:79]\n"
    "s_nop 0\n"
    "ds_read_b64_tr_b16 v[32:33], v246 offset:512\n"
    "ds_read_b64_tr_b16 v[34:35], v246 offset:2560\n"
    "v_mfma_f32_16x16x32_bf16 v[76:79], v[60:63], v[172:175], v[76:79]\n"
    "s_waitcnt lgkmcnt(14)\n"
    "buffer_load_dwordx4 v252, s[76:79], s12 offen offset:1024 lds\n"
    "v_mfma_f32_16x16x32_bf16 v[64:67], v[0:3], v[112:115], 0\n"
    "s_nop 0\n"
    "ds_read_b64_tr_b16 v[36:37], v246 offset:4608\n"
    "ds_read_b64_tr_b16 v[38:39], v246 offset:6656\n"
    "v_mfma_f32_16x16x32_bf16 v[64:67], v[4:7], v[116:119], v[64:67]\n"
    "s_waitcnt lgkmcnt(12)\n"
    "buffer_load_dwordx4 v252, s[76:79], s13 offen offset:2048 lds\n"
    "v_mfma_f32_16x16x32_bf16 v[64:67], v[8:11], v[120:123], v[64:67]\n"
    "s_nop 0\n"
    "ds_read_b64_tr_b16 v[40:41], v246 offset:8704\n"
    "ds_read_b64_tr_b16 v[42:43], v246 offset:10752\n"
    "v_mfma_f32_16x16x32_bf16 v[64:67], v[12:15], v[124:127], v[64:67]\n"
    "s_waitcnt lgkmcnt(10)\n"
    "v_cvt_pk_bf16_f32 v84, v72, v73\n"
    "v_cvt_pk_bf16_f32 v85, v74, v75\n"
    "v_mfma_f32_16x16x32_bf16 v[64:67], v[16:19], v[128:131], v[64:67]\n"
    "s_nop 0\n"
    "ds_read_b64_tr_b16 v[44:45], v246 offset:12800\n"
    "ds_read_b64_tr_b16 v[46:47], v246 offset:14848\n"
    "v_mfma_f32_16x16x32_bf16 v[64:67], v[20:23], v[132:135], v[64:67]\n"
    "s_waitcnt lgkmcnt(8)\n"
    "buffer_load_dwordx4 v252, s[76:79], s16 offen offset:3072 lds\n"
    "v_mfma_f32_16x16x32_bf16 v[64:67], v[24:27], v[136:139], v[64:67]\n"
    "s_add_u32 s97, s67, 16384\n"
    "ds_read_b64_tr_b16 v[48:49], v246 offset:16896\n"
    "ds_read_b64_tr_b16 v[50:51], v246 offset:18944\n"
    "v_mfma_f32_16x16x32_bf16 v[64:67], v[28:31], v[140:143], v[64:67]\n"
    "v_add_u32 v252, 28672, v252\n"
    "v_mfma_f32_16x16x32_bf16 v[68:71], v[0:3], v[144:147], 0\n"
    "s_add_u32 m0, s10, 36864\n"
    "ds_read_b64_tr_b16 v[52:53], v246 offset:20992\n"
    "ds_read_b64_tr_b16 v[54:55], v246 offset:23040\n"
    "v_mfma_f32_16x16x32_bf16 v[68:71], v[4:7], v[148:151], v[68:71]\n"
    "buffer_load_dwordx4 v252, s[76:79], s11 offen offset:0 lds\n"
    "v_mfma_f32_16x16x32_bf16 v[68:71], v[8:11], v[152:155], v[68:71]\n"
    "s_nop 0\n"
    "ds_read_b64_tr_b16 v[56:57], v246 offset:25088\n"
    "ds_read_b64_tr_b16 v[58:59], v246 offset:27136\n"
    "v_mfma_f32_16x16x32_bf16 v[68:71], v[12:15], v[156:159], v[68:71]\n"
    "v_cvt_pk_bf16_f32 v86, v76, v77\n"
    "v_cvt_pk_bf16_f32 v87, v78, v79\n"
    "v_mfma_f32_16x16x32_bf16 v[68:71], v[16:19], v[160:163], v[68:71]\n"
    "s_nop 0\n"
    "ds_read_b64_tr_b16 v[60:61], v246 offset:29184\n"
    "ds_read_b64_tr_b16 v[62:63], v246 offset:31232\n"
    "v_mfma_f32_16x16x32_bf16 v[68:71], v[20:23], v[164:167], v[68:71]\n"
    "buffer_store_dwordx4 v[84:87], v242, s[88:91], s97 offen offset:0 sc1\n"
    "v_mfma_f32_16x16x32_bf16 v[68:71], v[24:27], v[168:171], v[68:71]\n"
    "buffer_load_dwordx4 v252, s[76:79], s12 offen offset:1024 lds\n"
    "v_mfma_f32_16x16x32_bf16 v[68:71], v[28:31], v[172:175], v[68:71]\n"
    "s_waitcnt lgkmcnt(12)\n"
    "buffer_load_dwordx4 v252, s[76:79], s13 offen offset:2048 lds\n"
    "v_mfma_f32_16x16x32_bf16 v[72:75], v[32:35], v[112:115], 0\n"
    "buffer_load_dwordx4 v252, s[76:79], s16 offen offset:3072 lds\n"
    "v_mfma_f32_16x16x32_bf16 v[72:75], v[36:39], v[116:119], v[72:75]\n"
    "s_waitcnt lgkmcnt(8)\n"
    "v_add_u32 v252, 28672, v252\n"
    "v_cvt_pk_bf16_f32 v80, v64, v65\n"
    "v_mfma_f32_16x16x32_bf16 v[72:75], v[40:43], v[120:123], v[72:75]\n"
    "s_nop 0\n"
    "v_cvt_pk_bf16_f32 v81, v66, v67\n"
    "v_mfma_f32_16x16x32_bf16 v[72:75], v[44:47], v[124:127], v[72:75]\n"
    "s_waitcnt lgkmcnt(4)\n"
    "ds_read_b64_tr_b16 v[96:97], v244 offset:0\n"
    "ds_read_b64_tr_b16 v[98:99], v244 offset:2048\n"
    "v_mfma_f32_16x16x32_bf16 v[72:75], v[48:51], v[128:131], v[72:75]\n"
    "s_lshr_b32 s19, s65, 8\n"
    "ds_read_b64_tr_b16 v[100:101], v244 offset:4096\n"
    "ds_read_b64_tr_b16 v[102:103], v244 offset:6144\n"
    "v_mfma_f32_16x16x32_bf16 v[72:75], v[52:55], v[132:135], v[72:75]\n"
    "s_waitcnt lgkmcnt(4)\n"
    "buffer_store_dwordx2 v[248:249], v247, s[36:39], s19 offen offset:0 sc1\n"
    "v_mfma_f32_16x16x32_bf16 v[72:75], v[56:59], v[136:139], v[72:75]\n"
    "s_add_u32 s96, s67, 32768\n"
    "global_load_dwordx2 v[254:255], v250, s[20:21] sc1\n"
    "v_mfma_f32_16x16x32_bf16 v[72:75], v[60:63], v[140:143], v[72:75]\n"
    "buffer_load_dwordx4 a[128:131], v242, s[84:87], s92 offen offset:0 sc1\n"
    "v_mfma_f32_16x16x32_bf16 v[76:79], v[32:35], v[144:147], 0\n"
    "buffer_load_dwordx4 a[132:135], v242, s[84:87], s93 offen offset:0 sc1\n"
    "v_mfma_f32_16x16x32_bf16 v[76:79], v[36:39], v[148:151], v[76:79]\n"
    "v_cvt_pk_bf16_f32 v82, v68, v69\n"
    "v_cvt_pk_bf16_f32 v83, v70, v71\n"
    "v_mfma_f32_16x16x32_bf16 v[76:79], v[40:43], v[152:155], v[76:79]\n"
    "buffer_store_dwordx4 v[80:83], v242, s[88:91], s96 offen offset:0 sc1\n"
    "v_mfma_f32_16x16x32_bf16 v[76:79], v[44:47], v[156:159], v[76:79]\n"
    "s_nop 0\n"
    "ds_read_b64_tr_b16 v[104:105], v251 offset:0\n"
    "ds_read_b64_tr_b16 v[106:107], v251 offset:2048\n"
    "v_mfma_f32_16x16x32_bf16 v[76:79], v[48:51], v[160:163], v[76:79]\n"
    "s_nop 0\n"
    "ds_read_b64_tr_b16 v[108:109], v251 offset:4096\n"
    "ds_read_b64_tr_b16 v[110:111], v251 offset:6144\n"
    "v_mfma_f32_16x16x32_bf16 v[76:79], v[52:55], v[164:167], v[76:79]\n"
    "buffer_load_dwordx4 a[144:147], v242, s[84:87], s94 offen offset:0 sc1\n"
    "v_mfma_f32_16x16x32_bf16 v[76:79], v[56:59], v[168:171], v[76:79]\n"
    "buffer_load_dwordx4 a[148:151], v242, s[84:87], s95 offen offset:0 sc1\n"
    "v_mfma_f32_16x16x32_bf16 v[76:79], v[60:63], v[172:175], v[76:79]\n"
    "s_waitcnt lgkmcnt(4)\n"
    "v_mfma_f32_16x16x32_bf16 v[176:179], v[96:99], a[136:139], v[176:179]\n"
    "v_cvt_pk_bf16_f32 v84, v72, v73\n"
    "v_cvt_pk_bf16_f32 v85, v74, v75\n"
    "v_mfma_f32_16x16x32_bf16 v[176:179], v[100:103], a[200:203], v[176:179]\n"
    "buffer_load_dwordx4 a[160:163], v242, s[84:87], s92 offen offset:1024 sc1\n"
    "v_mfma_f32_16x16x32_bf16 v[180:183], v[96:99], a[152:155], v[180:183]\n"
    "v_mfma_f32_16x16x32_bf16 v[180:183], v[100:103], a[216:219], v[180:183]\n"
    "s_nop 0\n"
    "ds_read_b64_tr_b16 v[88:89], v244 offset:512\n"
    "ds_read_b64_tr_b16 v[90:91], v244 offset:2560\n"
    "v_mfma_f32_16x16x32_bf16 v[184:187], v[96:99], a[168:171], v[184:187]\n"
    "s_nop 0\n"
    "ds_read_b64_tr_b16 v[92:93], v244 offset:4608\n"
    "ds_read_b64_tr_b16 v[94:95], v244 offset:6656\n"
    "v_mfma_f32_16x16x32_bf16 v[184:187], v[100:103], a[232:235], v[184:187]\n"
    "buffer_load_dwordx4 a[164:167], v242, s[84:87], s93 offen offset:1024 sc1\n"
    "v_mfma_f32_16x16x32_bf16 v[188:191], v[96:99], a[184:187], v[188:191]\n"
    "s_add_u32 s97, s67, 49152\n"
    "v_mfma_f32_16x16x32_bf16 v[188:191], v[100:103], a[248:251], v[188:191]\n"
    "s_waitcnt lgkmcnt(4)\n"
    "v_mfma_f32_16x16x32_bf16 v[192:195], v[104:107], a[136:139], v[192:195]\n"
    "v_cvt_pk_bf16_f32 v86, v76, v77\n"
    "v_cvt_pk_bf16_f32 v87, v78, v79\n"
    "v_mfma_f32_16x16x32_bf16 v[192:195], v[108:111], a[200:203], v[192:195]\n"
    "buffer_store_dwordx4 v[84:87], v242, s[88:91], s97 offen offset:0 sc1\n"
    "v_mfma_f32_16x16x32_bf16 v[196:199], v[104:107], a[152:155], v[196:199]\n"
    "buffer_load_dwordx4 a[176:179], v242, s[84:87], s94 offen offset:1024 sc1\n"
    "v_mfma_f32_16x16x32_bf16 v[196:199], v[108:111], a[216:219], v[196:199]\n"
    "s_nop 0\n"
    "ds_read_b64_tr_b16 v[96:97], v251 offset:512\n"
    "ds_read_b64_tr_b16 v[98:99], v251 offset:2560\n"
    "v_mfma_f32_16x16x32_bf16 v[200:203], v[104:107], a[168:171], v[200:203]\n"
    "s_nop 0\n"
    "ds_read_b64_tr_b16 v[100:101], v251 offset:4608\n"
    "ds_read_b64_tr_b16 v[102:103], v251 offset:6656\n"
    "v_mfma_f32_16x16x32_bf16 v[200:203], v[108:111], a[232:235], v[200:203]\n"
    "s_waitcnt vmcnt(9)\n"
    "s_barrier\n"
    "buffer_load_dwordx4 a[180:183], v242, s[84:87], s95 offen offset:1024 sc1\n"
    "v_mfma_f32_16x16x32_bf16 v[204:207], v[104:107], a[184:187], v[204:207]\n"
    "v_cmp_eq_u64 vcc, s[70:71], v[254:255]\n"
    "v_cmp_eq_u32 s[58:59], 1, v243\n"
    "s_andn2_b64 vcc, s[58:59], vcc\n"
    "s_cselect_b32 s58, 1, 0\n"
    "s_or_b32 s28, s28, s58\n"
    "ds_read_b64_tr_b16 v[0:1], v245 offset:32768\n"
    "ds_read_b64_tr_b16 v[2:3], v245 offset:34816\n"
    "v_mfma_f32_16x16x32_bf16 v[204:207], v[108:111], a[248:251], v[204:207]\n"
    "s_waitcnt lgkmcnt(6)\n"
    "v_mfma_f32_16x16x32_bf16 v[208:211], v[88:91], a[136:139], v[208:211]\n"
    "s_lshr_b32 s18, s66, 8\n"
    "ds_read_b64_tr_b16 v[4:5], v245 offset:36864\n"
    "ds_read_b64_tr_b16 v[6:7], v245 offset:38912\n"
    "v_mfma_f32_16x16x32_bf16 v[208:211], v[92:95], a[200:203], v[208:211]\n"
    "buffer_store_dwordx2 v[248:249], v247, s[36:39], s18 offen offset:0 sc1\n"
    "v_mfma_f32_16x16x32_bf16 v[212:215], v[88:91], a[152:155], v[212:215]\n"
    "s_nop 0\n"
    "ds_read_b64_tr_b16 v[8:9], v245 offset:40960\n"
    "ds_read_b64_tr_b16 v[10:11], v245 offset:43008\n"
    "v_mfma_f32_16x16x32_bf16 v[212:215], v[92:95], a[216:219], v[212:215]\n"
    "global_load_dwordx2 v[254:255], v250, s[20:21] sc1\n"
    "v_mfma_f32_16x16x32_bf16 v[216:219], v[88:91], a[168:171], v[216:219]\n"
    "s_nop 0\n"
    "ds_read_b64_tr_b16 v[12:13], v245 offset:45056\n"
    "ds_read_b64_tr_b16 v[14:15], v245 offset:47104\n"
    "v_mfma_f32_16x16x32_bf16 v[216:219], v[92:95], a[232:235], v[216:219]\n"
    "v_add_u32 v253, s98, v253\n"
    "v_mfma_f32_16x16x32_bf16 v[220:223], v[88:91], a[184:187], v[220:223]\n"
    "s_add_u32 m0, s52, 65536\n"
    "ds_read_b64_tr_b16 v[16:17], v245 offset:49152\n"
    "ds_read_b64_tr_b16 v[18:19], v245 offset:51200\n"
    "v_mfma_f32_16x16x32_bf16 v[220:223], v[92:95], a[248:251], v[220:223]\n"
    "s_waitcnt lgkmcnt(10)\n"
    "buffer_load_dwordx4 v253, s[72:75], s44 offen offset:0 lds\n"
    "v_mfma_f32_16x16x32_bf16 v[224:227], v[96:99], a[136:139], v[224:227]\n"
    "s_nop 0\n"
    "ds_read_b64_tr_b16 v[20:21], v245 offset:53248\n"
    "ds_read_b64_tr_b16 v[22:23], v245 offset:55296\n"
    "v_mfma_f32_16x16x32_bf16 v[224:227], v[100:103], a[200:203], v[224:227]\n"
    "buffer_load_dwordx4 v253, s[72:75], s45 offen offset:1024 lds\n"
    "v_mfma_f32_16x16x32_bf16 v[228:231], v[96:99], a[152:155], v[228:231]\n"
    "s_nop 0\n"
    "ds_read_b64_tr_b16 v[24:25], v245 offset:57344\n"
    "ds_read_b64_tr_b16 v[26:27], v245 offset:59392\n"
    "v_mfma_f32_16x16x32_bf16 v[228:231], v[100:103], a[216:219], v[228:231]\n"
    "buffer_load_dwordx4 v253, s[72:75], s46 offen offset:2048 lds\n"
    "v_mfma_f32_16x16x32_bf16 v[232:235], v[96:99], a[168:171], v[232:235]\n"
    "s_nop 0\n"
    "ds_read_b64_tr_b16 v[28:29], v245 offset:61440\n"
    "ds_read_b64_tr_b16 v[30:31], v245 offset:63488\n"
    "v_mfma_f32_16x16x32_bf16 v[232:235], v[100:103], a[232:235], v[232:235]\n"
    "v_mfma_f32_16x16x32_bf16 v[236:239], v[96:99], a[184:187], v[236:239]\n"
    "s_nop 0\n"
    "ds_read_b64_tr_b16 v[32:33], v246 offset:32768\n"
    "ds_read_b64_tr_b16 v[34:35], v246 offset:34816\n"
    "v_mfma_f32_16x16x32_bf16 v[236:239], v[100:103], a[248:251], v[236:239]\n"
    "s_waitcnt lgkmcnt(14)\n"
    "buffer_load_dwordx4 v253, s[72:75], s47 offen offset:3072 lds\n"
    "v_mfma_f32_16x16x32_bf16 v[64:67], v[0:3], v[112:115], 0\n"
    "s_add_u32 m0, s52, 69632\n"
    "ds_read_b64_tr_b16 v[36:37], v246 offset:36864\n"
    "ds_read_b64_tr_b16 v[38:39], v246 offset:38912\n"
    "v_mfma_f32_16x16x32_bf16 v[64:67], v[4:7], v[116:119], v[64:67]\n"
    "s_waitcnt lgkmcnt(12)\n"
    "v_mfma_f32_16x16x32_bf16 v[64:67], v[8:11], v[120:123], v[64:67]\n"
    "s_nop 0\n"
    "ds_read_b64_tr_b16 v[40:41], v246 offset:40960\n"
    "ds_read_b64_tr_b16 v[42:43], v246 offset:43008\n"
    "v_mfma_f32_16x16x32_bf16 v[64:67], v[12:15], v[124:127], v[64:67]\n"
    "s_waitcnt lgkmcnt(10)\n"
    "buffer_load_dwordx4 v253, s[72:75], s48 offen offset:0 lds\n"
    "v_mfma_f32_16x16x32_bf16 v[64:67], v[16:19], v[128:131], v[64:67]\n"
    "s_nop 0\n"
    "ds_read_b64_tr_b16 v[44:45], v246 offset:45056\n"
    "ds_read_b64_tr_b16 v[46:47], v246 offset:47104\n"
    "v_mfma_f32_16x16x32_bf16 v[64:67], v[20:23], v[132:135], v[64:67]\n"
    "s_waitcnt lgkmcnt(8)\n"
    "v_mfma_f32_16x16x32_bf16 v[64:67], v[24:27], v[136:139], v[64:67]\n"
    "s_nop 0\n"
    "ds_read_b64_tr_b16 v[48:49], v246 offset:49152\n"
    "ds_read_b64_tr_b16 v[50:51], v246 offset:51200\n"
    "v_mfma_f32_16x16x32_bf16 v[64:67], v[28:31], v[140:143], v[64:67]\n"
    "buffer_load_dwordx4 v253, s[72:75], s49 offen offset:1024 lds\n"
    "v_mfma_f32_16x16x32_bf16 v[68:71], v[0:3], v[144:147], 0\n"
    "s_nop 0\n"
    "ds_read_b64_tr_b16 v[52:53], v246 offset:53248\n"
    "ds_read_b64_tr_b16 v[54:55], v246 offset:55296\n"
    "v_mfma_f32_16x16x32_bf16 v[68:71], v[4:7], v[148:151], v[68:71]\n"
    "v_mfma_f32_16x16x32_bf16 v[68:71], v[8:11], v[152:155], v[68:71]\n"
    "s_nop 0\n"
    "ds_read_b64_tr_b16 v[56:57], v246 offset:57344\n"
    "ds_read_b64_tr_b16 v[58:59], v246 offset:59392\n"
    "v_mfma_f32_16x16x32_bf16 v[68:71], v[12:15], v[156:159], v[68:71]\n"
    "buffer_load_dwordx4 v253, s[72:75], s50 offen offset:2048 lds\n"
    "v_mfma_f32_16x16x32_bf16 v[68:71], v[16:19], v[160:163], v[68:71]\n"
    "s_nop 0\n"
    "ds_read_b64_tr_b16 v[60:61], v246 offset:61440\n"
    "ds_read_b64_tr_b16 v[62:63], v246 offset:63488\n"
    "v_mfma_f32_16x16x32_bf16 v[68:71], v[20:23], v[164:167], v[68:71]\n"
    "v_mfma_f32_16x16x32_bf16 v[68:71], v[24:27], v[168:171], v[68:71]\n"
    "s_nop 0\n"
    "ds_read_b64_tr_b16 v[0:1], v245 offset:33280\n"
    "ds_read_b64_tr_b16 v[2:3], v245 offset:35328\n"
    "v_mfma_f32_16x16x32_bf16 v[68:71], v[28:31], v[172:175], v[68:71]\n"
    "s_waitcnt lgkmcnt(14)\n"
    "buffer_load_dwordx4 v253, s[72:75], s51 offen offset:3072 lds\n"
    "v_mfma_f32_16x16x32_bf16 v[72:75], v[32:35], v[112:115], 0\n"
    "s_nop 0\n"
    "ds_read_b64_tr_b16 v[4:5], v245 offset:37376\n"
    "ds_read_b64_tr_b16 v[6:7], v245 offset:39424\n"
    "v_mfma_f32_16x16x32_bf16 v[72:75], v[36:39], v[116:119], v[72:75]\n"
    "s_waitcnt lgkmcnt(12)\n"
    "v_add_u32 v253, s99, v253\n"
    "v_cvt_pk_bf16_f32 v80, v64, v65\n"
    "v_mfma_f32_16x16x32_bf16 v[72:75], v[40:43], v[120:123], v[72:75]\n"
    "s_add_u32 m0, s52, 0\n"
    "ds_read_b64_tr_b16 v[8:9], v245 offset:41472\n"
    "ds_read_b64_tr_b16 v[10:11], v245 offset:43520\n"
    "v_mfma_f32_16x16x32_bf16 v[72:75], v[44:47], v[124:127], v[72:75]\n"
    "s_waitcnt lgkmcnt(10)\n"
    "v_cvt_pk_bf16_f32 v81, v66, v67\n"
    "v_mfma_f32_16x16x32_bf16 v[72:75], v[48:51], v[128:131], v[72:75]\n"
    "s_nop 0\n"
    "ds_read_b64_tr_b16 v[12:13], v245 offset:45568\n"
    "ds_read_b64_tr_b16 v[14:15], v245 offset:47616\n"
    "v_mfma_f32_16x16x32_bf16 v[72:75], v[52:55], v[132:135], v[72:75]\n"
    "s_waitcnt lgkmcnt(8)\n"
    "buffer_load_dwordx4 v253, s[72:75], s44 offen offset:0 lds\n"
    "v_mfma_f32_16x16x32_bf16 v[72:75], v[56:59], v[136:139], v[72:75]\n"
    "s_add_u32 s96, s68, 0\n"
    "ds_read_b64_tr_b16 v[16:17], v245 offset:49664\n"
    "ds_read_b64_tr_b16 v[18:19], v245 offset:51712\n"
    "v_mfma_f32_16x16x32_bf16 v[72:75], v[60:63], v[140:143], v[72:75]\n"
    "buffer_load_dwordx4 v253, s[72:75], s45 offen offset:1024 lds\n"
    "v_mfma_f32_16x16x32_bf16 v[76:79], v[32:35], v[144:147], 0\n"
    "s_nop 0\n"
    "ds_read_b64_tr_b16 v[20:21], v245 offset:53760\n"
    "ds_read_b64_tr_b16 v[22:23], v245 offset:55808\n"
    "v_mfma_f32_16x16x32_bf16 v[76:79], v[36:39], v[148:151], v[76:79]\n"
    "v_cvt_pk_bf16_f32 v82, v68, v69\n"
    "v_cvt_pk_bf16_f32 v83, v70, v71\n"
    "v_mfma_f32_16x16x32_bf16 v[76:79], v[40:43], v[152:155], v[76:79]\n"
    "s_nop 0\n"
    "ds_read_b64_tr_b16 v[24:25], v245 offset:57856\n"
    "ds_read_b64_tr_b16 v[26:27], v245 offset:59904\n"
    "v_mfma_f32_16x16x32_bf16 v[76:79], v[44:47], v[156:159], v[76:79]\n"
    "buffer_store_dwordx4 v[80:83], v242, s[88:91], s96 offen offset:0 sc1\n"
    "v_mfma_f32_16x16x32_bf16 v[76:79], v[48:51], v[160:163], v[76:79]\n"
    "s_nop 0\n"
    "ds_read_b64_tr_b16 v[28:29], v245 offset:61952\n"
    "ds_read_b64_tr_b16 v[30:31], v245 offset:64000\n"
    "v_mfma_f32_16x16x32_bf16 v[76:79], v[52:55], v[164:167], v[76:79]\n"
    "buffer_load_dwordx4 v253, s[72:75], s46 offen offset:2048 lds\n"
    "v_mfma_f32_16x16x32_bf16 v[76:79], v[56:59], v[168:171], v[76:79]\n"
    "s_nop 0\n"
    "ds_read_b64_tr_b16 v[32:33], v246 offset:33280\n"
    "ds_read_b64_tr_b16 v[34:35], v246 offset:35328\n"
    "v_mfma_f32_16x16x32_bf16 v[76:79], v[60:63], v[172:175], v[76:79]\n"
    "s_waitcnt lgkmcnt(14)\n"
    "buffer_load_dwordx4 v253, s[72:75], s47 offen offset:3072 lds\n"
    "v_mfma_f32_16x16x32_bf16 v[64:67], v[0:3], v[112:115], 0\n"
    "s_add_u32 m0, s52, 4096\n"
    "ds_read_b64_tr_b16 v[36:37], v246 offset:37376\n"
    "ds_read_b64_tr_b16 v[38:39], v246 offset:39424\n"
    "v_mfma_f32_16x16x32_bf16 v[64:67], v[4:7], v[116:119], v[64:67]\n"
    "s_waitcnt lgkmcnt(12)\n"
    "v_cvt_pk_bf16_f32 v84, v72, v73\n"
    "v_cvt_pk_bf16_f32 v85, v74, v75\n"
    "v_mfma_f32_16x16x32_bf16 v[64:67], v[8:11], v[120:123], v[64:67]\n"
    "s_nop 0\n"
    "ds_read_b64_tr_b16 v[40:41], v246 offset:41472\n"
    "ds_read_b64_tr_b16 v[42:43], v246 offset:43520\n"
    "v_mfma_f32_16x16x32_bf16 v[64:67], v[12:15], v[124:127], v[64:67]\n"
    "s_waitcnt lgkmcnt(10)\n"
    "buffer_load_dwordx4 v253, s[72:75], s48 offen offset:0 lds\n"
    "v_mfma_f32_16x16x32_bf16 v[64:67], v[16:19], v[128:131], v[64:67]\n"
    "s_nop 0\n"
    "ds_read_b64_tr_b16 v[44:45], v246 offset:45568\n"
    "ds_read_b64_tr_b16 v[46:47], v246 offset:47616\n"
    "v_mfma_f32_16x16x32_bf16 v[64:67], v[20:23], v[132:135], v[64:67]\n"
    "s_waitcnt lgkmcnt(8)\n"
    "v_mfma_f32_16x16x32_bf16 v[64:67], v[24:27], v[136:139], v[64:67]\n"
    "s_add_u32 s97, s68, 16384\n"
    "ds_read_b64_tr_b16 v[48:49], v246 offset:49664\n"
    "ds_read_b64_tr_b16 v[50:51], v246 offset:51712\n"
    "v_mfma_f32_16x16x32_bf16 v[64:67], v[28:31], v[140:143], v[64:67]\n"
    "buffer_load_dwordx4 v253, s[72:75], s49 offen offset:1024 lds\n"
    "v_mfma_f32_16x16x32_bf16 v[68:71], v[0:3], v[144:147], 0\n"
    "s_nop 0\n"
    "ds_read_b64_tr_b16 v[52:53], v246 offset:53760\n"
    "ds_read_b64_tr_b16 v[54:55], v246 offset:55808\n"
    "v_mfma_f32_16x16x32_bf16 v[68:71], v[4:7], v[148:151], v[68:71]\n"
    "v_cvt_pk_bf16_f32 v86, v76, v77\n"
    "v_cvt_pk_bf16_f32 v87, v78, v79\n"
    "v_mfma_f32_16x16x32_bf16 v[68:71], v[8:11], v[152:155], v[68:71]\n"
    "s_nop 0\n"
    "ds_read_b64_tr_b16 v[56:57], v246 offset:57856\n"
    "ds_read_b64_tr_b16 v[58:59], v246 offset:59904\n"
    "v_mfma_f32_16x16x32_bf16 v[68:71], v[12:15], v[156:159], v[68:71]\n"
    "buffer_store_dwordx4 v[84:87], v242, s[88:91], s97 offen offset:0 sc1\n"
    "v_mfma_f32_16x16x32_bf16 v[68:71], v[16:19], v[160:163], v[68:71]\n"
    "s_nop 0\n"
    "ds_read_b64_tr_b16 v[60:61], v246 offset:61952\n"
    "ds_read_b64_tr_b16 v[62:63], v246 offset:64000\n"
    "v_mfma_f32_16x16x32_bf16 v[68:71], v[20:23], v[164:167], v[68:71]\n"
    "s_waitcnt vmcnt(8)\n"
    "s_barrier\n"
    "buffer_load_dwordx4 v253, s[72:75], s50 offen offset:2048 lds\n"
    "v_mfma_f32_16x16x32_bf16 v[68:71], v[24:27], v[168:171], v[68:71]\n"
    "v_cmp_eq_u64 vcc, s[70:71], v[254:255]\n"
    "v_cmp_eq_u32 s[58:59], 2, v243\n"
    "s_andn2_b64 vcc, s[58:59], vcc\n"
    "s_cselect_b32 s58, 2, 0\n"
    "s_or_b32 s28, s28, s58\n"
    "ds_read_b64_tr_b16 v[0:1], v240 offset:0\n"
    "ds_read_b64_tr_b16 v[2:3], v240 offset:2048\n"
    "v_mfma_f32_16x16x32_bf16 v[68:71], v[28:31], v[172:175], v[68:71]\n"
    "s_waitcnt lgkmcnt(14)\n"
    "buffer_load_dwordx4 v253, s[72:75], s51 offen offset:3072 lds\n"
    "v_mfma_f32_16x16x32_bf16 v[72:75], v[32:35], v[112:115], 0\n"
    "s_sub_u32 s31, s31, 1024\n"
    "ds_read_b64_tr_b16 v[4:5], v240 offset:4096\n"
    "ds_read_b64_tr_b16 v[6:7], v240 offset:6144\n"
    "v_mfma_f32_16x16x32_bf16 v[72:75], v[36:39], v[116:119], v[72:75]\n"
    "s_waitcnt lgkmcnt(12)\n"
    "v_cvt_pk_bf16_f32 v80, v64, v65\n"
    "v_cvt_pk_bf16_f32 v81, v66, v67\n"
    "v_mfma_f32_16x16x32_bf16 v[72:75], v[40:43], v[120:123], v[72:75]\n"
    "s_min_u32 s31, s31, 6144\n"
    "ds_read_b64_tr_b16 v[8:9], v240 offset:8192\n"
    "ds_read_b64_tr_b16 v[10:11], v240 offset:10240\n"
    "v_mfma_f32_16x16x32_bf16 v[72:75], v[44:47], v[124:127], v[72:75]\n"
    "s_waitcnt lgkmcnt(10)\n"
    "v_mfma_f32_16x16x32_bf16 v[72:75], v[48:51], v[128:131], v[72:75]\n"
    "s_bfe_u32 s22, s28, 65536\n"
    "ds_read_b64_tr_b16 v[12:13], v240 offset:12288\n"
    "ds_read_b64_tr_b16 v[14:15], v240 offset:14336\n"
    "v_mfma_f32_16x16x32_bf16 v[72:75], v[52:55], v[132:135], v[72:75]\n"
    "s_waitcnt lgkmcnt(8)\n"
    "v_mfma_f32_16x16x32_bf16 v[72:75], v[56:59], v[136:139], v[72:75]\n"
    "s_add_u32 s96, s68, 32768\n"
    "ds_read_b64_tr_b16 v[16:17], v240 offset:16384\n"
    "ds_read_b64_tr_b16 v[18:19], v240 offset:18432\n"
    "v_mfma_f32_16x16x32_bf16 v[72:75], v[60:63], v[140:143], v[72:75]\n"
    "s_lshl_b32 s22, s22, 30\n"
    "v_mfma_f32_16x16x32_bf16 v[76:79], v[32:35], v[144:147], 0\n"
    "s_add_u32 s22, s22, s31\n"
    "ds_read_b64_tr_b16 v[20:21], v240 offset:20480\n"
    "ds_read_b64_tr_b16 v[22:23], v240 offset:22528\n"
    "v_mfma_f32_16x16x32_bf16 v[76:79], v[36:39], v[148:151], v[76:79]\n"
    "s_add_u32 s56, s22, 7168\n"
    "v_cvt_pk_bf16_f32 v82, v68, v69\n"
    "v_cvt_pk_bf16_f32 v83, v70, v71\n"
    "v_mfma_f32_16x16x32_bf16 v[76:79], v[40:43], v[152:155], v[76:79]\n"
    "s_add_u32 s23, s22, 458752\n"
    "ds_read_b64_tr_b16 v[24:25], v240 offset:24576\n"
    "ds_read_b64_tr_b16 v[26:27], v240 offset:26624\n"
    "v_mfma_f32_16x16x32_bf16 v[76:79], v[44:47], v[156:159], v[76:79]\n"
    "s_add_u32 s57, s56, 458752\n"
    "buffer_store_dwordx4 v[80:83], v242, s[88:91], s96 offen offset:0 sc1\n"
    "v_mfma_f32_16x16x32_bf16 v[76:79], v[48:51], v[160:163], v[76:79]\n"
    "s_nop 0\n"
    "ds_read_b64_tr_b16 v[28:29], v240 offset:28672\n"
    "ds_read_b64_tr_b16 v[30:31], v240 offset:30720\n"
    "v_mfma_f32_16x16x32_bf16 v[76:79], v[52:55], v[164:167], v[76:79]\n"
    "buffer_load_dwordx4 a[8:11], v242, s[80:83], s22 offen offset:0 sc1\n"
    "v_mfma_f32_16x16x32_bf16 v[76:79], v[56:59], v[168:171], v[76:79]\n"
    "s_nop 0\n"
    "ds_read_b64_tr_b16 v[32:33], v241 offset:0\n"
    "ds_read_b64_tr_b16 v[34:35], v241 offset:2048\n"
    "v_mfma_f32_16x16x32_bf16 v[76:79], v[60:63], v[172:175], v[76:79]\n"
    "s_waitcnt lgkmcnt(14)\n"
    "buffer_load_dwordx4 a[24:27], v242, s[80:83], s23 offen offset:0 sc1\n"
    "v_mfma_f32_16x16x32_bf16 v[64:67], v[0:3], v[112:115], 0\n"
    "s_nop 0\n"
    "ds_read_b64_tr_b16 v[36:37], v241 offset:4096\n"
    "ds_read_b64_tr_b16 v[38:39], v241 offset:6144\n"
    "v_mfma_f32_16x16x32_bf16 v[64:67], v[4:7], v[116:119], v[64:67]\n"
    "s_waitcnt lgkmcnt(12)\n"
    "buffer_load_dwordx4 a[40:43], v242, s[80:83], s56 offen offset:0 sc1\n"
    "v_mfma_f32_16x16x32_bf16 v[64:67], v[8:11], v[120:123], v[64:67]\n"
    "s_nop 0\n"
    "ds_read_b64_tr_b16 v[40:41], v241 offset:8192\n"
    "ds_read_b64_tr_b16 v[42:43], v241 offset:10240\n"
    "v_mfma_f32_16x16x32_bf16 v[64:67], v[12:15], v[124:127], v[64:67]\n"
    "s_waitcnt lgkmcnt(10)\n"
    "v_cvt_pk_bf16_f32 v84, v72, v73\n"
    "v_cvt_pk_bf16_f32 v85, v74, v75\n"
    "v_mfma_f32_16x16x32_bf16 v[64:67], v[16:19], v[128:131], v[64:67]\n"
    "s_nop 0\n"
    "ds_read_b64_tr_b16 v[44:45], v241 offset:12288\n"
    "ds_read_b64_tr_b16 v[46:47], v241 offset:14336\n"
    "v_mfma_f32_16x16x32_bf16 v[64:67], v[20:23], v[132:135], v[64:67]\n"
    "s_waitcnt lgkmcnt(8)\n"
    "buffer_load_dwordx4 a[56:59], v242, s[80:83], s57 offen offset:0 sc1\n"
    "v_mfma_f32_16x16x32_bf16 v[64:67], v[24:27], v[136:139], v[64:67]\n"
    "s_add_u32 s97, s68, 49152\n"
    "ds_read_b64_tr_b16 v[48:49], v241 offset:16384\n"
    "ds_read_b64_tr_b16 v[50:51], v241 offset:18432\n"
    "v_mfma_f32_16x16x32_bf16 v[64:67], v[28:31], v[140:143], v[64:67]\n"
    "s_add_u32 m0, s10, 0\n"
    "v_mfma_f32_16x16x32_bf16 v[68:71], v[0:3], v[144:147], 0\n"
    "s_nop 0\n"
    "ds_read_b64_tr_b16 v[52:53], v241 offset:20480\n"
    "ds_read_b64_tr_b16 v[54:55], v241 offset:22528\n"
    "v_mfma_f32_16x16x32_bf16 v[68:71], v[4:7], v[148:151], v[68:71]\n"
    "buffer_load_dwordx4 v252, s[76:79], s11 offen offset:0 lds\n"
    "v_mfma_f32_16x16x32_bf16 v[68:71], v[8:11], v[152:155], v[68:71]\n"
    "s_nop 0\n"
    "ds_read_b64_tr_b16 v[56:57], v241 offset:24576\n"
    "ds_read_b64_tr_b16 v[58:59], v241 offset:26624\n"
    "v_mfma_f32_16x16x32_bf16 v[68:71], v[12:15], v[156:159], v[68:71]\n"
    "v_cvt_pk_bf16_f32 v86, v76, v77\n"
    "v_cvt_pk_bf16_f32 v87, v78, v79\n"
    "v_mfma_f32_16x16x32_bf16 v[68:71], v[16:19], v[160:163], v[68:71]\n"
    "s_nop 0\n"
    "ds_read_b64_tr_b16 v[60:61], v241 offset:28672\n"
    "ds_read_b64_tr_b16 v[62:63], v241 offset:30720\n"
    "v_mfma_f32_16x16x32_bf16 v[68:71], v[20:23], v[164:167], v[68:71]\n"
    "buffer_store_dwordx4 v[84:87], v242, s[88:91], s97 offen offset:0 sc1\n"
    "v_mfma_f32_16x16x32_bf16 v[68:71], v[24:27], v[168:171], v[68:71]\n"
    "s_nop 0\n"
    "ds_read_b64_tr_b16 v[0:1], v240 offset:512\n"
    "ds_read_b64_tr_b16 v[2:3], v240 offset:2560\n"
    "v_mfma_f32_16x16x32_bf16 v[68:71], v[28:31], v[172:175], v[68:71]\n"
    "s_waitcnt lgkmcnt(14)\n"
    "buffer_load_dwordx4 v252, s[76:79], s12 offen offset:1024 lds\n"
    "v_mfma_f32_16x16x32_bf16 v[72:75], v[32:35], v[112:115], 0\n"
    "s_nop 0\n"
    "ds_read_b64_tr_b16 v[4:5], v240 offset:4608\n"
    "ds_read_b64_tr_b16 v[6:7], v240 offset:6656\n"
    "v_mfma_f32_16x16x32_bf16 v[72:75], v[36:39], v[116:119], v[72:75]\n"
    "s_waitcnt lgkmcnt(12)\n"
    "buffer_load_dwordx4 v252, s[76:79], s13 offen offset:2048 lds\n"
    "v_mfma_f32_16x16x32_bf16 v[72:75], v[40:43], v[120:123], v[72:75]\n"
    "s_nop 0\n"
    "ds_read_b64_tr_b16 v[8:9], v240 offset:8704\n"
    "ds_read_b64_tr_b16 v[10:11], v240 offset:10752\n"
    "v_mfma_f32_16x16x32_bf16 v[72:75], v[44:47], v[124:127], v[72:75]\n"
    "s_waitcnt lgkmcnt(10)\n"
    "v_cvt_pk_bf16_f32 v80, v64, v65\n"
    "v_cvt_pk_bf16_f32 v81, v66, v67\n"
    "v_mfma_f32_16x16x32_bf16 v[72:75], v[48:51], v[128:131], v[72:75]\n"
    "s_nop 0\n"
    "ds_read_b64_tr_b16 v[12:13], v240 offset:12800\n"
    "ds_read_b64_tr_b16 v[14:15], v240 offset:14848\n"
    "v_mfma_f32_16x16x32_bf16 v[72:75], v[52:55], v[132:135], v[72:75]\n"
    "s_waitcnt lgkmcnt(8)\n"
    "buffer_load_dwordx4 v252, s[76:79], s16 offen offset:3072 lds\n"
    "v_mfma_f32_16x16x32_bf16 v[72:75], v[56:59], v[136:139], v[72:75]\n"
    "s_add_u32 s96, s53, 0\n"
    "ds_read_b64_tr_b16 v[16:17], v240 offset:16896\n"
    "ds_read_b64_tr_b16 v[18:19], v240 offset:18944\n"
    "v_mfma_f32_16x16x32_bf16 v[72:75], v[60:63], v[140:143], v[72:75]\n"
    "v_add_u32 v252, 28672, v252\n"
    "v_mfma_f32_16x16x32_bf16 v[76:79], v[32:35], v[144:147], 0\n"
    "s_add_u32 m0, s10, 4096\n"
    "ds_read_b64_tr_b16 v[20:21], v240 offset:20992\n"
    "ds_read_b64_tr_b16 v[22:23], v240 offset:23040\n"
    "v_mfma_f32_16x16x32_bf16 v[76:79], v[36:39], v[148:151], v[76:79]\n"
    "buffer_load_dwordx4 v252, s[76:79], s11 offen offset:0 lds\n"
    "v_mfma_f32_16x16x32_bf16 v[76:79], v[40:43], v[152:155], v[76:79]\n"
    "s_nop 0\n"
    "ds_read_b64_tr_b16 v[24:25], v240 offset:25088\n"
    "ds_read_b64_tr_b16 v[26:27], v240 offset:27136\n"
    "v_mfma_f32_16x16x32_bf16 v[76:79], v[44:47], v[156:159], v[76:79]\n"
    "v_cvt_pk_bf16_f32 v82, v68, v69\n"
    "v_cvt_pk_bf16_f32 v83, v70, v71\n"
    "v_mfma_f32_16x16x32_bf16 v[76:79], v[48:51], v[160:163], v[76:79]\n"
    "s_nop 0\n"
    "ds_read_b64_tr_b16 v[28:29], v240 offset:29184\n"
    "ds_read_b64_tr_b16 v[30:31], v240 offset:31232\n"
    "v_mfma_f32_16x16x32_bf16 v[76:79], v[52:55], v[164:167], v[76:79]\n"
    "buffer_store_dwordx4 v[80:83], v242, s[88:91], s96 offen offset:0 sc1\n"
    "v_mfma_f32_16x16x32_bf16 v[76:79], v[56:59], v[168:171], v[76:79]\n"
    "s_nop 0\n"
    "ds_read_b64_tr_b16 v[32:33], v241 offset:512\n"
    "ds_read_b64_tr_b16 v[34:35], v241 offset:2560\n"
    "v_mfma_f32_16x16x32_bf16 v[76:79], v[60:63], v[172:175], v[76:79]\n"
    "s_waitcnt lgkmcnt(14)\n"
    "buffer_load_dwordx4 v252, s[76:79], s12 offen offset:1024 lds\n"
    "v_mfma_f32_16x16x32_bf16 v[64:67], v[0:3], v[112:115], 0\n"
    "s_nop 0\n"
    "ds_read_b64_tr_b16 v[36:37], v241 offset:4608\n"
    "ds_read_b64_tr_b16 v[38:39], v241 offset:6656\n"
    "v_mfma_f32_16x16x32_bf16 v[64:67], v[4:7], v[116:119], v[64:67]\n"
    "s_waitcnt lgkmcnt(12)\n"
    "buffer_load_dwordx4 v252, s[76:79], s13 offen offset:2048 lds\n"
    "v_mfma_f32_16x16x32_bf16 v[64:67], v[8:11], v[120:123], v[64:67]\n"
    "s_nop 0\n"
    "ds_read_b64_tr_b16 v[40:41], v241 offset:8704\n"
    "ds_read_b64_tr_b16 v[42:43], v241 offset:10752\n"
    "v_mfma_f32_16x16x32_bf16 v[64:67], v[12:15], v[124:127], v[64:67]\n"
    "s_waitcnt lgkmcnt(10)\n"
    "v_cvt_pk_bf16_f32 v84, v72, v73\n"
    "v_cvt_pk_bf16_f32 v85, v74, v75\n"
    "v_mfma_f32_16x16x32_bf16 v[64:67], v[16:19], v[128:131], v[64:67]\n"
    "s_nop 0\n"
    "ds_read_b64_tr_b16 v[44:45], v241 offset:12800\n"
    "ds_read_b64_tr_b16 v[46:47], v241 offset:14848\n"
    "v_mfma_f32_16x16x32_bf16 v[64:67], v[20:23], v[132:135], v[64:67]\n"
    "s_waitcnt lgkmcnt(8)\n"
    "buffer_load_dwordx4 v252, s[76:79], s16 offen offset:3072 lds\n"
    "v_mfma_f32_16x16x32_bf16 v[64:67], v[24:27], v[136:139], v[64:67]\n"
    "s_add_u32 s97, s53, 16384\n"
    "ds_read_b64_tr_b16 v[48:49], v241 offset:16896\n"
    "ds_read_b64_tr_b16 v[50:51], v241 offset:18944\n"
    "v_mfma_f32_16x16x32_bf16 v[64:67], v[28:31], v[140:143], v[64:67]\n"
    "v_add_u32 v252, 28672, v252\n"
    "v_mfma_f32_16x16x32_bf16 v[68:71], v[0:3], v[144:147], 0\n"
    "s_lshr_b32 s19, s67, 8\n"
    "ds_read_b64_tr_b16 v[52:53], v241 offset:20992\n"
    "ds_read_b64_tr_b16 v[54:55], v241 offset:23040\n"
    "v_mfma_f32_16x16x32_bf16 v[68:71], v[4:7], v[148:151], v[68:71]\n"
    "buffer_store_dwordx2 v[248:249], v247, s[36:39], s19 offen offset:0 sc1\n"
    "v_mfma_f32_16x16x32_bf16 v[68:71], v[8:11], v[152:155], v[68:71]\n"
    "s_nop 0\n"
    "ds_read_b64_tr_b16 v[56:57], v241 offset:25088\n"
    "ds_read_b64_tr_b16 v[58:59], v241 offset:27136\n"
    "v_mfma_f32_16x16x32_bf16 v[68:71], v[12:15], v[156:159], v[68:71]\n"
    "v_cvt_pk_bf16_f32 v86, v76, v77\n"
    "v_cvt_pk_bf16_f32 v87, v78, v79\n"
    "v_mfma_f32_16x16x32_bf16 v[68:71], v[16:19], v[160:163], v[68:71]\n"
    "s_nop 0\n"
    "ds_read_b64_tr_b16 v[60:61], v241 offset:29184\n"
    "ds_read_b64_tr_b16 v[62:63], v241 offset:31232\n"
    "v_mfma_f32_16x16x32_bf16 v[68:71], v[20:23], v[164:167], v[68:71]\n"
    "buffer_store_dwordx4 v[84:87], v242, s[88:91], s97 offen offset:0 sc1\n"
    "v_mfma_f32_16x16x32_bf16 v[68:71], v[24:27], v[168:171], v[68:71]\n"
    "global_load_dwordx2 v[254:255], v250, s[20:21] sc1\n"
    "v_mfma_f32_16x16x32_bf16 v[68:71], v[28:31], v[172:175], v[68:71]\n"
    "s_waitcnt lgkmcnt(12)\n"
    "v_mfma_f32_16x16x32_bf16 v[72:75], v[32:35], v[112:115], 0\n"
    "s_sub_u32 s31, s31, 1024\n"
    "v_cvt_pk_bf16_f32 v80, v64, v65\n"
    "v_cvt_pk_bf16_f32 v81, v66, v67\n"
    "v_mfma_f32_16x16x32_bf16 v[72:75], v[36:39], v[116:119], v[72:75]\n"
    "s_waitcnt lgkmcnt(8)\n"
    "v_mfma_f32_16x16x32_bf16 v[72:75], v[40:43], v[120:123], v[72:75]\n"
    "s_nop 0\n"
    "v_mfma_f32_16x16x32_bf16 v[72:75], v[44:47], v[124:127], v[72:75]\n"
    "s_waitcnt lgkmcnt(4)\n"
    "ds_read_b64_tr_b16 v[104:105], v244 offset:32768\n"
    "ds_read_b64_tr_b16 v[106:107], v244 offset:34816\n"
    "v_mfma_f32_16x16x32_bf16 v[72:75], v[48:51], v[128:131], v[72:75]\n"
    "s_min_u32 s31, s31, 6144\n"
    "ds_read_b64_tr_b16 v[108:109], v244 offset:36864\n"
    "ds_read_b64_tr_b16 v[110:111], v244 offset:38912\n"
    "v_mfma_f32_16x16x32_bf16 v[72:75], v[52:55], v[132:135], v[72:75]\n"
    "s_waitcnt lgkmcnt(4)\n"
    "v_mfma_f32_16x16x32_bf16 v[72:75], v[56:59], v[136:139], v[72:75]\n"
    "s_add_u32 s96, s53, 32768\n"
    "v_mfma_f32_16x16x32_bf16 v[72:75], v[60:63], v[140:143], v[72:75]\n"
    "s_bfe_u32 s22, s28, 65537\n"
    "v_mfma_f32_16x16x32_bf16 v[76:79], v[32:35], v[144:147], 0\n"
    "s_lshl_b32 s22, s22, 30\n"
    "v_cvt_pk_bf16_f32 v82, v68, v69\n"
    "v_cvt_pk_bf16_f32 v83, v70, v71\n"
    "v_mfma_f32_16x16x32_bf16 v[76:79], v[36:39], v[148:151], v[76:79]\n"
    "s_add_u32 s22, s22, s31\n"
    "buffer_store_dwordx4 v[80:83], v242, s[88:91], s96 offen offset:0 sc1\n"
    "v_mfma_f32_16x16x32_bf16 v[76:79], v[40:43], v[152:155], v[76:79]\n"
    "s_add_u32 s56, s22, 7168\n"
    "v_mfma_f32_16x16x32_bf16 v[76:79], v[44:47], v[156:159], v[76:79]\n"
    "s_add_u32 s23, s22, 458752\n"
    "ds_read_b64_tr_b16 v[88:89], v251 offset:32768\n"
    "ds_read_b64_tr_b16 v[90:91], v251 offset:34816\n"
    "v_mfma_f32_16x16x32_bf16 v[76:79], v[48:51], v[160:163], v[76:79]\n"
    "s_add_u32 s57, s56, 458752\n"
    "ds_read_b64_tr_b16 v[92:93], v251 offset:36864\n"
    "ds_read_b64_tr_b16 v[94:95], v251 offset:38912\n"
    "v_mfma_f32_16x16x32_bf16 v[76:79], v[52:55], v[164:167], v[76:79]\n"
    "buffer_load_dwordx4 a[72:75], v242, s[80:83], s22 offen offset:0 sc1\n"
    "v_mfma_f32_16x16x32_bf16 v[76:79], v[56:59], v[168:171], v[76:79]\n"
    "buffer_load_dwordx4 a[88:91], v242, s[80:83], s23 offen offset:0 sc1\n"
    "v_mfma_f32_16x16x32_bf16 v[76:79], v[60:63], v[172:175], v[76:79]\n"
    "s_waitcnt lgkmcnt(4)\n"
    "buffer_load_dwordx4 a[104:107], v242, s[80:83], s56 offen offset:0 sc1\n"
    "v_mfma_f32_16x16x32_bf16 v[176:179], v[104:107], a[12:15], v[176:179]\n"
    "buffer_load_dwordx4 a[120:123], v242, s[80:83], s57 offen offset:0 sc1\n"
    "v_mfma_f32_16x16x32_bf16 v[176:179], v[108:111], a[76:79], v[176:179]\n"
    "s_lshr_b32 s18, s68, 8\n"
    "v_cvt_pk_bf16_f32 v84, v72, v73\n"
    "v_cvt_pk_bf16_f32 v85, v74, v75\n"
    "v_mfma_f32_16x16x32_bf16 v[180:183], v[104:107], a[28:31], v[180:183]\n"
    "v_mfma_f32_16x16x32_bf16 v[180:183], v[108:111], a[92:95], v[180:183]\n"
    "s_nop 0\n"
    "ds_read_b64_tr_b16 v[96:97], v244 offset:33280\n"
    "ds_read_b64_tr_b16 v[98:99], v244 offset:35328\n"
    "v_mfma_f32_16x16x32_bf16 v[184:187], v[104:107], a[44:47], v[184:187]\n"
    "s_nop 0\n"
    "ds_read_b64_tr_b16 v[100:101], v244 offset:37376\n"
    "ds_read_b64_tr_b16 v[102:103], v244 offset:39424\n"
    "v_mfma_f32_16x16x32_bf16 v[184:187], v[108:111], a[108:111], v[184:187]\n"
    "v_mfma_f32_16x16x32_bf16 v[188:191], v[104:107], a[60:63], v[188:191]\n"
    "s_add_u32 s97, s53, 49152\n"
    "v_mfma_f32_16x16x32_bf16 v[188:191], v[108:111], a[124:127], v[188:191]\n"
    "s_waitcnt lgkmcnt(4)\n"
    "v_mfma_f32_16x16x32_bf16 v[192:195], v[88:91], a[12:15], v[192:195]\n"
    "v_cvt_pk_bf16_f32 v86, v76, v77\n"
    "v_cvt_pk_bf16_f32 v87, v78, v79\n"
    "v_mfma_f32_16x16x32_bf16 v[192:195], v[92:95], a[76:79], v[192:195]\n"
    "buffer_store_dwordx4 v[84:87], v242, s[88:91], s97 offen offset:0 sc1\n"
    "v_mfma_f32_16x16x32_bf16 v[196:199], v[88:91], a[28:31], v[196:199]\n"
    "v_mfma_f32_16x16x32_bf16 v[196:199], v[92:95], a[92:95], v[196:199]\n"
    "s_nop 0\n"
    "ds_read_b64_tr_b16 v[104:105], v251 offset:33280\n"
    "ds_read_b64_tr_b16 v[106:107], v251 offset:35328\n"
    "v_mfma_f32_16x16x32_bf16 v[200:203], v[88:91], a[44:47], v[200:203]\n"
    "s_nop 0\n"
    "ds_read_b64_tr_b16 v[108:109], v251 offset:37376\n"
    "ds_read_b64_tr_b16 v[110:111], v251 offset:39424\n"
    "v_mfma_f32_16x16x32_bf16 v[200:203], v[92:95], a[108:111], v[200:203]\n"
    "s_waitcnt vmcnt(6)\n"
    "s_barrier\n"
    "v_mfma_f32_16x16x32_bf16 v[204:207], v[88:91], a[60:63], v[204:207]\n"
    "v_cmp_eq_u64 vcc, s[70:71], v[254:255]\n"
    "v_cmp_eq_u32 s[58:59], 3, v243\n"
    "s_andn2_b64 vcc, s[58:59], vcc\n"
    "s_cselect_b32 s58, 4, 0\n"
    "s_or_b32 s28, s28, s58\n"
    "ds_read_b64_tr_b16 v[0:1], v245 offset:0\n"
    "ds_read_b64_tr_b16 v[2:3], v245 offset:2048\n"
    "v_mfma_f32_16x16x32_bf16 v[204:207], v[92:95], a[124:127], v[204:207]\n"
    "s_waitcnt lgkmcnt(6)\n"
    "buffer_store_dwordx2 v[248:249], v247, s[36:39], s18 offen offset:0 sc1\n"
    "v_mfma_f32_16x16x32_bf16 v[208:211], v[96:99], a[12:15], v[208:211]\n"
    "s_nop 0\n"
    "ds_read_b64_tr_b16 v[4:5], v245 offset:4096\n"
    "ds_read_b64_tr_b16 v[6:7], v245 offset:6144\n"
    "v_mfma_f32_16x16x32_bf16 v[208:211], v[100:103], a[76:79], v[208:211]\n"
    "global_load_dwordx2 v[254:255], v250, s[20:21] sc1\n"
    "v_mfma_f32_16x16x32_bf16 v[212:215], v[96:99], a[28:31], v[212:215]\n"
    "s_sub_u32 s31, s31, 1024\n"
    "ds_read_b64_tr_b16 v[8:9], v245 offset:8192\n"
    "ds_read_b64_tr_b16 v[10:11], v245 offset:10240\n"
    "v_mfma_f32_16x16x32_bf16 v[212:215], v[100:103], a[92:95], v[212:215]\n"
    "s_min_u32 s31, s31, 6144\n"
    "v_mfma_f32_16x16x32_bf16 v[216:219], v[96:99], a[44:47], v[216:219]\n"
    "s_bfe_u32 s22, s28, 65538\n"
    "ds_read_b64_tr_b16 v[12:13], v245 offset:12288\n"
    "ds_read_b64_tr_b16 v[14:15], v245 offset:14336\n"
    "v_mfma_f32_16x16x32_bf16 v[216:219], v[100:103], a[108:111], v[216:219]\n"
    "s_lshl_b32 s22, s22, 30\n"
    "v_mfma_f32_16x16x32_bf16 v[220:223], v[96:99], a[60:63], v[220:223]\n"
    "s_add_u32 s22, s22, s31\n"
    "ds_read_b64_tr_b16 v[16:17], v245 offset:16384\n"
    "ds_read_b64_tr_b16 v[18:19], v245 offset:18432\n"
    "v_mfma_f32_16x16x32_bf16 v[220:223], v[100:103], a[124:127], v[220:223]\n"
    "s_waitcnt lgkmcnt(10)\n"
    "v_mfma_f32_16x16x32_bf16 v[224:227], v[104:107], a[12:15], v[224:227]\n"
    "s_add_u32 s56, s22, 7168\n"
    "ds_read_b64_tr_b16 v[20:21], v245 offset:20480\n"
    "ds_read_b64_tr_b16 v[22:23], v245 offset:22528\n"
    "v_mfma_f32_16x16x32_bf16 v[224:227], v[108:111], a[76:79], v[224:227]\n"
    "s_add_u32 s23, s22, 458752\n"
    "v_mfma_f32_16x16x32_bf16 v[228:231], v[104:107], a[28:31], v[228:231]\n"
    "s_add_u32 s57, s56, 458752\n"
    "ds_read_b64_tr_b16 v[24:25], v245 offset:24576\n"
    "ds_read_b64_tr_b16 v[26:27], v245 offset:26624\n"
    "v_mfma_f32_16x16x32_bf16 v[228:231], v[108:111], a[92:95], v[228:231]\n"
    "buffer_load_dwordx4 a[136:139], v242, s[80:83], s22 offen offset:0 sc1\n"
    "v_mfma_f32_16x16x32_bf16 v[232:235], v[104:107], a[44:47], v[232:235]\n"
    "s_nop 0\n"
    "ds_read_b64_tr_b16 v[28:29], v245 offset:28672\n"
    "ds_read_b64_tr_b16 v[30:31], v245 offset:30720\n"
    "v_mfma_f32_16x16x32_bf16 v[232:235], v[108:111], a[108:111], v[232:235]\n"
    "buffer_load_dwordx4 a[152:155], v242, s[80:83], s23 offen offset:0 sc1\n"
    "v_mfma_f32_16x16x32_bf16 v[236:239], v[104:107], a[60:63], v[236:239]\n"
    "s_nop 0\n"
    "ds_read_b64_tr_b16 v[32:33], v246 offset:0\n"
    "ds_read_b64_tr_b16 v[34:35], v246 offset:2048\n"
    "v_mfma_f32_16x16x32_bf16 v[236:239], v[108:111], a[124:127], v[236:239]\n"
    "s_waitcnt lgkmcnt(14)\n"
    "buffer_load_dwordx4 a[168:171], v242, s[80:83], s56 offen offset:0 sc1\n"
    "v_mfma_f32_16x16x32_bf16 v[64:67], v[0:3], v[112:115], 0\n"
    "s_nop 0\n"
    "ds_read_b64_tr_b16 v[36:37], v246 offset:4096\n"
    "ds_read_b64_tr_b16 v[38:39], v246 offset:6144\n"
    "v_mfma_f32_16x16x32_bf16 v[64:67], v[4:7], v[116:119], v[64:67]\n"
    "s_waitcnt lgkmcnt(12)\n"
    "buffer_load_dwordx4 a[184:187], v242, s[80:83], s57 offen offset:0 sc1\n"
    "v_mfma_f32_16x16x32_bf16 v[64:67], v[8:11], v[120:123], v[64:67]\n"
    "s_add_u32 m0, s10, 32768\n"
    "ds_read_b64_tr_b16 v[40:41], v246 offset:8192\n"
    "ds_read_b64_tr_b16 v[42:43], v246 offset:10240\n"
    "v_mfma_f32_16x16x32_bf16 v[64:67], v[12:15], v[124:127], v[64:67]\n"
    "s_waitcnt lgkmcnt(10)\n"
    "buffer_load_dwordx4 v252, s[76:79], s11 offen offset:0 lds\n"
    "v_mfma_f32_16x16x32_bf16 v[64:67], v[16:19], v[128:131], v[64:67]\n"
    "s_nop 0\n"
    "ds_read_b64_tr_b16 v[44:45], v246 offset:12288\n"
    "ds_read_b64_tr_b16 v[46:47], v246 offset:14336\n"
    "v_mfma_f32_16x16x32_bf16 v[64:67], v[20:23], v[132:135], v[64:67]\n"
    "s_waitcnt lgkmcnt(8)\n"
    "buffer_load_dwordx4 v252, s[76:79], s12 offen offset:1024 lds\n"
    "v_mfma_f32_16x16x32_bf16 v[64:67], v[24:27], v[136:139], v[64:67]\n"
    "s_nop 0\n"
    "ds_read_b64_tr_b16 v[48:49], v246 offset:16384\n"
    "ds_read_b64_tr_b16 v[50:51], v246 offset:18432\n"
    "v_mfma_f32_16x16x32_bf16 v[64:67], v[28:31], v[140:143], v[64:67]\n"
    "buffer_load_dwordx4 v252, s[76:79], s13 offen offset:2048 lds\n"
    "v_mfma_f32_16x16x32_bf16 v[68:71], v[0:3], v[144:147], 0\n"
    "s_nop 0\n"
    "ds_read_b64_tr_b16 v[52:53], v246 offset:20480\n"
    "ds_read_b64_tr_b16 v[54:55], v246 offset:22528\n"
    "v_mfma_f32_16x16x32_bf16 v[68:71], v[4:7], v[148:151], v[68:71]\n"
    "v_mfma_f32_16x16x32_bf16 v[68:71], v[8:11], v[152:155], v[68:71]\n"
    "s_nop 0\n"
    "ds_read_b64_tr_b16 v[56:57], v246 offset:24576\n"
    "ds_read_b64_tr_b16 v[58:59], v246 offset:26624\n"
    "v_mfma_f32_16x16x32_bf16 v[68:71], v[12:15], v[156:159], v[68:71]\n"
    "buffer_load_dwordx4 v252, s[76:79], s16 offen offset:3072 lds\n"
    "v_mfma_f32_16x16x32_bf16 v[68:71], v[16:19], v[160:163], v[68:71]\n"
    "s_nop 0\n"
    "ds_read_b64_tr_b16 v[60:61], v246 offset:28672\n"
    "ds_read_b64_tr_b16 v[62:63], v246 offset:30720\n"
    "v_mfma_f32_16x16x32_bf16 v[68:71], v[20:23], v[164:167], v[68:71]\n"
    "v_add_u32 v252, 28672, v252\n"
    "v_mfma_f32_16x16x32_bf16 v[68:71], v[24:27], v[168:171], v[68:71]\n"
    "s_add_u32 m0, s10, 36864\n"
    "ds_read_b64_tr_b16 v[0:1], v245 offset:512\n"
    "ds_read_b64_tr_b16 v[2:3], v245 offset:2560\n"
    "v_mfma_f32_16x16x32_bf16 v[68:71], v[28:31], v[172:175], v[68:71]\n"
    "s_waitcnt lgkmcnt(14)\n"
    "buffer_load_dwordx4 v252, s[76:79], s11 offen offset:0 lds\n"
    "v_mfma_f32_16x16x32_bf16 v[72:75], v[32:35], v[112:115], 0\n"
    "s_nop 0\n"
    "ds_read_b64_tr_b16 v[4:5], v245 offset:4608\n"
    "ds_read_b64_tr_b16 v[6:7], v245 offset:6656\n"
    "v_mfma_f32_16x16x32_bf16 v[72:75], v[36:39], v[116:119], v[72:75]\n"
    "s_waitcnt lgkmcnt(12)\n"
    "v_cvt_pk_bf16_f32 v80, v64, v65\n"
    "v_cvt_pk_bf16_f32 v81, v66, v67\n"
    "v_mfma_f32_16x16x32_bf16 v[72:75], v[40:43], v[120:123], v[72:75]\n"
    "s_nop 0\n"
    "ds_read_b64_tr_b16 v[8:9], v245 offset:8704\n"
    "ds_read_b64_tr_b16 v[10:11], v245 offset:10752\n"
    "v_mfma_f32_16x16x32_bf16 v[72:75], v[44:47], v[124:127], v[72:75]\n"
    "s_waitcnt lgkmcnt(10)\n"
    "buffer_load_dwordx4 v252, s[76:79], s12 offen offset:1024 lds\n"
    "v_mfma_f32_16x16x32_bf16 v[72:75], v[48:51], v[128:131], v[72:75]\n"
    "s_nop 0\n"
    "ds_read_b64_tr_b16 v[12:13], v245 offset:12800\n"
    "ds_read_b64_tr_b16 v[14:15], v245 offset:14848\n"
    "v_mfma_f32_16x16x32_bf16 v[72:75], v[52:55], v[132:135], v[72:75]\n"
    "s_waitcnt lgkmcnt(8)\n"
    "v_mfma_f32_16x16x32_bf16 v[72:75], v[56:59], v[136:139], v[72:75]\n"
    "s_add_u32 s96, s54, 0\n"
    "ds_read_b64_tr_b16 v[16:17], v245 offset:16896\n"
    "ds_read_b64_tr_b16 v[18:19], v245 offset:18944\n"
    "v_mfma_f32_16x16x32_bf16 v[72:75], v[60:63], v[140:143], v[72:75]\n"
    "buffer_load_dwordx4 v252, s[76:79], s13 offen offset:2048 lds\n"
    "v_mfma_f32_16x16x32_bf16 v[76:79], v[32:35], v[144:147], 0\n"
    "s_nop 0\n"
    "ds_read_b64_tr_b16 v[20:21], v245 offset:20992\n"
    "ds_read_b64_tr_b16 v[22:23], v245 offset:23040\n"
    "v_mfma_f32_16x16x32_bf16 v[76:79], v[36:39], v[148:151], v[76:79]\n"
    "v_cvt_pk_bf16_f32 v82, v68, v69\n"
    "v_cvt_pk_bf16_f32 v83, v70, v71\n"
    "v_mfma_f32_16x16x32_bf16 v[76:79], v[40:43], v[152:155], v[76:79]\n"
    "s_nop 0\n"
    "ds_read_b64_tr_b16 v[24:25], v245 offset:25088\n"
    "ds_read_b64_tr_b16 v[26:27], v245 offset:27136\n"
    "v_mfma_f32_16x16x32_bf16 v[76:79], v[44:47], v[156:159], v[76:79]\n"
    "buffer_store_dwordx4 v[80:83], v242, s[88:91], s96 offen offset:0 sc1\n"
    "v_mfma_f32_16x16x32_bf16 v[76:79], v[48:51], v[160:163], v[76:79]\n"
    "s_nop 0\n"
    "ds_read_b64_tr_b16 v[28:29], v245 offset:29184\n"
    "ds_read_b64_tr_b16 v[30:31], v245 offset:31232\n"
    "v_mfma_f32_16x16x32_bf16 v[76:79], v[52:55], v[164:167], v[76:79]\n"
    "buffer_load_dwordx4 v252, s[76:79], s16 offen offset:3072 lds\n"
    "v_mfma_f32_16x16x32_bf16 v[76:79], v[56:59], v[168:171], v[76:79]\n"
    "s_nop 0\n"
    "ds_read_b64_tr_b16 v[32:33], v246 offset:512\n"
    "ds_read_b64_tr_b16 v[34:35], v246 offset:2560\n"
    "v_mfma_f32_16x16x32_bf16 v[76:79], v[60:63], v[172:175], v[76:79]\n"
    "s_waitcnt lgkmcnt(14)\n"
    "v_add_u32 v252, 28672, v252\n"
    "v_mfma_f32_16x16x32_bf16 v[64:67], v[0:3], v[112:115], 0\n"
    "s_lshr_b32 s19, s53, 8\n"
    "ds_read_b64_tr_b16 v[36:37], v246 offset:4608\n"
    "ds_read_b64_tr_b16 v[38:39], v246 offset:6656\n"
    "v_mfma_f32_16x16x32_bf16 v[64:67], v[4:7], v[116:119], v[64:67]\n"
    "s_waitcnt lgkmcnt(12)\n"
    "v_cvt_pk_bf16_f32 v84, v72, v73\n"
    "v_cvt_pk_bf16_f32 v85, v74, v75\n"
    "v_mfma_f32_16x16x32_bf16 v[64:67], v[8:11], v[120:123], v[64:67]\n"
    "s_nop 0\n"
    "ds_read_b64_tr_b16 v[40:41], v246 offset:8704\n"
    "ds_read_b64_tr_b16 v[42:43], v246 offset:10752\n"
    "v_mfma_f32_16x16x32_bf16 v[64:67], v[12:15], v[124:127], v[64:67]\n"
    "s_waitcnt lgkmcnt(10)\n"
    "v_mfma_f32_16x16x32_bf16 v[64:67], v[16:19], v[128:131], v[64:67]\n"
    "s_nop 0\n"
    "ds_read_b64_tr_b16 v[44:45], v246 offset:12800\n"
    "ds_read_b64_tr_b16 v[46:47], v246 offset:14848\n"
    "v_mfma_f32_16x16x32_bf16 v[64:67], v[20:23], v[132:135], v[64:67]\n"
    "s_waitcnt lgkmcnt(8)\n"
    "v_mfma_f32_16x16x32_bf16 v[64:67], v[24:27], v[136:139], v[64:67]\n"
    "s_add_u32 s97, s54, 16384\n"
    "ds_read_b64_tr_b16 v[48:49], v246 offset:16896\n"
    "ds_read_b64_tr_b16 v[50:51], v246 offset:18944\n"
    "v_mfma_f32_16x16x32_bf16 v[64:67], v[28:31], v[140:143], v[64:67]\n"
    "v_mfma_f32_16x16x32_bf16 v[68:71], v[0:3], v[144:147], 0\n"
    "s_nop 0\n"
    "ds_read_b64_tr_b16 v[52:53], v246 offset:20992\n"
    "ds_read_b64_tr_b16 v[54:55], v246 offset:23040\n"
    "v_mfma_f32_16x16x32_bf16 v[68:71], v[4:7], v[148:151], v[68:71]\n"
    "v_cvt_pk_bf16_f32 v86, v76, v77\n"
    "v_cvt_pk_bf16_f32 v87, v78, v79\n"
    "v_mfma_f32_16x16x32_bf16 v[68:71], v[8:11], v[152:155], v[68:71]\n"
    "s_nop 0\n"
    "ds_read_b64_tr_b16 v[56:57], v246 offset:25088\n"
    "ds_read_b64_tr_b16 v[58:59], v246 offset:27136\n"
    "v_mfma_f32_16x16x32_bf16 v[68:71], v[12:15], v[156:159], v[68:71]\n"
    "buffer_store_dwordx4 v[84:87], v242, s[88:91], s97 offen offset:0 sc1\n"
    "v_mfma_f32_16x16x32_bf16 v[68:71], v[16:19], v[160:163], v[68:71]\n"
    "s_nop 0\n"
    "ds_read_b64_tr_b16 v[60:61], v246 offset:29184\n"
    "ds_read_b64_tr_b16 v[62:63], v246 offset:31232\n"
    "v_mfma_f32_16x16x32_bf16 v[68:71], v[20:23], v[164:167], v[68:71]\n"
    "v_mfma_f32_16x16x32_bf16 v[68:71], v[24:27], v[168:171], v[68:71]\n"
    "v_mfma_f32_16x16x32_bf16 v[68:71], v[28:31], v[172:175], v[68:71]\n"
    "s_waitcnt lgkmcnt(12)\n"
    "v_mfma_f32_16x16x32_bf16 v[72:75], v[32:35], v[112:115], 0\n"
    "v_cvt_pk_bf16_f32 v80, v64, v65\n"
    "v_cvt_pk_bf16_f32 v81, v66, v67\n"
    "v_mfma_f32_16x16x32_bf16 v[72:75], v[36:39], v[116:119], v[72:75]\n"
    "s_waitcnt lgkmcnt(8)\n"
    "v_mfma_f32_16x16x32_bf16 v[72:75], v[40:43], v[120:123], v[72:75]\n"
    "s_nop 0\n"
    "v_mfma_f32_16x16x32_bf16 v[72:75], v[44:47], v[124:127], v[72:75]\n"
    "s_waitcnt lgkmcnt(4)\n"
    "ds_read_b64_tr_b16 v[88:89], v244 offset:0\n"
    "ds_read_b64_tr_b16 v[90:91], v244 offset:2048\n"
    "v_mfma_f32_16x16x32_bf16 v[72:75], v[48:51], v[128:131], v[72:75]\n"
    "s_nop 0\n"
    "ds_read_b64_tr_b16 v[92:93], v244 offset:4096\n"
    "ds_read_b64_tr_b16 v[94:95], v244 offset:6144\n"
    "v_mfma_f32_16x16x32_bf16 v[72:75], v[52:55], v[132:135], v[72:75]\n"
    "s_waitcnt lgkmcnt(4)\n"
    "v_mfma_f32_16x16x32_bf16 v[72:75], v[56:59], v[136:139], v[72:75]\n"
    "s_add_u32 s96, s54, 32768\n"
    "v_mfma_f32_16x16x32_bf16 v[72:75], v[60:63], v[140:143], v[72:75]\n"
    "s_waitcnt vmcnt(14)\n"
    "s_barrier\n"
    "v_mfma_f32_16x16x32_bf16 v[76:79], v[32:35], v[144:147], 0\n"
    "v_cmp_eq_u64 vcc, s[70:71], v[254:255]\n"
    "v_cmp_eq_u32 s[58:59], 4, v243\n"
    "s_andn2_b64 vcc, s[58:59], vcc\n"
    "s_cselect_b32 s58, 8, 0\n"
    "s_or_b32 s28, s28, s58\n"
    "v_cvt_pk_bf16_f32 v82, v68, v69\n"
    "v_cvt_pk_bf16_f32 v83, v70, v71\n"
    "v_mfma_f32_16x16x32_bf16 v[76:79], v[36:39], v[148:151], v[76:79]\n"
    "buffer_store_dwordx2 v[248:249], v247, s[36:39], s19 offen offset:0 sc1\n"
    "v_mfma_f32_16x16x32_bf16 v[76:79], v[40:43], v[152:155], v[76:79]\n"
    "buffer_store_dwordx4 v[80:83], v242, s[88:91], s96 offen offset:0 sc1\n"
    "v_mfma_f32_16x16x32_bf16 v[76:79], v[44:47], v[156:159], v[76:79]\n"
    "s_nop 0\n"
    "ds_read_b64_tr_b16 v[96:97], v251 offset:0\n"
    "ds_read_b64_tr_b16 v[98:99], v251 offset:2048\n"
    "v_mfma_f32_16x16x32_bf16 v[76:79], v[48:51], v[160:163], v[76:79]\n"
    "s_nop 0\n"
    "ds_read_b64_tr_b16 v[100:101], v251 offset:4096\n"
    "ds_read_b64_tr_b16 v[102:103], v251 offset:6144\n"
    "v_mfma_f32_16x16x32_bf16 v[76:79], v[52:55], v[164:167], v[76:79]\n"
    "global_load_dwordx2 v[254:255], v250, s[20:21] sc1\n"
    "v_mfma_f32_16x16x32_bf16 v[76:79], v[56:59], v[168:171], v[76:79]\n"
    "s_sub_u32 s31, s31, 1024\n"
    "v_mfma_f32_16x16x32_bf16 v[76:79], v[60:63], v[172:175], v[76:79]\n"
    "s_waitcnt lgkmcnt(4)\n"
    "v_mfma_f32_16x16x32_bf16 v[176:179], v[88:91], a[140:143], v[176:179]\n"
    "s_min_u32 s31, s31, 6144\n"
    "v_cvt_pk_bf16_f32 v84, v72, v73\n"
    "v_cvt_pk_bf16_f32 v85, v74, v75\n"
    "v_mfma_f32_16x16x32_bf16 v[176:179], v[92:95], a[204:207], v[176:179]\n"
    "s_bfe_u32 s22, s28, 65539\n"
    "v_mfma_f32_16x16x32_bf16 v[180:183], v[88:91], a[156:159], v[180:183]\n"
    "s_lshl_b32 s22, s22, 30\n"
    "v_mfma_f32_16x16x32_bf16 v[180:183], v[92:95], a[220:223], v[180:183]\n"
    "s_add_u32 s22, s22, s31\n"
    "ds_read_b64_tr_b16 v[104:105], v244 offset:512\n"
    "ds_read_b64_tr_b16 v[106:107], v244 offset:2560\n"
    "v_mfma_f32_16x16x32_bf16 v[184:187], v[88:91], a[172:175], v[184:187]\n"
    "s_add_u32 s56, s22, 7168\n"
    "ds_read_b64_tr_b16 v[108:109], v244 offset:4608\n"
    "ds_read_b64_tr_b16 v[110:111], v244 offset:6656\n"
    "v_mfma_f32_16x16x32_bf16 v[184:187], v[92:95], a[236:239], v[184:187]\n"
    "s_add_u32 s23, s22, 458752\n"
    "v_mfma_f32_16x16x32_bf16 v[188:191], v[88:91], a[188:191], v[188:191]\n"
    "s_add_u32 s97, s54, 49152\n"
    "v_mfma_f32_16x16x32_bf16 v[188:191], v[92:95], a[252:255], v[188:191]\n"
    "s_waitcnt lgkmcnt(4)\n"
    "v_mfma_f32_16x16x32_bf16 v[192:195], v[96:99], a[140:143], v[192:195]\n"
    "s_add_u32 s57, s56, 458752\n"
    "v_cvt_pk_bf16_f32 v86, v76, v77\n"
    "v_cvt_pk_bf16_f32 v87, v78, v79\n"
    "v_mfma_f32_16x16x32_bf16 v[192:195], v[100:103], a[204:207], v[192:195]\n"
    "buffer_load_dwordx4 a[200:203], v242, s[80:83], s22 offen offset:0 sc1\n"
    "v_mfma_f32_16x16x32_bf16 v[196:199], v[96:99], a[156:159], v[196:199]\n"
    "buffer_store_dwordx4 v[84:87], v242, s[88:91], s97 offen offset:0 sc1\n"
    "v_mfma_f32_16x16x32_bf16 v[196:199], v[100:103], a[220:223], v[196:199]\n"
    "s_nop 0\n"
    "ds_read_b64_tr_b16 v[88:89], v251 offset:512\n"
    "ds_read_b64_tr_b16 v[90:91], v251 offset:2560\n"
    "v_mfma_f32_16x16x32_bf16 v[200:203], v[96:99], a[172:175], v[200:203]\n"
    "s_nop 0\n"
    "ds_read_b64_tr_b16 v[92:93], v251 offset:4608\n"
    "ds_read_b64_tr_b16 v[94:95], v251 offset:6656\n"
    "v_mfma_f32_16x16x32_bf16 v[200:203], v[100:103], a[236:239], v[200:203]\n"
    "buffer_load_dwordx4 a[216:219], v242, s[80:83], s23 offen offset:0 sc1\n"
    "v_mfma_f32_16x16x32_bf16 v[204:207], v[96:99], a[188:191], v[204:207]\n"
    "buffer_load_dwordx4 a[232:235], v242, s[80:83], s56 offen offset:0 sc1\n"
    "v_mfma_f32_16x16x32_bf16 v[204:207], v[100:103], a[252:255], v[204:207]\n"
    "s_waitcnt lgkmcnt(4)\n"
    "buffer_load_dwordx4 a[248:251], v242, s[80:83], s57 offen offset:0 sc1\n"
    "v_mfma_f32_16x16x32_bf16 v[208:211], v[104:107], a[140:143], v[208:211]\n"
    "s_mul_i32 s52, s30, 24576\n"
    "v_mfma_f32_16x16x32_bf16 v[208:211], v[108:111], a[204:207], v[208:211]\n"
    "s_add_u32 m0, s52, 0\n"
    "v_mfma_f32_16x16x32_bf16 v[212:215], v[104:107], a[156:159], v[212:215]\n"
    "s_waitcnt vmcnt(9)\n"
    "buffer_load_dwordx4 v252, s[76:79], s11 offen offset:0 lds\n"
    "v_mfma_f32_16x16x32_bf16 v[212:215], v[108:111], a[220:223], v[212:215]\n"
    "s_nop 0\n"
    "ds_read_b64_tr_b16 v[96:97], v244 offset:32768\n"
    "ds_read_b64_tr_b16 v[98:99], v244 offset:34816\n"
    "v_mfma_f32_16x16x32_bf16 v[216:219], v[104:107], a[172:175], v[216:219]\n"
    "s_nop 0\n"
    "ds_read_b64_tr_b16 v[100:101], v244 offset:36864\n"
    "ds_read_b64_tr_b16 v[102:103], v244 offset:38912\n"
    "v_mfma_f32_16x16x32_bf16 v[216:219], v[108:111], a[236:239], v[216:219]\n"
    "buffer_load_dwordx4 v252, s[76:79], s12 offen offset:1024 lds\n"
    "v_mfma_f32_16x16x32_bf16 v[220:223], v[104:107], a[188:191], v[220:223]\n"
    "buffer_load_dwordx4 v252, s[76:79], s13 offen offset:2048 lds\n"
    "v_mfma_f32_16x16x32_bf16 v[220:223], v[108:111], a[252:255], v[220:223]\n"
    "s_waitcnt lgkmcnt(4)\n"
    "buffer_load_dwordx4 v252, s[76:79], s16 offen offset:3072 lds\n"
    "v_mfma_f32_16x16x32_bf16 v[224:227], v[88:91], a[140:143], v[224:227]\n"
    "v_add_u32 v252, 28672, v252\n"
    "v_add_u32 v240, s52, v245\n"
    "v_mfma_f32_16x16x32_bf16 v[224:227], v[92:95], a[204:207], v[224:227]\n"
    "v_add_u32 v241, s52, v246\n"
    "v_mfma_f32_16x16x32_bf16 v[228:231], v[88:91], a[156:159], v[228:231]\n"
    "s_waitcnt vmcnt(7)\n"
    "s_barrier\n"
    "v_mfma_f32_16x16x32_bf16 v[228:231], v[92:95], a[220:223], v[228:231]\n"
    "v_cmp_eq_u64 vcc, s[70:71], v[254:255]\n"
    "v_cmp_eq_u32 s[58:59], 5, v243\n"
    "s_andn2_b64 vcc, s[58:59], vcc\n"
    "s_cselect_b32 s58, 16, 0\n"
    "s_or_b32 s28, s28, s58\n"
    "ds_read_b64_tr_b16 v[104:105], v251 offset:32768\n"
    "ds_read_b64_tr_b16 v[106:107], v251 offset:34816\n"
    "v_mfma_f32_16x16x32_bf16 v[232:235], v[88:91], a[172:175], v[232:235]\n"
    "s_add_u32 m0, s52, 4096\n"
    "ds_read_b64_tr_b16 v[108:109], v251 offset:36864\n"
    "ds_read_b64_tr_b16 v[110:111], v251 offset:38912\n"
    "v_mfma_f32_16x16x32_bf16 v[232:235], v[92:95], a[236:239], v[232:235]\n"
    "buffer_load_dwordx4 v252, s[76:79], s11 offen offset:0 lds\n"
    "v_mfma_f32_16x16x32_bf16 v[236:239], v[88:91], a[188:191], v[236:239]\n"
    "v_mbcnt_lo_u32_b32 v136, -1, 0\n"
    "v_mbcnt_hi_u32_b32 v136, -1, v136\n"
    "v_mfma_f32_16x16x32_bf16 v[236:239], v[92:95], a[252:255], v[236:239]\n"
    "s_waitcnt lgkmcnt(4)\n"
    "v_lshrrev_b32 v137, 4, v136\n"
    "v_bfe_u32 v116, v136, 2, 2\n"
    "v_mfma_f32_16x16x32_bf16 v[176:179], v[96:99], a[0:3], v[176:179]\n"
    "buffer_load_dwordx4 v252, s[76:79], s12 offen offset:1024 lds\n"
    "v_mfma_f32_16x16x32_bf16 v[176:179], v[100:103], a[32:35], v[176:179]\n"
    "v_cmp_eq_u32 vcc, v137, v116\n"
    "v_and_b32 v137, 1, v136\n"
    "v_mfma_f32_16x16x32_bf16 v[180:183], v[96:99], a[4:7], v[180:183]\n"
    "v_lshlrev_b32 v137, 4, v137\n"
    "v_mov_b32 v114, 0x3f80\n"
    "v_mfma_f32_16x16x32_bf16 v[180:183], v[100:103], a[36:39], v[180:183]\n"
    "s_nop 0\n"
    "ds_read_b64_tr_b16 v[88:89], v244 offset:33280\n"
    "ds_read_b64_tr_b16 v[90:91], v244 offset:35328\n"
    "v_mfma_f32_16x16x32_bf16 v[184:187], v[96:99], a[16:19], v[184:187]\n"
    "s_nop 0\n"
    "ds_read_b64_tr_b16 v[92:93], v244 offset:37376\n"
    "ds_read_b64_tr_b16 v[94:95], v244 offset:39424\n"
    "v_mfma_f32_16x16x32_bf16 v[184:187], v[100:103], a[48:51], v[184:187]\n"
    "buffer_load_dwordx4 v252, s[76:79], s13 offen offset:2048 lds\n"
    "v_mfma_f32_16x16x32_bf16 v[188:191], v[96:99], a[20:23], v[188:191]\n"
    "v_lshlrev_b32 v137, v137, v114\n"
    "v_cndmask_b32 v137, 0, v137, vcc\n"
    "v_mfma_f32_16x16x32_bf16 v[188:191], v[100:103], a[52:55], v[188:191]\n"
    "s_waitcnt lgkmcnt(4)\n"
    "v_and_b32 v136, 2, v136\n"
    "v_cmp_eq_u32 vcc, 0, v136\n"
    "v_mfma_f32_16x16x32_bf16 v[192:195], v[104:107], a[0:3], v[192:195]\n"
    "buffer_load_dwordx4 v252, s[76:79], s16 offen offset:3072 lds\n"
    "v_mfma_f32_16x16x32_bf16 v[192:195], v[108:111], a[32:35], v[192:195]\n"
    "v_add_u32 v252, 28672, v252\n"
    "v_mov_b32 v114, 0\n"
    "v_mfma_f32_16x16x32_bf16 v[196:199], v[104:107], a[4:7], v[196:199]\n"
    "s_add_u32 m0, s52, 8192\n"
    "v_mov_b32 v115, 0\n"
    "v_cndmask_b32 v112, 0, v137, vcc\n"
    "v_mfma_f32_16x16x32_bf16 v[196:199], v[108:111], a[36:39], v[196:199]\n"
    "s_nop 0\n"
    "ds_read_b64_tr_b16 v[96:97], v251 offset:33280\n"
    "ds_read_b64_tr_b16 v[98:99], v251 offset:35328\n"
    "v_mfma_f32_16x16x32_bf16 v[200:203], v[104:107], a[16:19], v[200:203]\n"
    "s_nop 0\n"
    "ds_read_b64_tr_b16 v[100:101], v251 offset:37376\n"
    "ds_read_b64_tr_b16 v[102:103], v251 offset:39424\n"
    "v_mfma_f32_16x16x32_bf16 v[200:203], v[108:111], a[48:51], v[200:203]\n"
    "buffer_load_dwordx4 v252, s[76:79], s11 offen offset:0 lds\n"
    "v_mfma_f32_16x16x32_bf16 v[204:207], v[104:107], a[20:23], v[204:207]\n"
    "v_cndmask_b32 v113, v137, 0, vcc\n"
    "v_mov_b32 v116, 0\n"
    "v_mfma_f32_16x16x32_bf16 v[204:207], v[108:111], a[52:55], v[204:207]\n"
    "s_waitcnt lgkmcnt(4)\n"
    "v_mov_b32 v117, 0\n"
    "v_mov_b32 v118, v112\n"
    "v_mfma_f32_16x16x32_bf16 v[208:211], v[88:91], a[0:3], v[208:211]\n"
    "buffer_load_dwordx4 v252, s[76:79], s12 offen offset:1024 lds\n"
    "v_mfma_f32_16x16x32_bf16 v[208:211], v[92:95], a[32:35], v[208:211]\n"
    "v_mov_b32 v119, v113\n"
    "v_mfma_f32_16x16x32_bf16 v[212:215], v[88:91], a[4:7], v[212:215]\n"
    "s_waitcnt vmcnt(2)\n"
    "buffer_load_dwordx4 v252, s[76:79], s13 offen offset:2048 lds\n"
    "v_mfma_f32_16x16x32_bf16 v[212:215], v[92:95], a[36:39], v[212:215]\n"
    "s_nop 0\n"
    "ds_read_b64_tr_b16 v[104:105], v240 offset:0\n"
    "ds_read_b64_tr_b16 v[106:107], v240 offset:2048\n"
    "v_mfma_f32_16x16x32_bf16 v[216:219], v[88:91], a[16:19], v[216:219]\n"
    "s_nop 0\n"
    "ds_read_b64_tr_b16 v[108:109], v240 offset:4096\n"
    "ds_read_b64_tr_b16 v[110:111], v240 offset:6144\n"
    "v_mfma_f32_16x16x32_bf16 v[216:219], v[92:95], a[48:51], v[216:219]\n"
    "v_mfma_f32_16x16x32_bf16 v[220:223], v[88:91], a[20:23], v[220:223]\n"
    "buffer_load_dwordx4 v252, s[76:79], s16 offen offset:3072 lds\n"
    "v_mfma_f32_16x16x32_bf16 v[220:223], v[92:95], a[52:55], v[220:223]\n"
    "s_waitcnt lgkmcnt(4)\n"
    "v_add_u32 v252, 28672, v252\n"
    "v_mfma_f32_16x16x32_bf16 v[224:227], v[96:99], a[0:3], v[224:227]\n"
    "s_add_u32 m0, s52, 12288\n"
    "v_mfma_f32_16x16x32_bf16 v[224:227], v[100:103], a[32:35], v[224:227]\n"
    "v_mfma_f32_16x16x32_bf16 v[228:231], v[96:99], a[4:7], v[228:231]\n"
    "buffer_load_dwordx4 v252, s[76:79], s11 offen offset:0 lds\n"
    "v_mfma_f32_16x16x32_bf16 v[228:231], v[100:103], a[36:39], v[228:231]\n"
    "s_nop 0\n"
    "ds_read_b64_tr_b16 v[88:89], v241 offset:0\n"
    "ds_read_b64_tr_b16 v[90:91], v241 offset:2048\n"
    "v_mfma_f32_16x16x32_bf16 v[232:235], v[96:99], a[16:19], v[232:235]\n"
    "s_nop 0\n"
    "ds_read_b64_tr_b16 v[92:93], v241 offset:4096\n"
    "ds_read_b64_tr_b16 v[94:95], v241 offset:6144\n"
    "v_mfma_f32_16x16x32_bf16 v[232:235], v[100:103], a[48:51], v[232:235]\n"
    "v_mfma_f32_16x16x32_bf16 v[236:239], v[96:99], a[20:23], v[236:239]\n"
    "buffer_load_dwordx4 v252, s[76:79], s12 offen offset:1024 lds\n"
    "v_mfma_f32_16x16x32_bf16 v[236:239], v[100:103], a[52:55], v[236:239]\n"
    "s_waitcnt lgkmcnt(4)\n"
    "v_mfma_f32_16x16x32_bf16 v[176:179], v[104:107], a[64:67], v[176:179]\n"
    "v_mfma_f32_16x16x32_bf16 v[176:179], v[108:111], a[96:99], v[176:179]\n"
    "v_mfma_f32_16x16x32_bf16 v[180:183], v[104:107], a[68:71], v[180:183]\n"
    "buffer_load_dwordx4 v252, s[76:79], s13 offen offset:2048 lds\n"
    "v_mfma_f32_16x16x32_bf16 v[180:183], v[108:111], a[100:103], v[180:183]\n"
    "s_nop 0\n"
    "ds_read_b64_tr_b16 v[96:97], v240 offset:512\n"
    "ds_read_b64_tr_b16 v[98:99], v240 offset:2560\n"
    "v_mfma_f32_16x16x32_bf16 v[184:187], v[104:107], a[80:83], v[184:187]\n"
    "s_nop 0\n"
    "ds_read_b64_tr_b16 v[100:101], v240 offset:4608\n"
    "ds_read_b64_tr_b16 v[102:103], v240 offset:6656\n"
    "v_mfma_f32_16x16x32_bf16 v[184:187], v[108:111], a[112:115], v[184:187]\n"
    "v_mfma_f32_16x16x32_bf16 v[188:191], v[104:107], a[84:87], v[188:191]\n"
    "buffer_load_dwordx4 v252, s[76:79], s16 offen offset:3072 lds\n"
    "v_mfma_f32_16x16x32_bf16 v[188:191], v[108:111], a[116:119], v[188:191]\n"
    "s_waitcnt lgkmcnt(4)\n"
    "v_add_u32 v252, 28672, v252\n"
    "v_mfma_f32_16x16x32_bf16 v[192:195], v[88:91], a[64:67], v[192:195]\n"
    "s_lshr_b32 s18, s54, 8\n"
    "v_mfma_f32_16x16x32_bf16 v[192:195], v[92:95], a[96:99], v[192:195]\n"
    "v_mfma_f32_16x16x32_bf16 v[196:199], v[88:91], a[68:71], v[196:199]\n"
    "buffer_store_dwordx2 v[248:249], v247, s[36:39], s18 offen offset:0 sc1\n"
    "v_mfma_f32_16x16x32_bf16 v[196:199], v[92:95], a[100:103], v[196:199]\n"
    "s_sub_u32 s31, s31, 1024\n"
    "ds_read_b64_tr_b16 v[104:105], v241 offset:512\n"
    "ds_read_b64_tr_b16 v[106:107], v241 offset:2560\n"
    "v_mfma_f32_16x16x32_bf16 v[200:203], v[88:91], a[80:83], v[200:203]\n"
    "s_min_u32 s31, s31, 6144\n"
    "ds_read_b64_tr_b16 v[108:109], v241 offset:4608\n"
    "ds_read_b64_tr_b16 v[110:111], v241 offset:6656\n"
    "v_mfma_f32_16x16x32_bf16 v[200:203], v[92:95], a[112:115], v[200:203]\n"
    "s_bfe_u32 s22, s28, 65540\n"
    "v_mfma_f32_16x16x32_bf16 v[204:207], v[88:91], a[84:87], v[204:207]\n"
    "s_lshl_b32 s22, s22, 30\n"
    "v_mfma_f32_16x16x32_bf16 v[204:207], v[92:95], a[116:119], v[204:207]\n"
    "s_waitcnt lgkmcnt(4)\n"
    "v_mfma_f32_16x16x32_bf16 v[208:211], v[96:99], a[64:67], v[208:211]\n"
    "s_add_u32 s22, s22, s31\n"
    "v_mfma_f32_16x16x32_bf16 v[208:211], v[100:103], a[96:99], v[208:211]\n"
    "s_add_u32 s56, s22, 7168\n"
    "v_mfma_f32_16x16x32_bf16 v[212:215], v[96:99], a[68:71], v[212:215]\n"
    "s_waitcnt vmcnt(1)\n"
    "v_mfma_f32_16x16x32_bf16 v[212:215], v[100:103], a[100:103], v[212:215]\n"
    "s_add_u32 s23, s22, 458752\n"
    "ds_read_b64_tr_b16 v[88:89], v240 offset:8192\n"
    "ds_read_b64_tr_b16 v[90:91], v240 offset:10240\n"
    "v_mfma_f32_16x16x32_bf16 v[216:219], v[96:99], a[80:83], v[216:219]\n"
    "s_add_u32 s57, s56, 458752\n"
    "ds_read_b64_tr_b16 v[92:93], v240 offset:12288\n"
    "ds_read_b64_tr_b16 v[94:95], v240 offset:14336\n"
    "v_mfma_f32_16x16x32_bf16 v[216:219], v[100:103], a[112:115], v[216:219]\n"
    "buffer_load_dwordx4 a[12:15], v242, s[80:83], s22 offen offset:0 sc1\n"
    "v_mfma_f32_16x16x32_bf16 v[220:223], v[96:99], a[84:87], v[220:223]\n"
    "buffer_load_dwordx4 a[28:31], v242, s[80:83], s23 offen offset:0 sc1\n"
    "v_mfma_f32_16x16x32_bf16 v[220:223], v[100:103], a[116:119], v[220:223]\n"
    "s_waitcnt lgkmcnt(4)\n"
    "buffer_load_dwordx4 a[44:47], v242, s[80:83], s56 offen offset:0 sc1\n"
    "v_mfma_f32_16x16x32_bf16 v[224:227], v[104:107], a[64:67], v[224:227]\n"
    "buffer_load_dwordx4 a[60:63], v242, s[80:83], s57 offen offset:0 sc1\n"
    "v_mfma_f32_16x16x32_bf16 v[224:227], v[108:111], a[96:99], v[224:227]\n"
    "v_mbcnt_lo_u32_b32 v87, -1, 0\n"
    "v_mbcnt_hi_u32_b32 v87, -1, v87\n"
    "v_mfma_f32_16x16x32_bf16 v[228:231], v[104:107], a[68:71], v[228:231]\n"
    "v_mul_u32_u24 v87, 896, v87\n"
    "v_mfma_f32_16x16x32_bf16 v[228:231], v[108:111], a[100:103], v[228:231]\n"
    "s_nop 0\n"
    "ds_read_b64_tr_b16 v[96:97], v241 offset:8192\n"
    "ds_read_b64_tr_b16 v[98:99], v241 offset:10240\n"
    "v_mfma_f32_16x16x32_bf16 v[232:235], v[104:107], a[80:83], v[232:235]\n"
    "s_nop 0\n"
    "ds_read_b64_tr_b16 v[100:101], v241 offset:12288\n"
    "ds_read_b64_tr_b16 v[102:103], v241 offset:14336\n"
    "v_mfma_f32_16x16x32_bf16 v[232:235], v[108:111], a[112:115], v[232:235]\n"
    "global_load_dwordx2 v[254:255], v250, s[20:21] sc1\n"
    "v_mfma_f32_16x16x32_bf16 v[236:239], v[104:107], a[84:87], v[236:239]\n"
    "v_mfma_f32_16x16x32_bf16 v[236:239], v[108:111], a[116:119], v[236:239]\n"
    "s_waitcnt lgkmcnt(4)\n"
    "v_mfma_f32_16x16x32_bf16 v[176:179], v[88:91], a[128:131], v[176:179]\n"
    "v_mfma_f32_16x16x32_bf16 v[176:179], v[92:95], a[160:163], v[176:179]\n"
    "v_mfma_f32_16x16x32_bf16 v[180:183], v[88:91], a[132:135], v[180:183]\n"
    "v_mfma_f32_16x16x32_bf16 v[180:183], v[92:95], a[164:167], v[180:183]\n"
    "s_nop 0\n"
    "ds_read_b64_tr_b16 v[104:105], v240 offset:8704\n"
    "ds_read_b64_tr_b16 v[106:107], v240 offset:10752\n"
    "v_mfma_f32_16x16x32_bf16 v[184:187], v[88:91], a[144:147], v[184:187]\n"
    "s_nop 0\n"
    "ds_read_b64_tr_b16 v[108:109], v240 offset:12800\n"
    "ds_read_b64_tr_b16 v[110:111], v240 offset:14848\n"
    "v_mfma_f32_16x16x32_bf16 v[184:187], v[92:95], a[176:179], v[184:187]\n"
    "v_mfma_f32_16x16x32_bf16 v[188:191], v[88:91], a[148:151], v[188:191]\n"
    "v_mfma_f32_16x16x32_bf16 v[188:191], v[92:95], a[180:183], v[188:191]\n"
    "s_waitcnt lgkmcnt(4)\n"
    "v_mfma_f32_16x16x32_bf16 v[192:195], v[96:99], a[128:131], v[192:195]\n"
    "v_mfma_f32_16x16x32_bf16 v[192:195], v[100:103], a[160:163], v[192:195]\n"
    "v_mfma_f32_16x16x32_bf16 v[196:199], v[96:99], a[132:135], v[196:199]\n"
    "v_mfma_f32_16x16x32_bf16 v[196:199], v[100:103], a[164:167], v[196:199]\n"
    "s_nop 0\n"
    "ds_read_b64_tr_b16 v[88:89], v241 offset:8704\n"
    "ds_read_b64_tr_b16 v[90:91], v241 offset:10752\n"
    "v_mfma_f32_16x16x32_bf16 v[200:203], v[96:99], a[144:147], v[200:203]\n"
    "s_nop 0\n"
    "ds_read_b64_tr_b16 v[92:93], v241 offset:12800\n"
    "ds_read_b64_tr_b16 v[94:95], v241 offset:14848\n"
    "v_mfma_f32_16x16x32_bf16 v[200:203], v[100:103], a[176:179], v[200:203]\n"
    "v_mfma_f32_16x16x32_bf16 v[204:207], v[96:99], a[148:151], v[204:207]\n"
    "v_mfma_f32_16x16x32_bf16 v[204:207], v[100:103], a[180:183], v[204:207]\n"
    "s_waitcnt lgkmcnt(4)\n"
    "v_mfma_f32_16x16x32_bf16 v[208:211], v[104:107], a[128:131], v[208:211]\n"
    "v_mfma_f32_16x16x32_bf16 v[208:211], v[108:111], a[160:163], v[208:211]\n"
    "v_mfma_f32_16x16x32_bf16 v[212:215], v[104:107], a[132:135], v[212:215]\n"
    "v_mfma_f32_16x16x32_bf16 v[212:215], v[108:111], a[164:167], v[212:215]\n"
    "v_mfma_f32_16x16x32_bf16 v[216:219], v[104:107], a[144:147], v[216:219]\n"
    "v_mfma_f32_16x16x32_bf16 v[216:219], v[108:111], a[176:179], v[216:219]\n"
    "v_mfma_f32_16x16x32_bf16 v[220:223], v[104:107], a[148:151], v[220:223]\n"
    "v_mfma_f32_16x16x32_bf16 v[220:223], v[108:111], a[180:183], v[220:223]\n"
    "s_waitcnt lgkmcnt(0)\n"
    "v_mfma_f32_16x16x32_bf16 v[224:227], v[88:91], a[128:131], v[224:227]\n"
    "v_mfma_f32_16x16x32_bf16 v[224:227], v[92:95], a[160:163], v[224:227]\n"
    "v_mfma_f32_16x16x32_bf16 v[228:231], v[88:91], a[132:135], v[228:231]\n"
    "v_mfma_f32_16x16x32_bf16 v[228:231], v[92:95], a[164:167], v[228:231]\n"
    "v_mfma_f32_16x16x32_bf16 v[232:235], v[88:91], a[144:147], v[232:235]\n"
    "v_mfma_f32_16x16x32_bf16 v[232:235], v[92:95], a[176:179], v[232:235]\n"
    "v_mfma_f32_16x16x32_bf16 v[236:239], v[88:91], a[148:151], v[236:239]\n"
    "v_mfma_f32_16x16x32_bf16 v[236:239], v[92:95], a[180:183], v[236:239]\n"
    "s_nop 7\n"
    "s_cmp_lt_u32 %1, 224\n"
    "s_cbranch_scc1 FINGO\n"
    "s_cmp_eq_u32 %1, 255\n"
    "s_cbranch_scc0 FINEND\n"
    "v_mbcnt_lo_u32_b32 v255, -1, 0\n"
    "v_mbcnt_hi_u32_b32 v255, -1, v255\n"
    "s_lshl_b32 s57, s30, 2\n"
    "v_and_b32 v140, 63, v255\n"
    "v_lshlrev_b32 v140, 3, v140\n"
    "s_lshl_b32 s31, s57, 7\n"
    "s_add_u32 s31, s31, 4096\n"
    "v_add_u32 v140, s31, v140\n"
    "s_mov_b32 s31, 131072\n"
    "POLLG:\n"
    "global_load_dwordx2 v[142:143], v140, s[20:21] sc1\n"
    "s_waitcnt vmcnt(0)\n"
    "v_cmp_ne_u64 vcc, s[70:71], v[142:143]\n"
    "s_cbranch_vccz POLLGD\n"
    "s_sleep 4\n"
    "s_sub_u32 s31, s31, 1\n"
    "s_cbranch_scc0 POLLG\n"
    "POLLGD:\n"
    "s_barrier\n"
    "s_mov_b32 s57, 0\n"
    "s_cmp_lg_u32 s30, 0\n"
    "s_cbranch_scc1 GST\n"
    "v_mov_b32 v144, s70\n"
    "v_mov_b32 v145, s71\n"
    "v_mov_b32 v146, s57\n"
    "s_mov_b64 exec, 1\n"
    "global_store_dwordx2 v146, v[144:145], s[20:21] sc1\n"
    "s_mov_b64 exec, -1\n"
    "GST:\n"
    "s_branch FINEND\n"
    "FINGO:\n"
    "s_waitcnt vmcnt(0)\n"
    "v_cmp_eq_u64 s[18:19], s[70:71], v[254:255]\n"
    "s_mov_b32 s96, 131072\n"
    "s_cmp_lg_u64 s[18:19], -1\n"
    "s_cbranch_scc1 DYN\n"
    "s_cmp_lg_u32 s28, 0\n"
    "s_cbranch_scc1 DYN\n"
    "DYNR:\n"
    "s_mov_b32 s72, s24\n"
    "s_sub_u32 s31, s31, 1024\n"
    "s_min_u32 s31, s31, 6144\n"
    "s_mov_b32 s22, s31\n"
    "s_add_u32 s56, s22, 7168\n"
    "s_add_u32 s23, s22, 458752\n"
    "s_add_u32 s57, s56, 458752\n"
    "buffer_load_dwordx4 a[140:143], v242, s[80:83], s22 offen offset:0 sc1\n"
    "s_and_b32 s73, s25, 65535\n"
    "buffer_load_dwordx4 a[156:159], v242, s[80:83], s23 offen offset:0 sc1\n"
    "s_mov_b32 s74, 1835008\n"
    "buffer_load_dwordx4 a[172:175], v242, s[80:83], s56 offen offset:0 sc1\n"
    "s_mov_b32 s75, 131072\n"
    "buffer_load_dwordx4 a[188:191], v242, s[80:83], s57 offen offset:0 sc1\n"
    "v_add_u32 v240, s52, v242\n"
    "s_sub_u32 s31, s31, 1024\n"
    "s_min_u32 s31, s31, 6144\n"
    "s_mov_b32 s22, s31\n"
    "s_add_u32 s56, s22, 7168\n"
    "s_add_u32 s23, s22, 458752\n"
    "s_add_u32 s57, s56, 458752\n"
    "buffer_load_dwordx4 a[204:207], v242, s[80:83], s22 offen offset:0 sc1\n"
    "s_add_u32 s58, s30, 3\n"
    "buffer_load_dwordx4 a[220:223], v242, s[80:83], s23 offen offset:0 sc1\n"
    "s_and_b32 s58, s58, 3\n"
    "buffer_load_dwordx4 a[236:239], v242, s[80:83], s56 offen offset:0 sc1\n"
    "s_mul_i32 s58, s58, 24576\n"
    "buffer_load_dwordx4 a[252:255], v242, s[80:83], s57 offen offset:0 sc1\n"
    "v_add_u32 v241, s58, v242\n"
    "s_mov_b32 s22, 14336\n"
    "s_mov_b32 s56, 15360\n"
    "s_add_u32 s23, s22, 458752\n"
    "s_add_u32 s57, s56, 458752\n"
    "buffer_load_dwordx4 a[0:3], v242, s[80:83], s22 offen offset:0 sc1\n"
    "s_add_u32 s58, s30, 2\n"
    "buffer_load_dwordx4 a[4:7], v242, s[80:83], s23 offen offset:0 sc1\n"
    "s_and_b32 s58, s58, 3\n"
    "buffer_load_dwordx4 a[16:19], v242, s[80:83], s56 offen offset:0 sc1\n"
    "s_mul_i32 s58, s58, 24576\n"
    "buffer_load_dwordx4 a[20:23], v242, s[80:83], s57 offen offset:0 sc1\n"
    "v_add_u32 v246, s58, v242\n"
    "s_add_u32 s58, s30, 1\n"
    "s_and_b32 s58, s58, 3\n"
    "s_mul_i32 s58, s58, 24576\n"
    "v_add_u32 v247, s58, v242\n"
    "s_nop 0\n"
    "v_accvgpr_read_b32 v144, a192\n"
    "v_accvgpr_read_b32 v145, a193\n"
    "v_accvgpr_read_b32 v146, a194\n"
    "v_accvgpr_read_b32 v147, a195\n"
    "v_accvgpr_read_b32 v148, a196\n"
    "v_accvgpr_read_b32 v149, a197\n"
    "v_accvgpr_read_b32 v150, a198\n"
    "v_accvgpr_read_b32 v151, a199\n"
    "s_nop 0\n"
    "v_permlane16_swap_b32 v144, v146\n"
    "v_permlane16_swap_b32 v148, v150\n"
    "v_permlane16_swap_b32 v145, v147\n"
    "v_permlane16_swap_b32 v149, v151\n"
    "s_nop 0\n"
    "v_permlane32_swap_b32 v144, v148\n"
    "v_permlane32_swap_b32 v146, v150\n"
    "v_permlane32_swap_b32 v145, v149\n"
    "v_permlane32_swap_b32 v147, v151\n"
    "v_lshlrev_b32 v64, 16, v144\n"
    "v_and_b32 v65, 0xffff0000, v144\n"
    "v_lshlrev_b32 v66, 16, v145\n"
    "v_and_b32 v67, 0xffff0000, v145\n"
    "v_lshlrev_b32 v68, 16, v146\n"
    "v_and_b32 v69, 0xffff0000, v146\n"
    "v_lshlrev_b32 v70, 16, v147\n"
    "v_and_b32 v71, 0xffff0000, v147\n"
    "v_lshlrev_b32 v72, 16, v148\n"
    "v_and_b32 v73, 0xffff0000, v148\n"
    "v_lshlrev_b32 v74, 16, v149\n"
    "v_and_b32 v75, 0xffff0000, v149\n"
    "v_lshlrev_b32 v76, 16, v150\n"
    "v_and_b32 v77, 0xffff0000, v150\n"
    "v_lshlrev_b32 v78, 16, v151\n"
    "v_and_b32 v79, 0xffff0000, v151\n"
    "s_cmp_eq_u32 s30, 0\n"
    "s_cbranch_scc1 YW0\n"
    "s_cmp_eq_u32 s30, 1\n"
    "s_cbranch_scc1 YW1\n"
    "s_cmp_eq_u32 s30, 2\n"
    "s_cbranch_scc1 YW2\n"
    "YW3:\n"
    "ds_write_b128 v240, v[176:179] offset:0\n"
    "ds_write_b128 v240, v[180:183] offset:1024\n"
    "ds_write_b128 v240, v[184:187] offset:2048\n"
    "ds_write_b128 v240, v[188:191] offset:3072\n"
    "ds_write_b128 v240, v[192:195] offset:4096\n"
    "ds_write_b128 v240, v[196:199] offset:5120\n"
    "ds_write_b128 v240, v[200:203] offset:6144\n"
    "ds_write_b128 v240, v[204:207] offset:7168\n"
    "ds_write_b128 v240, v[208:211] offset:8192\n"
    "ds_write_b128 v240, v[212:215] offset:9216\n"
    "ds_write_b128 v240, v[216:219] offset:10240\n"
    "ds_write_b128 v240, v[220:223] offset:11264\n"
    "YWD:\n"
    "s_waitcnt lgkmcnt(0)\n"
    "s_barrier\n"
    "ds_read_b128 v[176:179], v241 offset:0\n"
    "ds_read_b128 v[180:183], v241 offset:1024\n"
    "ds_read_b128 v[184:187], v241 offset:2048\n"
    "ds_read_b128 v[188:191], v241 offset:3072\n"
    "ds_read_b128 v[192:195], v246 offset:4096\n"
    "ds_read_b128 v[196:199], v246 offset:5120\n"
    "ds_read_b128 v[200:203], v246 offset:6144\n"
    "ds_read_b128 v[204:207], v246 offset:7168\n"
    "ds_read_b128 v[208:211], v247 offset:8192\n"
    "ds_read_b128 v[212:215], v247 offset:9216\n"
    "ds_read_b128 v[216:219], v247 offset:10240\n"
    "ds_read_b128 v[220:223], v247 offset:11264\n"
    "s_waitcnt vmcnt(12)\n"
    "v_mfma_f32_16x16x32_bf16 v[120:123], v[112:115], a[8:11], 0\n"
    "v_mfma_f32_16x16x32_bf16 v[124:127], v[116:119], a[8:11], 0\n"
    "v_mfma_f32_16x16x32_bf16 v[128:131], v[112:115], a[24:27], 0\n"
    "v_mfma_f32_16x16x32_bf16 v[132:135], v[116:119], a[24:27], 0\n"
    "v_mfma_f32_16x16x32_bf16 v[120:123], v[112:115], a[40:43], v[120:123]\n"
    "v_mfma_f32_16x16x32_bf16 v[124:127], v[116:119], a[40:43], v[124:127]\n"
    "v_mfma_f32_16x16x32_bf16 v[128:131], v[112:115], a[56:59], v[128:131]\n"
    "v_mfma_f32_16x16x32_bf16 v[132:135], v[116:119], a[56:59], v[132:135]\n"
    "s_waitcnt lgkmcnt(0)\n"
    "v_mov_b32 v160, 0xbfb8aa3b\n"
    "v_mfma_f32_16x16x32_bf16 v[120:123], v[112:115], a[72:75], v[120:123]\n"
    "v_mov_b32 v161, 1.0\n"
    "v_add_f32 v224, v224, v176\n"
    "v_add_f32 v225, v225, v177\n"
    "v_mfma_f32_16x16x32_bf16 v[124:127], v[116:119], a[72:75], v[124:127]\n"
    "v_add_f32 v226, v226, v178\n"
    "v_add_f32 v227, v227, v179\n"
    "v_add_f32 v228, v228, v180\n"
    "v_mfma_f32_16x16x32_bf16 v[128:131], v[112:115], a[88:91], v[128:131]\n"
    "v_add_f32 v229, v229, v181\n"
    "v_add_f32 v230, v230, v182\n"
    "v_add_f32 v231, v231, v183\n"
    "v_mfma_f32_16x16x32_bf16 v[132:135], v[116:119], a[88:91], v[132:135]\n"
    "v_add_f32 v232, v232, v184\n"
    "v_add_f32 v233, v233, v185\n"
    "v_add_f32 v234, v234, v186\n"
    "v_mfma_f32_16x16x32_bf16 v[120:123], v[112:115], a[104:107], v[120:123]\n"
    "v_add_f32 v235, v235, v187\n"
    "v_add_f32 v236, v236, v188\n"
    "v_add_f32 v237, v237, v189\n"
    "v_mfma_f32_16x16x32_bf16 v[124:127], v[116:119], a[104:107], v[124:127]\n"
    "v_add_f32 v238, v238, v190\n"
    "v_add_f32 v239, v239, v191\n"
    "v_add_f32 v224, v224, v192\n"
    "v_mfma_f32_16x16x32_bf16 v[128:131], v[112:115], a[120:123], v[128:131]\n"
    "v_add_f32 v225, v225, v193\n"
    "v_add_f32 v226, v226, v194\n"
    "v_add_f32 v227, v227, v195\n"
    "v_mfma_f32_16x16x32_bf16 v[132:135], v[116:119], a[120:123], v[132:135]\n"
    "v_add_f32 v228, v228, v196\n"
    "v_add_f32 v229, v229, v197\n"
    "v_add_f32 v230, v230, v198\n"
    "v_mfma_f32_16x16x32_bf16 v[120:123], v[112:115], a[136:139], v[120:123]\n"
    "v_add_f32 v231, v231, v199\n"
    "v_add_f32 v232, v232, v200\n"
    "v_add_f32 v233, v233, v201\n"
    "v_mfma_f32_16x16x32_bf16 v[124:127], v[116:119], a[136:139], v[124:127]\n"
    "v_add_f32 v234, v234, v202\n"
    "v_add_f32 v235, v235, v203\n"
    "v_add_f32 v236, v236, v204\n"
    "v_mfma_f32_16x16x32_bf16 v[128:131], v[112:115], a[152:155], v[128:131]\n"
    "v_add_f32 v237, v237, v205\n"
    "v_add_f32 v238, v238, v206\n"
    "v_add_f32 v239, v239, v207\n"
    "v_mfma_f32_16x16x32_bf16 v[132:135], v[116:119], a[152:155], v[132:135]\n"
    "v_add_f32 v224, v224, v208\n"
    "v_add_f32 v225, v225, v209\n"
    "v_add_f32 v226, v226, v210\n"
    "v_mfma_f32_16x16x32_bf16 v[120:123], v[112:115], a[168:171], v[120:123]\n"
    "v_add_f32 v227, v227, v211\n"
    "v_add_f32 v228, v228, v212\n"
    "v_add_f32 v229, v229, v213\n"
    "v_mfma_f32_16x16x32_bf16 v[124:127], v[116:119], a[168:171], v[124:127]\n"
    "v_add_f32 v230, v230, v214\n"
    "v_add_f32 v231, v231, v215\n"
    "v_add_f32 v232, v232, v216\n"
    "v_mfma_f32_16x16x32_bf16 v[128:131], v[112:115], a[184:187], v[128:131]\n"
    "v_add_f32 v233, v233, v217\n"
    "v_add_f32 v234, v234, v218\n"
    "v_add_f32 v235, v235, v219\n"
    "v_mfma_f32_16x16x32_bf16 v[132:135], v[116:119], a[184:187], v[132:135]\n"
    "v_add_f32 v236, v236, v220\n"
    "v_add_f32 v237, v237, v221\n"
    "v_add_f32 v238, v238, v222\n"
    "v_mfma_f32_16x16x32_bf16 v[120:123], v[112:115], a[200:203], v[120:123]\n"
    "v_add_f32 v239, v239, v223\n"
    "v_mul_f32 v144, v160, v224\n"
    "v_mul_f32 v145, v160, v225\n"
    "v_mfma_f32_16x16x32_bf16 v[124:127], v[116:119], a[200:203], v[124:127]\n"
    "v_mul_f32 v146, v160, v226\n"
    "v_mul_f32 v147, v160, v227\n"
    "v_mul_f32 v148, v160, v228\n"
    "v_mfma_f32_16x16x32_bf16 v[128:131], v[112:115], a[216:219], v[128:131]\n"
    "v_mul_f32 v149, v160, v229\n"
    "v_mul_f32 v150, v160, v230\n"
    "v_mul_f32 v151, v160, v231\n"
    "v_mfma_f32_16x16x32_bf16 v[132:135], v[116:119], a[216:219], v[132:135]\n"
    "v_mul_f32 v152, v160, v232\n"
    "v_mul_f32 v153, v160, v233\n"
    "v_mul_f32 v154, v160, v234\n"
    "v_mfma_f32_16x16x32_bf16 v[120:123], v[112:115], a[232:235], v[120:123]\n"
    "v_mul_f32 v155, v160, v235\n"
    "v_mul_f32 v156, v160, v236\n"
    "v_mul_f32 v157, v160, v237\n"
    "v_mfma_f32_16x16x32_bf16 v[124:127], v[116:119], a[232:235], v[124:127]\n"
    "v_mul_f32 v158, v160, v238\n"
    "v_mul_f32 v159, v160, v239\n"
    "v_exp_f32 v144, v144\n"
    "v_mfma_f32_16x16x32_bf16 v[128:131], v[112:115], a[248:251], v[128:131]\n"
    "v_exp_f32 v145, v145\n"
    "v_exp_f32 v146, v146\n"
    "v_exp_f32 v147, v147\n"
    "v_mfma_f32_16x16x32_bf16 v[132:135], v[116:119], a[248:251], v[132:135]\n"
    "v_exp_f32 v148, v148\n"
    "v_exp_f32 v149, v149\n"
    "v_exp_f32 v150, v150\n"
    "v_mfma_f32_16x16x32_bf16 v[120:123], v[112:115], a[12:15], v[120:123]\n"
    "v_exp_f32 v151, v151\n"
    "v_exp_f32 v152, v152\n"
    "v_exp_f32 v153, v153\n"
    "v_mfma_f32_16x16x32_bf16 v[124:127], v[116:119], a[12:15], v[124:127]\n"
    "v_exp_f32 v154, v154\n"
    "v_exp_f32 v155, v155\n"
    "v_exp_f32 v156, v156\n"
    "v_mfma_f32_16x16x32_bf16 v[128:131], v[112:115], a[28:31], v[128:131]\n"
    "v_exp_f32 v157, v157\n"
    "v_exp_f32 v158, v158\n"
    "v_exp_f32 v159, v159\n"
    "v_mfma_f32_16x16x32_bf16 v[132:135], v[116:119], a[28:31], v[132:135]\n"
    "v_add_f32 v144, 1.0, v144\n"
    "v_add_f32 v145, 1.0, v145\n"
    "v_add_f32 v146, 1.0, v146\n"
    "v_mfma_f32_16x16x32_bf16 v[120:123], v[112:115], a[44:47], v[120:123]\n"
    "v_add_f32 v147, 1.0, v147\n"
    "v_add_f32 v148, 1.0, v148\n"
    "v_add_f32 v149, 1.0, v149\n"
    "v_mfma_f32_16x16x32_bf16 v[124:127], v[116:119], a[44:47], v[124:127]\n"
    "v_add_f32 v150, 1.0, v150\n"
    "v_add_f32 v151, 1.0, v151\n"
    "v_add_f32 v152, 1.0, v152\n"
    "v_mfma_f32_16x16x32_bf16 v[128:131], v[112:115], a[60:63], v[128:131]\n"
    "v_add_f32 v153, 1.0, v153\n"
    "v_add_f32 v154, 1.0, v154\n"
    "v_add_f32 v155, 1.0, v155\n"
    "v_mfma_f32_16x16x32_bf16 v[132:135], v[116:119], a[60:63], v[132:135]\n"
    "v_add_f32 v156, 1.0, v156\n"
    "v_add_f32 v157, 1.0, v157\n"
    "v_add_f32 v158, 1.0, v158\n"
    "v_add_f32 v159, 1.0, v159\n"
    "v_rcp_f32 v144, v144\n"
    "v_rcp_f32 v145, v145\n"
    "v_rcp_f32 v146, v146\n"
    "v_rcp_f32 v147, v147\n"
    "v_rcp_f32 v148, v148\n"
    "v_rcp_f32 v149, v149\n"
    "v_rcp_f32 v150, v150\n"
    "v_rcp_f32 v151, v151\n"
    "v_rcp_f32 v152, v152\n"
    "v_rcp_f32 v153, v153\n"
    "v_rcp_f32 v154, v154\n"
    "v_rcp_f32 v155, v155\n"
    "v_rcp_f32 v156, v156\n"
    "v_rcp_f32 v157, v157\n"
    "v_rcp_f32 v158, v158\n"
    "v_rcp_f32 v159, v159\n"
    "v_mul_f32 v224, v224, v144\n"
    "v_mul_f32 v225, v225, v145\n"
    "v_mul_f32 v226, v226, v146\n"
    "v_mul_f32 v227, v227, v147\n"
    "v_mul_f32 v228, v228, v148\n"
    "v_mul_f32 v229, v229, v149\n"
    "v_mul_f32 v230, v230, v150\n"
    "v_mul_f32 v231, v231, v151\n"
    "v_mul_f32 v232, v232, v152\n"
    "v_mul_f32 v233, v233, v153\n"
    "v_mul_f32 v234, v234, v154\n"
    "v_mul_f32 v235, v235, v155\n"
    "v_mul_f32 v236, v236, v156\n"
    "v_mul_f32 v237, v237, v157\n"
    "v_mul_f32 v238, v238, v158\n"
    "v_mul_f32 v239, v239, v159\n"
    "s_waitcnt vmcnt(8)\n"
    "v_mfma_f32_16x16x32_bf16 v[120:123], v[112:115], a[140:143], v[120:123]\n"
    "v_mfma_f32_16x16x32_bf16 v[124:127], v[116:119], a[140:143], v[124:127]\n"
    "v_mfma_f32_16x16x32_bf16 v[128:131], v[112:115], a[156:159], v[128:131]\n"
    "v_mfma_f32_16x16x32_bf16 v[132:135], v[116:119], a[156:159], v[132:135]\n"
    "v_mfma_f32_16x16x32_bf16 v[120:123], v[112:115], a[172:175], v[120:123]\n"
    "v_mfma_f32_16x16x32_bf16 v[124:127], v[116:119], a[172:175], v[124:127]\n"
    "v_mfma_f32_16x16x32_bf16 v[128:131], v[112:115], a[188:191], v[128:131]\n"
    "v_mfma_f32_16x16x32_bf16 v[132:135], v[116:119], a[188:191], v[132:135]\n"
    "s_waitcnt vmcnt(4)\n"
    "v_mfma_f32_16x16x32_bf16 v[120:123], v[112:115], a[204:207], v[120:123]\n"
    "v_mfma_f32_16x16x32_bf16 v[124:127], v[116:119], a[204:207], v[124:127]\n"
    "v_mfma_f32_16x16x32_bf16 v[128:131], v[112:115], a[220:223], v[128:131]\n"
    "v_mfma_f32_16x16x32_bf16 v[132:135], v[116:119], a[220:223], v[132:135]\n"
    "v_mfma_f32_16x16x32_bf16 v[120:123], v[112:115], a[236:239], v[120:123]\n"
    "v_mfma_f32_16x16x32_bf16 v[124:127], v[116:119], a[236:239], v[124:127]\n"
    "v_mfma_f32_16x16x32_bf16 v[128:131], v[112:115], a[252:255], v[128:131]\n"
    "v_mfma_f32_16x16x32_bf16 v[132:135], v[116:119], a[252:255], v[132:135]\n"
    "s_waitcnt vmcnt(0)\n"
    "v_mfma_f32_16x16x32_bf16 v[120:123], v[112:115], a[0:3], v[120:123]\n"
    "v_mfma_f32_16x16x32_bf16 v[124:127], v[116:119], a[0:3], v[124:127]\n"
    "v_mfma_f32_16x16x32_bf16 v[128:131], v[112:115], a[4:7], v[128:131]\n"
    "v_mfma_f32_16x16x32_bf16 v[132:135], v[116:119], a[4:7], v[132:135]\n"
    "v_mfma_f32_16x16x32_bf16 v[120:123], v[112:115], a[16:19], v[120:123]\n"
    "v_mfma_f32_16x16x32_bf16 v[124:127], v[116:119], a[16:19], v[124:127]\n"
    "v_mfma_f32_16x16x32_bf16 v[128:131], v[112:115], a[20:23], v[128:131]\n"
    "v_mfma_f32_16x16x32_bf16 v[132:135], v[116:119], a[20:23], v[132:135]\n"
    "s_nop 7\n"
    "v_fma_f32 v224, v224, v120, v64\n"
    "v_fma_f32 v225, v225, v121, v65\n"
    "v_fma_f32 v226, v226, v122, v66\n"
    "v_fma_f32 v227, v227, v123, v67\n"
    "v_cvt_pk_bf16_f32 v136, v224, v225\n"
    "v_cvt_pk_bf16_f32 v137, v226, v227\n"
    "v_fma_f32 v228, v228, v124, v68\n"
    "v_fma_f32 v229, v229, v125, v69\n"
    "v_fma_f32 v230, v230, v126, v70\n"
    "v_fma_f32 v231, v231, v127, v71\n"
    "v_cvt_pk_bf16_f32 v138, v228, v229\n"
    "v_cvt_pk_bf16_f32 v139, v230, v231\n"
    "v_fma_f32 v232, v232, v128, v72\n"
    "v_fma_f32 v233, v233, v129, v73\n"
    "v_fma_f32 v234, v234, v130, v74\n"
    "v_fma_f32 v235, v235, v131, v75\n"
    "v_cvt_pk_bf16_f32 v140, v232, v233\n"
    "v_cvt_pk_bf16_f32 v141, v234, v235\n"
    "v_fma_f32 v236, v236, v132, v76\n"
    "v_fma_f32 v237, v237, v133, v77\n"
    "v_fma_f32 v238, v238, v134, v78\n"
    "v_fma_f32 v239, v239, v135, v79\n"
    "v_cvt_pk_bf16_f32 v142, v236, v237\n"
    "v_cvt_pk_bf16_f32 v143, v238, v239\n"
    "s_nop 0\n"
    "v_permlane32_swap_b32 v136, v140\n"
    "v_permlane32_swap_b32 v138, v142\n"
    "v_permlane32_swap_b32 v137, v141\n"
    "v_permlane32_swap_b32 v139, v143\n"
    "s_nop 0\n"
    "v_permlane16_swap_b32 v136, v138\n"
    "v_permlane16_swap_b32 v140, v142\n"
    "v_permlane16_swap_b32 v137, v139\n"
    "v_permlane16_swap_b32 v141, v143\n"
    "buffer_store_dwordx4 v[136:139], v87, s[72:75], s60 offen offset:0\n"
    "buffer_store_dwordx4 v[140:143], v87, s[72:75], s60 offen offset:16\n"
    "FINEND:\n"
    "s_waitcnt vmcnt(0)\n"
    "s_endpgm\n"
    "DYN:\n"
    "s_bitcmp1_b32 s28, 0\n"
    "s_cbranch_scc0 DEF0\n"
    "v_cmp_eq_u32 s[58:59], 1, v243\n"
    "s_andn2_b64 vcc, s[58:59], s[18:19]\n"
    "s_cbranch_scc1 DEF0\n"
    "s_bitset0_b32 s28, 0\n"
    "s_add_u32 s22, s9, 7\n"
    "s_mul_i32 s56, s22, 37\n"
    "s_lshr_b32 s56, s56, 8\n"
    "s_mul_i32 s56, s56, 7\n"
    "s_sub_u32 s22, s22, s56\n"
    "s_lshl_b32 s22, s22, 10\n"
    "s_add_u32 s56, s22, 7168\n"
    "s_add_u32 s23, s22, 458752\n"
    "s_add_u32 s57, s56, 458752\n"
    "buffer_load_dwordx4 a[8:11], v242, s[80:83], s22 offen offset:0 sc1\n"
    "buffer_load_dwordx4 a[24:27], v242, s[80:83], s23 offen offset:0 sc1\n"
    "buffer_load_dwordx4 a[40:43], v242, s[80:83], s56 offen offset:0 sc1\n"
    "buffer_load_dwordx4 a[56:59], v242, s[80:83], s57 offen offset:0 sc1\n"
    "DEF0:\n"
    "s_bitcmp1_b32 s28, 1\n"
    "s_cbranch_scc0 DEF1\n"
    "v_cmp_eq_u32 s[58:59], 2, v243\n"
    "s_andn2_b64 vcc, s[58:59], s[18:19]\n"
    "s_cbranch_scc1 DEF1\n"
    "s_bitset0_b32 s28, 1\n"
    "s_add_u32 s22, s9, 6\n"
    "s_mul_i32 s56, s22, 37\n"
    "s_lshr_b32 s56, s56, 8\n"
    "s_mul_i32 s56, s56, 7\n"
    "s_sub_u32 s22, s22, s56\n"
    "s_lshl_b32 s22, s22, 10\n"
    "s_add_u32 s56, s22, 7168\n"
    "s_add_u32 s23, s22, 458752\n"
    "s_add_u32 s57, s56, 458752\n"
    "buffer_load_dwordx4 a[72:75], v242, s[80:83], s22 offen offset:0 sc1\n"
    "buffer_load_dwordx4 a[88:91], v242, s[80:83], s23 offen offset:0 sc1\n"
    "buffer_load_dwordx4 a[104:107], v242, s[80:83], s56 offen offset:0 sc1\n"
    "buffer_load_dwordx4 a[120:123], v242, s[80:83], s57 offen offset:0 sc1\n"
    "DEF1:\n"
    "s_bitcmp1_b32 s28, 2\n"
    "s_cbranch_scc0 DEF2\n"
    "v_cmp_eq_u32 s[58:59], 3, v243\n"
    "s_andn2_b64 vcc, s[58:59], s[18:19]\n"
    "s_cbranch_scc1 DEF2\n"
    "s_bitset0_b32 s28, 2\n"
    "s_add_u32 s22, s9, 5\n"
    "s_mul_i32 s56, s22, 37\n"
    "s_lshr_b32 s56, s56, 8\n"
    "s_mul_i32 s56, s56, 7\n"
    "s_sub_u32 s22, s22, s56\n"
    "s_lshl_b32 s22, s22, 10\n"
    "s_add_u32 s56, s22, 7168\n"
    "s_add_u32 s23, s22, 458752\n"
    "s_add_u32 s57, s56, 458752\n"
    "buffer_load_dwordx4 a[136:139], v242, s[80:83], s22 offen offset:0 sc1\n"
    "buffer_load_dwordx4 a[152:155], v242, s[80:83], s23 offen offset:0 sc1\n"
    "buffer_load_dwordx4 a[168:171], v242, s[80:83], s56 offen offset:0 sc1\n"
    "buffer_load_dwordx4 a[184:187], v242, s[80:83], s57 offen offset:0 sc1\n"
    "DEF2:\n"
    "s_bitcmp1_b32 s28, 3\n"
    "s_cbranch_scc0 DEF3\n"
    "v_cmp_eq_u32 s[58:59], 4, v243\n"
    "s_andn2_b64 vcc, s[58:59], s[18:19]\n"
    "s_cbranch_scc1 DEF3\n"
    "s_bitset0_b32 s28, 3\n"
    "s_add_u32 s22, s9, 4\n"
    "s_mul_i32 s56, s22, 37\n"
    "s_lshr_b32 s56, s56, 8\n"
    "s_mul_i32 s56, s56, 7\n"
    "s_sub_u32 s22, s22, s56\n"
    "s_lshl_b32 s22, s22, 10\n"
    "s_add_u32 s56, s22, 7168\n"
    "s_add_u32 s23, s22, 458752\n"
    "s_add_u32 s57, s56, 458752\n"
    "buffer_load_dwordx4 a[200:203], v242, s[80:83], s22 offen offset:0 sc1\n"
    "buffer_load_dwordx4 a[216:219], v242, s[80:83], s23 offen offset:0 sc1\n"
    "buffer_load_dwordx4 a[232:235], v242, s[80:83], s56 offen offset:0 sc1\n"
    "buffer_load_dwordx4 a[248:251], v242, s[80:83], s57 offen offset:0 sc1\n"
    "DEF3:\n"
    "s_bitcmp1_b32 s28, 4\n"
    "s_cbranch_scc0 DEF4\n"
    "v_cmp_eq_u32 s[58:59], 5, v243\n"
    "s_andn2_b64 vcc, s[58:59], s[18:19]\n"
    "s_cbranch_scc1 DEF4\n"
    "s_bitset0_b32 s28, 4\n"
    "s_add_u32 s22, s9, 3\n"
    "s_mul_i32 s56, s22, 37\n"
    "s_lshr_b32 s56, s56, 8\n"
    "s_mul_i32 s56, s56, 7\n"
    "s_sub_u32 s22, s22, s56\n"
    "s_lshl_b32 s22, s22, 10\n"
    "s_add_u32 s56, s22, 7168\n"
    "s_add_u32 s23, s22, 458752\n"
    "s_add_u32 s57, s56, 458752\n"
    "buffer_load_dwordx4 a[12:15], v242, s[80:83], s22 offen offset:0 sc1\n"
    "buffer_load_dwordx4 a[28:31], v242, s[80:83], s23 offen offset:0 sc1\n"
    "buffer_load_dwordx4 a[44:47], v242, s[80:83], s56 offen offset:0 sc1\n"
    "buffer_load_dwordx4 a[60:63], v242, s[80:83], s57 offen offset:0 sc1\n"
    "DEF4:\n"
    "s_cmp_eq_u64 s[18:19], -1\n"
    "s_cbranch_scc1 DYNR\n"
    "s_sub_u32 s96, s96, 1\n"
    "s_cbranch_scc1 DYNR\n"
    "s_sleep 2\n"
    "global_load_dwordx2 v[254:255], v250, s[20:21] sc1\n"
    "s_waitcnt vmcnt(0)\n"
    "v_cmp_eq_u64 s[18:19], s[70:71], v[254:255]\n"
    "s_nop 0\n"
    "s_branch DYN\n"
    "YW0:\n"
    "ds_write_b128 v240, v[192:195] offset:0\n"
    "ds_write_b128 v240, v[196:199] offset:1024\n"
    "ds_write_b128 v240, v[200:203] offset:2048\n"
    "ds_write_b128 v240, v[204:207] offset:3072\n"
    "ds_write_b128 v240, v[208:211] offset:4096\n"
    "ds_write_b128 v240, v[212:215] offset:5120\n"
    "ds_write_b128 v240, v[216:219] offset:6144\n"
    "ds_write_b128 v240, v[220:223] offset:7168\n"
    "ds_write_b128 v240, v[224:227] offset:8192\n"
    "ds_write_b128 v240, v[228:231] offset:9216\n"
    "ds_write_b128 v240, v[232:235] offset:10240\n"
    "ds_write_b128 v240, v[236:239] offset:11264\n"
    "s_waitcnt lgkmcnt(0)\n"
    "v_mov_b32 v224, v176\n"
    "v_mov_b32 v225, v177\n"
    "v_mov_b32 v226, v178\n"
    "v_mov_b32 v227, v179\n"
    "v_mov_b32 v228, v180\n"
    "v_mov_b32 v229, v181\n"
    "v_mov_b32 v230, v182\n"
    "v_mov_b32 v231, v183\n"
    "v_mov_b32 v232, v184\n"
    "v_mov_b32 v233, v185\n"
    "v_mov_b32 v234, v186\n"
    "v_mov_b32 v235, v187\n"
    "v_mov_b32 v236, v188\n"
    "v_mov_b32 v237, v189\n"
    "v_mov_b32 v238, v190\n"
    "v_mov_b32 v239, v191\n"
    "s_branch YWD\n"
    "YW1:\n"
    "ds_write_b128 v240, v[208:211] offset:0\n"
    "ds_write_b128 v240, v[212:215] offset:1024\n"
    "ds_write_b128 v240, v[216:219] offset:2048\n"
    "ds_write_b128 v240, v[220:223] offset:3072\n"
    "ds_write_b128 v240, v[224:227] offset:4096\n"
    "ds_write_b128 v240, v[228:231] offset:5120\n"
    "ds_write_b128 v240, v[232:235] offset:6144\n"
    "ds_write_b128 v240, v[236:239] offset:7168\n"
    "ds_write_b128 v240, v[176:179] offset:8192\n"
    "ds_write_b128 v240, v[180:183] offset:9216\n"
    "ds_write_b128 v240, v[184:187] offset:10240\n"
    "ds_write_b128 v240, v[188:191] offset:11264\n"
    "s_waitcnt lgkmcnt(0)\n"
    "v_mov_b32 v224, v192\n"
    "v_mov_b32 v225, v193\n"
    "v_mov_b32 v226, v194\n"
    "v_mov_b32 v227, v195\n"
    "v_mov_b32 v228, v196\n"
    "v_mov_b32 v229, v197\n"
    "v_mov_b32 v230, v198\n"
    "v_mov_b32 v231, v199\n"
    "v_mov_b32 v232, v200\n"
    "v_mov_b32 v233, v201\n"
    "v_mov_b32 v234, v202\n"
    "v_mov_b32 v235, v203\n"
    "v_mov_b32 v236, v204\n"
    "v_mov_b32 v237, v205\n"
    "v_mov_b32 v238, v206\n"
    "v_mov_b32 v239, v207\n"
    "s_branch YWD\n"
    "YW2:\n"
    "ds_write_b128 v240, v[224:227] offset:0\n"
    "ds_write_b128 v240, v[228:231] offset:1024\n"
    "ds_write_b128 v240, v[232:235] offset:2048\n"
    "ds_write_b128 v240, v[236:239] offset:3072\n"
    "ds_write_b128 v240, v[176:179] offset:4096\n"
    "ds_write_b128 v240, v[180:183] offset:5120\n"
    "ds_write_b128 v240, v[184:187] offset:6144\n"
    "ds_write_b128 v240, v[188:191] offset:7168\n"
    "ds_write_b128 v240, v[192:195] offset:8192\n"
    "ds_write_b128 v240, v[196:199] offset:9216\n"
    "ds_write_b128 v240, v[200:203] offset:10240\n"
    "ds_write_b128 v240, v[204:207] offset:11264\n"
    "s_waitcnt lgkmcnt(0)\n"
    "v_mov_b32 v224, v208\n"
    "v_mov_b32 v225, v209\n"
    "v_mov_b32 v226, v210\n"
    "v_mov_b32 v227, v211\n"
    "v_mov_b32 v228, v212\n"
    "v_mov_b32 v229, v213\n"
    "v_mov_b32 v230, v214\n"
    "v_mov_b32 v231, v215\n"
    "v_mov_b32 v232, v216\n"
    "v_mov_b32 v233, v217\n"
    "v_mov_b32 v234, v218\n"
    "v_mov_b32 v235, v219\n"
    "v_mov_b32 v236, v220\n"
    "v_mov_b32 v237, v221\n"
    "v_mov_b32 v238, v222\n"
    "v_mov_b32 v239, v223\n"
    "s_branch YWD\n"
    "K1END:\n"
    "SPINH:\n"
    "s_mov_b32 s31, 131072\n"
    "SPINH_:\n"
    "s_sleep 2\n"
    "global_load_dwordx2 v[128:129], v130, s[20:21] sc1\n"
    "s_waitcnt vmcnt(0)\n"
    "v_cmp_ne_u64 vcc, s[70:71], v[128:129]\n"
    "s_cmp_lg_u64 vcc, 0\n"
    "s_cbranch_scc0 SPINH_D\n"
    "s_sub_u32 s31, s31, 1\n"
    "s_cbranch_scc0 SPINH_\n"
    "SPINH_D:\n"
    "s_branch SPINHD\n"
    "STUB1z_m:\n"
    "s_mov_b32 s59, 0\n"
    "s_branch RARE1_m\n"
    "STUB1_m:\n"
    "s_mov_b32 s59, 1\n"
    "s_branch RARE1_m\n"
    "STUB1l_m:\n"
    "s_mov_b32 s59, 2\n"
    "s_branch RARE1_m\n"
    "RARE1_m:\n"
    "s_nop 15\n"
    "v_max3_f32 v249, v128, v129, v130\n"
    "v_max3_f32 v249, v249, v131, v144\n"
    "v_max3_f32 v249, v249, v145, v146\n"
    "v_max_f32 v249, v249, v147\n"
    "v_mov_b32 v232, v249\n"
    "s_nop 1\n"
    "v_permlane32_swap_b32 v249, v232\n"
    "s_nop 1\n"
    "v_max_f32 v249, v249, v232\n"
    "v_mov_b32 v232, v249\n"
    "s_nop 1\n"
    "v_permlane16_swap_b32 v249, v232\n"
    "s_nop 1\n"
    "v_max_f32 v249, v249, v232\n"
    "v_max_f32 v249, 0, v249\n"
    "v_exp_f32 v250, -v249\n"
    "v_sub_f32 v192, v192, v249\n"
    "v_sub_f32 v193, v193, v249\n"
    "v_sub_f32 v194, v194, v249\n"
    "v_sub_f32 v195, v195, v249\n"
    "v_mul_f32 v240, v240, v250\n"
    "v_accvgpr_read_b32 v232, a0\n"
    "v_accvgpr_read_b32 v233, a1\n"
    "v_accvgpr_read_b32 v234, a2\n"
    "v_accvgpr_read_b32 v235, a3\n"
    "v_mul_f32 v232, v232, v250\n"
    "v_mul_f32 v233, v233, v250\n"
    "v_mul_f32 v234, v234, v250\n"
    "v_mul_f32 v235, v235, v250\n"
    "v_accvgpr_write_b32 a0, v232\n"
    "v_accvgpr_write_b32 a1, v233\n"
    "v_accvgpr_write_b32 a2, v234\n"
    "v_accvgpr_write_b32 a3, v235\n"
    "v_accvgpr_read_b32 v232, a16\n"
    "v_accvgpr_read_b32 v233, a17\n"
    "v_accvgpr_read_b32 v234, a18\n"
    "v_accvgpr_read_b32 v235, a19\n"
    "v_mul_f32 v232, v232, v250\n"
    "v_mul_f32 v233, v233, v250\n"
    "v_mul_f32 v234, v234, v250\n"
    "v_mul_f32 v235, v235, v250\n"
    "v_accvgpr_write_b32 a16, v232\n"
    "v_accvgpr_write_b32 a17, v233\n"
    "v_accvgpr_write_b32 a18, v234\n"
    "v_accvgpr_write_b32 a19, v235\n"
    "v_accvgpr_read_b32 v232, a32\n"
    "v_accvgpr_read_b32 v233, a33\n"
    "v_accvgpr_read_b32 v234, a34\n"
    "v_accvgpr_read_b32 v235, a35\n"
    "v_mul_f32 v232, v232, v250\n"
    "v_mul_f32 v233, v233, v250\n"
    "v_mul_f32 v234, v234, v250\n"
    "v_mul_f32 v235, v235, v250\n"
    "v_accvgpr_write_b32 a32, v232\n"
    "v_accvgpr_write_b32 a33, v233\n"
    "v_accvgpr_write_b32 a34, v234\n"
    "v_accvgpr_write_b32 a35, v235\n"
    "v_accvgpr_read_b32 v232, a48\n"
    "v_accvgpr_read_b32 v233, a49\n"
    "v_accvgpr_read_b32 v234, a50\n"
    "v_accvgpr_read_b32 v235, a51\n"
    "v_mul_f32 v232, v232, v250\n"
    "v_mul_f32 v233, v233, v250\n"
    "v_mul_f32 v234, v234, v250\n"
    "v_mul_f32 v235, v235, v250\n"
    "v_accvgpr_write_b32 a48, v232\n"
    "v_accvgpr_write_b32 a49, v233\n"
    "v_accvgpr_write_b32 a50, v234\n"
    "v_accvgpr_write_b32 a51, v235\n"
    "v_accvgpr_read_b32 v232, a64\n"
    "v_accvgpr_read_b32 v233, a65\n"
    "v_accvgpr_read_b32 v234, a66\n"
    "v_accvgpr_read_b32 v235, a67\n"
    "v_mul_f32 v232, v232, v250\n"
    "v_mul_f32 v233, v233, v250\n"
    "v_mul_f32 v234, v234, v250\n"
    "v_mul_f32 v235, v235, v250\n"
    "v_accvgpr_write_b32 a64, v232\n"
    "v_accvgpr_write_b32 a65, v233\n"
    "v_accvgpr_write_b32 a66, v234\n"
    "v_accvgpr_write_b32 a67, v235\n"
    "v_accvgpr_read_b32 v232, a80\n"
    "v_accvgpr_read_b32 v233, a81\n"
    "v_accvgpr_read_b32 v234, a82\n"
    "v_accvgpr_read_b32 v235, a83\n"
    "v_mul_f32 v232, v232, v250\n"
    "v_mul_f32 v233, v233, v250\n"
    "v_mul_f32 v234, v234, v250\n"
    "v_mul_f32 v235, v235, v250\n"
    "v_accvgpr_write_b32 a80, v232\n"
    "v_accvgpr_write_b32 a81, v233\n"
    "v_accvgpr_write_b32 a82, v234\n"
    "v_accvgpr_write_b32 a83, v235\n"
    "v_accvgpr_read_b32 v232, a96\n"
    "v_accvgpr_read_b32 v233, a97\n"
    "v_accvgpr_read_b32 v234, a98\n"
    "v_accvgpr_read_b32 v235, a99\n"
    "v_mul_f32 v232, v232, v250\n"
    "v_mul_f32 v233, v233, v250\n"
    "v_mul_f32 v234, v234, v250\n"
    "v_mul_f32 v235, v235, v250\n"
    "v_accvgpr_write_b32 a96, v232\n"
    "v_accvgpr_write_b32 a97, v233\n"
    "v_accvgpr_write_b32 a98, v234\n"
    "v_accvgpr_write_b32 a99, v235\n"
    "v_accvgpr_read_b32 v232, a112\n"
    "v_accvgpr_read_b32 v233, a113\n"
    "v_accvgpr_read_b32 v234, a114\n"
    "v_accvgpr_read_b32 v235, a115\n"
    "v_mul_f32 v232, v232, v250\n"
    "v_mul_f32 v233, v233, v250\n"
    "v_mul_f32 v234, v234, v250\n"
    "v_mul_f32 v235, v235, v250\n"
    "v_accvgpr_write_b32 a112, v232\n"
    "v_accvgpr_write_b32 a113, v233\n"
    "v_accvgpr_write_b32 a114, v234\n"
    "v_accvgpr_write_b32 a115, v235\n"
    "v_accvgpr_read_b32 v232, a128\n"
    "v_accvgpr_read_b32 v233, a129\n"
    "v_accvgpr_read_b32 v234, a130\n"
    "v_accvgpr_read_b32 v235, a131\n"
    "v_mul_f32 v232, v232, v250\n"
    "v_mul_f32 v233, v233, v250\n"
    "v_mul_f32 v234, v234, v250\n"
    "v_mul_f32 v235, v235, v250\n"
    "v_accvgpr_write_b32 a128, v232\n"
    "v_accvgpr_write_b32 a129, v233\n"
    "v_accvgpr_write_b32 a130, v234\n"
    "v_accvgpr_write_b32 a131, v235\n"
    "v_accvgpr_read_b32 v232, a144\n"
    "v_accvgpr_read_b32 v233, a145\n"
    "v_accvgpr_read_b32 v234, a146\n"
    "v_accvgpr_read_b32 v235, a147\n"
    "v_mul_f32 v232, v232, v250\n"
    "v_mul_f32 v233, v233, v250\n"
    "v_mul_f32 v234, v234, v250\n"
    "v_mul_f32 v235, v235, v250\n"
    "v_accvgpr_write_b32 a144, v232\n"
    "v_accvgpr_write_b32 a145, v233\n"
    "v_accvgpr_write_b32 a146, v234\n"
    "v_accvgpr_write_b32 a147, v235\n"
    "v_accvgpr_read_b32 v232, a160\n"
    "v_accvgpr_read_b32 v233, a161\n"
    "v_accvgpr_read_b32 v234, a162\n"
    "v_accvgpr_read_b32 v235, a163\n"
    "v_mul_f32 v232, v232, v250\n"
    "v_mul_f32 v233, v233, v250\n"
    "v_mul_f32 v234, v234, v250\n"
    "v_mul_f32 v235, v235, v250\n"
    "v_accvgpr_write_b32 a160, v232\n"
    "v_accvgpr_write_b32 a161, v233\n"
    "v_accvgpr_write_b32 a162, v234\n"
    "v_accvgpr_write_b32 a163, v235\n"
    "v_accvgpr_read_b32 v232, a176\n"
    "v_accvgpr_read_b32 v233, a177\n"
    "v_accvgpr_read_b32 v234, a178\n"
    "v_accvgpr_read_b32 v235, a179\n"
    "v_mul_f32 v232, v232, v250\n"
    "v_mul_f32 v233, v233, v250\n"
    "v_mul_f32 v234, v234, v250\n"
    "v_mul_f32 v235, v235, v250\n"
    "v_accvgpr_write_b32 a176, v232\n"
    "v_accvgpr_write_b32 a177, v233\n"
    "v_accvgpr_write_b32 a178, v234\n"
    "v_accvgpr_write_b32 a179, v235\n"
    "v_accvgpr_read_b32 v232, a192\n"
    "v_accvgpr_read_b32 v233, a193\n"
    "v_accvgpr_read_b32 v234, a194\n"
    "v_accvgpr_read_b32 v235, a195\n"
    "v_mul_f32 v232, v232, v250\n"
    "v_mul_f32 v233, v233, v250\n"
    "v_mul_f32 v234, v234, v250\n"
    "v_mul_f32 v235, v235, v250\n"
    "v_accvgpr_write_b32 a192, v232\n"
    "v_accvgpr_write_b32 a193, v233\n"
    "v_accvgpr_write_b32 a194, v234\n"
    "v_accvgpr_write_b32 a195, v235\n"
    "v_accvgpr_read_b32 v232, a208\n"
    "v_accvgpr_read_b32 v233, a209\n"
    "v_accvgpr_read_b32 v234, a210\n"
    "v_accvgpr_read_b32 v235, a211\n"
    "v_mul_f32 v232, v232, v250\n"
    "v_mul_f32 v233, v233, v250\n"
    "v_mul_f32 v234, v234, v250\n"
    "v_mul_f32 v235, v235, v250\n"
    "v_accvgpr_write_b32 a208, v232\n"
    "v_accvgpr_write_b32 a209, v233\n"
    "v_accvgpr_write_b32 a210, v234\n"
    "v_accvgpr_write_b32 a211, v235\n"
    "v_accvgpr_read_b32 v232, a224\n"
    "v_accvgpr_read_b32 v233, a225\n"
    "v_accvgpr_read_b32 v234, a226\n"
    "v_accvgpr_read_b32 v235, a227\n"
    "v_mul_f32 v232, v232, v250\n"
    "v_mul_f32 v233, v233, v250\n"
    "v_mul_f32 v234, v234, v250\n"
    "v_mul_f32 v235, v235, v250\n"
    "v_accvgpr_write_b32 a224, v232\n"
    "v_accvgpr_write_b32 a225, v233\n"
    "v_accvgpr_write_b32 a226, v234\n"
    "v_accvgpr_write_b32 a227, v235\n"
    "v_accvgpr_read_b32 v232, a240\n"
    "v_accvgpr_read_b32 v233, a241\n"
    "v_accvgpr_read_b32 v234, a242\n"
    "v_accvgpr_read_b32 v235, a243\n"
    "v_mul_f32 v232, v232, v250\n"
    "v_mul_f32 v233, v233, v250\n"
    "v_mul_f32 v234, v234, v250\n"
    "v_mul_f32 v235, v235, v250\n"
    "v_accvgpr_write_b32 a240, v232\n"
    "v_accvgpr_write_b32 a241, v233\n"
    "v_accvgpr_write_b32 a242, v234\n"
    "v_accvgpr_write_b32 a243, v235\n"
    "v_sub_f32 v232, v128, v249\n"
    "v_sub_f32 v233, v129, v249\n"
    "v_sub_f32 v234, v130, v249\n"
    "v_sub_f32 v235, v131, v249\n"
    "v_exp_f32 v232, v232\n"
    "v_exp_f32 v233, v233\n"
    "v_exp_f32 v234, v234\n"
    "v_exp_f32 v235, v235\n"
    "s_nop 0\n"
    "v_add_f32 v236, v232, v233\n"
    "v_add_f32 v236, v236, v234\n"
    "v_add_f32 v236, v236, v235\n"
    "v_cvt_pk_bf16_f32 v176, v232, v233\n"
    "v_cvt_pk_bf16_f32 v177, v234, v235\n"
    "v_sub_f32 v232, v144, v249\n"
    "v_sub_f32 v233, v145, v249\n"
    "v_sub_f32 v234, v146, v249\n"
    "v_sub_f32 v235, v147, v249\n"
    "v_exp_f32 v232, v232\n"
    "v_exp_f32 v233, v233\n"
    "v_exp_f32 v234, v234\n"
    "v_exp_f32 v235, v235\n"
    "s_nop 0\n"
    "v_add_f32 v236, v236, v232\n"
    "v_add_f32 v236, v236, v233\n"
    "v_add_f32 v236, v236, v234\n"
    "v_add_f32 v236, v236, v235\n"
    "v_cvt_pk_bf16_f32 v178, v232, v233\n"
    "v_cvt_pk_bf16_f32 v179, v234, v235\n"
    "v_max3_f32 v249, v132, v133, v134\n"
    "v_max3_f32 v249, v249, v135, v148\n"
    "v_max3_f32 v249, v249, v149, v150\n"
    "v_max_f32 v249, v249, v151\n"
    "v_mov_b32 v232, v249\n"
    "s_nop 1\n"
    "v_permlane32_swap_b32 v249, v232\n"
    "s_nop 1\n"
    "v_max_f32 v249, v249, v232\n"
    "v_mov_b32 v232, v249\n"
    "s_nop 1\n"
    "v_permlane16_swap_b32 v249, v232\n"
    "s_nop 1\n"
    "v_max_f32 v249, v249, v232\n"
    "v_max_f32 v249, 0, v249\n"
    "v_exp_f32 v250, -v249\n"
    "v_sub_f32 v196, v196, v249\n"
    "v_sub_f32 v197, v197, v249\n"
    "v_sub_f32 v198, v198, v249\n"
    "v_sub_f32 v199, v199, v249\n"
    "v_mul_f32 v241, v241, v250\n"
    "v_accvgpr_read_b32 v232, a4\n"
    "v_accvgpr_read_b32 v233, a5\n"
    "v_accvgpr_read_b32 v234, a6\n"
    "v_accvgpr_read_b32 v235, a7\n"
    "v_mul_f32 v232, v232, v250\n"
    "v_mul_f32 v233, v233, v250\n"
    "v_mul_f32 v234, v234, v250\n"
    "v_mul_f32 v235, v235, v250\n"
    "v_accvgpr_write_b32 a4, v232\n"
    "v_accvgpr_write_b32 a5, v233\n"
    "v_accvgpr_write_b32 a6, v234\n"
    "v_accvgpr_write_b32 a7, v235\n"
    "v_accvgpr_read_b32 v232, a20\n"
    "v_accvgpr_read_b32 v233, a21\n"
    "v_accvgpr_read_b32 v234, a22\n"
    "v_accvgpr_read_b32 v235, a23\n"
    "v_mul_f32 v232, v232, v250\n"
    "v_mul_f32 v233, v233, v250\n"
    "v_mul_f32 v234, v234, v250\n"
    "v_mul_f32 v235, v235, v250\n"
    "v_accvgpr_write_b32 a20, v232\n"
    "v_accvgpr_write_b32 a21, v233\n"
    "v_accvgpr_write_b32 a22, v234\n"
    "v_accvgpr_write_b32 a23, v235\n"
    "v_accvgpr_read_b32 v232, a36\n"
    "v_accvgpr_read_b32 v233, a37\n"
    "v_accvgpr_read_b32 v234, a38\n"
    "v_accvgpr_read_b32 v235, a39\n"
    "v_mul_f32 v232, v232, v250\n"
    "v_mul_f32 v233, v233, v250\n"
    "v_mul_f32 v234, v234, v250\n"
    "v_mul_f32 v235, v235, v250\n"
    "v_accvgpr_write_b32 a36, v232\n"
    "v_accvgpr_write_b32 a37, v233\n"
    "v_accvgpr_write_b32 a38, v234\n"
    "v_accvgpr_write_b32 a39, v235\n"
    "v_accvgpr_read_b32 v232, a52\n"
    "v_accvgpr_read_b32 v233, a53\n"
    "v_accvgpr_read_b32 v234, a54\n"
    "v_accvgpr_read_b32 v235, a55\n"
    "v_mul_f32 v232, v232, v250\n"
    "v_mul_f32 v233, v233, v250\n"
    "v_mul_f32 v234, v234, v250\n"
    "v_mul_f32 v235, v235, v250\n"
    "v_accvgpr_write_b32 a52, v232\n"
    "v_accvgpr_write_b32 a53, v233\n"
    "v_accvgpr_write_b32 a54, v234\n"
    "v_accvgpr_write_b32 a55, v235\n"
    "v_accvgpr_read_b32 v232, a68\n"
    "v_accvgpr_read_b32 v233, a69\n"
    "v_accvgpr_read_b32 v234, a70\n"
    "v_accvgpr_read_b32 v235, a71\n"
    "v_mul_f32 v232, v232, v250\n"
    "v_mul_f32 v233, v233, v250\n"
    "v_mul_f32 v234, v234, v250\n"
    "v_mul_f32 v235, v235, v250\n"
    "v_accvgpr_write_b32 a68, v232\n"
    "v_accvgpr_write_b32 a69, v233\n"
    "v_accvgpr_write_b32 a70, v234\n"
    "v_accvgpr_write_b32 a71, v235\n"
    "v_accvgpr_read_b32 v232, a84\n"
    "v_accvgpr_read_b32 v233, a85\n"
    "v_accvgpr_read_b32 v234, a86\n"
    "v_accvgpr_read_b32 v235, a87\n"
    "v_mul_f32 v232, v232, v250\n"
    "v_mul_f32 v233, v233, v250\n"
    "v_mul_f32 v234, v234, v250\n"
    "v_mul_f32 v235, v235, v250\n"
    "v_accvgpr_write_b32 a84, v232\n"
    "v_accvgpr_write_b32 a85, v233\n"
    "v_accvgpr_write_b32 a86, v234\n"
    "v_accvgpr_write_b32 a87, v235\n"
    "v_accvgpr_read_b32 v232, a100\n"
    "v_accvgpr_read_b32 v233, a101\n"
    "v_accvgpr_read_b32 v234, a102\n"
    "v_accvgpr_read_b32 v235, a103\n"
    "v_mul_f32 v232, v232, v250\n"
    "v_mul_f32 v233, v233, v250\n"
    "v_mul_f32 v234, v234, v250\n"
    "v_mul_f32 v235, v235, v250\n"
    "v_accvgpr_write_b32 a100, v232\n"
    "v_accvgpr_write_b32 a101, v233\n"
    "v_accvgpr_write_b32 a102, v234\n"
    "v_accvgpr_write_b32 a103, v235\n"
    "v_accvgpr_read_b32 v232, a116\n"
    "v_accvgpr_read_b32 v233, a117\n"
    "v_accvgpr_read_b32 v234, a118\n"
    "v_accvgpr_read_b32 v235, a119\n"
    "v_mul_f32 v232, v232, v250\n"
    "v_mul_f32 v233, v233, v250\n"
    "v_mul_f32 v234, v234, v250\n"
    "v_mul_f32 v235, v235, v250\n"
    "v_accvgpr_write_b32 a116, v232\n"
    "v_accvgpr_write_b32 a117, v233\n"
    "v_accvgpr_write_b32 a118, v234\n"
    "v_accvgpr_write_b32 a119, v235\n"
    "v_accvgpr_read_b32 v232, a132\n"
    "v_accvgpr_read_b32 v233, a133\n"
    "v_accvgpr_read_b32 v234, a134\n"
    "v_accvgpr_read_b32 v235, a135\n"
    "v_mul_f32 v232, v232, v250\n"
    "v_mul_f32 v233, v233, v250\n"
    "v_mul_f32 v234, v234, v250\n"
    "v_mul_f32 v235, v235, v250\n"
    "v_accvgpr_write_b32 a132, v232\n"
    "v_accvgpr_write_b32 a133, v233\n"
    "v_accvgpr_write_b32 a134, v234\n"
    "v_accvgpr_write_b32 a135, v235\n"
    "v_accvgpr_read_b32 v232, a148\n"
    "v_accvgpr_read_b32 v233, a149\n"
    "v_accvgpr_read_b32 v234, a150\n"
    "v_accvgpr_read_b32 v235, a151\n"
    "v_mul_f32 v232, v232, v250\n"
    "v_mul_f32 v233, v233, v250\n"
    "v_mul_f32 v234, v234, v250\n"
    "v_mul_f32 v235, v235, v250\n"
    "v_accvgpr_write_b32 a148, v232\n"
    "v_accvgpr_write_b32 a149, v233\n"
    "v_accvgpr_write_b32 a150, v234\n"
    "v_accvgpr_write_b32 a151, v235\n"
    "v_accvgpr_read_b32 v232, a164\n"
    "v_accvgpr_read_b32 v233, a165\n"
    "v_accvgpr_read_b32 v234, a166\n"
    "v_accvgpr_read_b32 v235, a167\n"
    "v_mul_f32 v232, v232, v250\n"
    "v_mul_f32 v233, v233, v250\n"
    "v_mul_f32 v234, v234, v250\n"
    "v_mul_f32 v235, v235, v250\n"
    "v_accvgpr_write_b32 a164, v232\n"
    "v_accvgpr_write_b32 a165, v233\n"
    "v_accvgpr_write_b32 a166, v234\n"
    "v_accvgpr_write_b32 a167, v235\n"
    "v_accvgpr_read_b32 v232, a180\n"
    "v_accvgpr_read_b32 v233, a181\n"
    "v_accvgpr_read_b32 v234, a182\n"
    "v_accvgpr_read_b32 v235, a183\n"
    "v_mul_f32 v232, v232, v250\n"
    "v_mul_f32 v233, v233, v250\n"
    "v_mul_f32 v234, v234, v250\n"
    "v_mul_f32 v235, v235, v250\n"
    "v_accvgpr_write_b32 a180, v232\n"
    "v_accvgpr_write_b32 a181, v233\n"
    "v_accvgpr_write_b32 a182, v234\n"
    "v_accvgpr_write_b32 a183, v235\n"
    "v_accvgpr_read_b32 v232, a196\n"
    "v_accvgpr_read_b32 v233, a197\n"
    "v_accvgpr_read_b32 v234, a198\n"
    "v_accvgpr_read_b32 v235, a199\n"
    "v_mul_f32 v232, v232, v250\n"
    "v_mul_f32 v233, v233, v250\n"
    "v_mul_f32 v234, v234, v250\n"
    "v_mul_f32 v235, v235, v250\n"
    "v_accvgpr_write_b32 a196, v232\n"
    "v_accvgpr_write_b32 a197, v233\n"
    "v_accvgpr_write_b32 a198, v234\n"
    "v_accvgpr_write_b32 a199, v235\n"
    "v_accvgpr_read_b32 v232, a212\n"
    "v_accvgpr_read_b32 v233, a213\n"
    "v_accvgpr_read_b32 v234, a214\n"
    "v_accvgpr_read_b32 v235, a215\n"
    "v_mul_f32 v232, v232, v250\n"
    "v_mul_f32 v233, v233, v250\n"
    "v_mul_f32 v234, v234, v250\n"
    "v_mul_f32 v235, v235, v250\n"
    "v_accvgpr_write_b32 a212, v232\n"
    "v_accvgpr_write_b32 a213, v233\n"
    "v_accvgpr_write_b32 a214, v234\n"
    "v_accvgpr_write_b32 a215, v235\n"
    "v_accvgpr_read_b32 v232, a228\n"
    "v_accvgpr_read_b32 v233, a229\n"
    "v_accvgpr_read_b32 v234, a230\n"
    "v_accvgpr_read_b32 v235, a231\n"
    "v_mul_f32 v232, v232, v250\n"
    "v_mul_f32 v233, v233, v250\n"
    "v_mul_f32 v234, v234, v250\n"
    "v_mul_f32 v235, v235, v250\n"
    "v_accvgpr_write_b32 a228, v232\n"
    "v_accvgpr_write_b32 a229, v233\n"
    "v_accvgpr_write_b32 a230, v234\n"
    "v_accvgpr_write_b32 a231, v235\n"
    "v_accvgpr_read_b32 v232, a244\n"
    "v_accvgpr_read_b32 v233, a245\n"
    "v_accvgpr_read_b32 v234, a246\n"
    "v_accvgpr_read_b32 v235, a247\n"
    "v_mul_f32 v232, v232, v250\n"
    "v_mul_f32 v233, v233, v250\n"
    "v_mul_f32 v234, v234, v250\n"
    "v_mul_f32 v235, v235, v250\n"
    "v_accvgpr_write_b32 a244, v232\n"
    "v_accvgpr_write_b32 a245, v233\n"
    "v_accvgpr_write_b32 a246, v234\n"
    "v_accvgpr_write_b32 a247, v235\n"
    "v_sub_f32 v232, v132, v249\n"
    "v_sub_f32 v233, v133, v249\n"
    "v_sub_f32 v234, v134, v249\n"
    "v_sub_f32 v235, v135, v249\n"
    "v_exp_f32 v232, v232\n"
    "v_exp_f32 v233, v233\n"
    "v_exp_f32 v234, v234\n"
    "v_exp_f32 v235, v235\n"
    "s_nop 0\n"
    "v_add_f32 v237, v232, v233\n"
    "v_add_f32 v237, v237, v234\n"
    "v_add_f32 v237, v237, v235\n"
    "v_cvt_pk_bf16_f32 v180, v232, v233\n"
    "v_cvt_pk_bf16_f32 v181, v234, v235\n"
    "v_sub_f32 v232, v148, v249\n"
    "v_sub_f32 v233, v149, v249\n"
    "v_sub_f32 v234, v150, v249\n"
    "v_sub_f32 v235, v151, v249\n"
    "v_exp_f32 v232, v232\n"
    "v_exp_f32 v233, v233\n"
    "v_exp_f32 v234, v234\n"
    "v_exp_f32 v235, v235\n"
    "s_nop 0\n"
    "v_add_f32 v237, v237, v232\n"
    "v_add_f32 v237, v237, v233\n"
    "v_add_f32 v237, v237, v234\n"
    "v_add_f32 v237, v237, v235\n"
    "v_cvt_pk_bf16_f32 v182, v232, v233\n"
    "v_cvt_pk_bf16_f32 v183, v234, v235\n"
    "v_max3_f32 v249, v136, v137, v138\n"
    "v_max3_f32 v249, v249, v139, v152\n"
    "v_max3_f32 v249, v249, v153, v154\n"
    "v_max_f32 v249, v249, v155\n"
    "v_mov_b32 v232, v249\n"
    "s_nop 1\n"
    "v_permlane32_swap_b32 v249, v232\n"
    "s_nop 1\n"
    "v_max_f32 v249, v249, v232\n"
    "v_mov_b32 v232, v249\n"
    "s_nop 1\n"
    "v_permlane16_swap_b32 v249, v232\n"
    "s_nop 1\n"
    "v_max_f32 v249, v249, v232\n"
    "v_max_f32 v249, 0, v249\n"
    "v_exp_f32 v250, -v249\n"
    "v_sub_f32 v200, v200, v249\n"
    "v_sub_f32 v201, v201, v249\n"
    "v_sub_f32 v202, v202, v249\n"
    "v_sub_f32 v203, v203, v249\n"
    "v_mul_f32 v242, v242, v250\n"
    "v_accvgpr_read_b32 v232, a8\n"
    "v_accvgpr_read_b32 v233, a9\n"
    "v_accvgpr_read_b32 v234, a10\n"
    "v_accvgpr_read_b32 v235, a11\n"
    "v_mul_f32 v232, v232, v250\n"
    "v_mul_f32 v233, v233, v250\n"
    "v_mul_f32 v234, v234, v250\n"
    "v_mul_f32 v235, v235, v250\n"
    "v_accvgpr_write_b32 a8, v232\n"
    "v_accvgpr_write_b32 a9, v233\n"
    "v_accvgpr_write_b32 a10, v234\n"
    "v_accvgpr_write_b32 a11, v235\n"
    "v_accvgpr_read_b32 v232, a24\n"
    "v_accvgpr_read_b32 v233, a25\n"
    "v_accvgpr_read_b32 v234, a26\n"
    "v_accvgpr_read_b32 v235, a27\n"
    "v_mul_f32 v232, v232, v250\n"
    "v_mul_f32 v233, v233, v250\n"
    "v_mul_f32 v234, v234, v250\n"
    "v_mul_f32 v235, v235, v250\n"
    "v_accvgpr_write_b32 a24, v232\n"
    "v_accvgpr_write_b32 a25, v233\n"
    "v_accvgpr_write_b32 a26, v234\n"
    "v_accvgpr_write_b32 a27, v235\n"
    "v_accvgpr_read_b32 v232, a40\n"
    "v_accvgpr_read_b32 v233, a41\n"
    "v_accvgpr_read_b32 v234, a42\n"
    "v_accvgpr_read_b32 v235, a43\n"
    "v_mul_f32 v232, v232, v250\n"
    "v_mul_f32 v233, v233, v250\n"
    "v_mul_f32 v234, v234, v250\n"
    "v_mul_f32 v235, v235, v250\n"
    "v_accvgpr_write_b32 a40, v232\n"
    "v_accvgpr_write_b32 a41, v233\n"
    "v_accvgpr_write_b32 a42, v234\n"
    "v_accvgpr_write_b32 a43, v235\n"
    "v_accvgpr_read_b32 v232, a56\n"
    "v_accvgpr_read_b32 v233, a57\n"
    "v_accvgpr_read_b32 v234, a58\n"
    "v_accvgpr_read_b32 v235, a59\n"
    "v_mul_f32 v232, v232, v250\n"
    "v_mul_f32 v233, v233, v250\n"
    "v_mul_f32 v234, v234, v250\n"
    "v_mul_f32 v235, v235, v250\n"
    "v_accvgpr_write_b32 a56, v232\n"
    "v_accvgpr_write_b32 a57, v233\n"
    "v_accvgpr_write_b32 a58, v234\n"
    "v_accvgpr_write_b32 a59, v235\n"
    "v_accvgpr_read_b32 v232, a72\n"
    "v_accvgpr_read_b32 v233, a73\n"
    "v_accvgpr_read_b32 v234, a74\n"
    "v_accvgpr_read_b32 v235, a75\n"
    "v_mul_f32 v232, v232, v250\n"
    "v_mul_f32 v233, v233, v250\n"
    "v_mul_f32 v234, v234, v250\n"
    "v_mul_f32 v235, v235, v250\n"
    "v_accvgpr_write_b32 a72, v232\n"
    "v_accvgpr_write_b32 a73, v233\n"
    "v_accvgpr_write_b32 a74, v234\n"
    "v_accvgpr_write_b32 a75, v235\n"
    "v_accvgpr_read_b32 v232, a88\n"
    "v_accvgpr_read_b32 v233, a89\n"
    "v_accvgpr_read_b32 v234, a90\n"
    "v_accvgpr_read_b32 v235, a91\n"
    "v_mul_f32 v232, v232, v250\n"
    "v_mul_f32 v233, v233, v250\n"
    "v_mul_f32 v234, v234, v250\n"
    "v_mul_f32 v235, v235, v250\n"
    "v_accvgpr_write_b32 a88, v232\n"
    "v_accvgpr_write_b32 a89, v233\n"
    "v_accvgpr_write_b32 a90, v234\n"
    "v_accvgpr_write_b32 a91, v235\n"
    "v_accvgpr_read_b32 v232, a104\n"
    "v_accvgpr_read_b32 v233, a105\n"
    "v_accvgpr_read_b32 v234, a106\n"
    "v_accvgpr_read_b32 v235, a107\n"
    "v_mul_f32 v232, v232, v250\n"
    "v_mul_f32 v233, v233, v250\n"
    "v_mul_f32 v234, v234, v250\n"
    "v_mul_f32 v235, v235, v250\n"
    "v_accvgpr_write_b32 a104, v232\n"
    "v_accvgpr_write_b32 a105, v233\n"
    "v_accvgpr_write_b32 a106, v234\n"
    "v_accvgpr_write_b32 a107, v235\n"
    "v_accvgpr_read_b32 v232, a120\n"
    "v_accvgpr_read_b32 v233, a121\n"
    "v_accvgpr_read_b32 v234, a122\n"
    "v_accvgpr_read_b32 v235, a123\n"
    "v_mul_f32 v232, v232, v250\n"
    "v_mul_f32 v233, v233, v250\n"
    "v_mul_f32 v234, v234, v250\n"
    "v_mul_f32 v235, v235, v250\n"
    "v_accvgpr_write_b32 a120, v232\n"
    "v_accvgpr_write_b32 a121, v233\n"
    "v_accvgpr_write_b32 a122, v234\n"
    "v_accvgpr_write_b32 a123, v235\n"
    "v_accvgpr_read_b32 v232, a136\n"
    "v_accvgpr_read_b32 v233, a137\n"
    "v_accvgpr_read_b32 v234, a138\n"
    "v_accvgpr_read_b32 v235, a139\n"
    "v_mul_f32 v232, v232, v250\n"
    "v_mul_f32 v233, v233, v250\n"
    "v_mul_f32 v234, v234, v250\n"
    "v_mul_f32 v235, v235, v250\n"
    "v_accvgpr_write_b32 a136, v232\n"
    "v_accvgpr_write_b32 a137, v233\n"
    "v_accvgpr_write_b32 a138, v234\n"
    "v_accvgpr_write_b32 a139, v235\n"
    "v_accvgpr_read_b32 v232, a152\n"
    "v_accvgpr_read_b32 v233, a153\n"
    "v_accvgpr_read_b32 v234, a154\n"
    "v_accvgpr_read_b32 v235, a155\n"
    "v_mul_f32 v232, v232, v250\n"
    "v_mul_f32 v233, v233, v250\n"
    "v_mul_f32 v234, v234, v250\n"
    "v_mul_f32 v235, v235, v250\n"
    "v_accvgpr_write_b32 a152, v232\n"
    "v_accvgpr_write_b32 a153, v233\n"
    "v_accvgpr_write_b32 a154, v234\n"
    "v_accvgpr_write_b32 a155, v235\n"
    "v_accvgpr_read_b32 v232, a168\n"
    "v_accvgpr_read_b32 v233, a169\n"
    "v_accvgpr_read_b32 v234, a170\n"
    "v_accvgpr_read_b32 v235, a171\n"
    "v_mul_f32 v232, v232, v250\n"
    "v_mul_f32 v233, v233, v250\n"
    "v_mul_f32 v234, v234, v250\n"
    "v_mul_f32 v235, v235, v250\n"
    "v_accvgpr_write_b32 a168, v232\n"
    "v_accvgpr_write_b32 a169, v233\n"
    "v_accvgpr_write_b32 a170, v234\n"
    "v_accvgpr_write_b32 a171, v235\n"
    "v_accvgpr_read_b32 v232, a184\n"
    "v_accvgpr_read_b32 v233, a185\n"
    "v_accvgpr_read_b32 v234, a186\n"
    "v_accvgpr_read_b32 v235, a187\n"
    "v_mul_f32 v232, v232, v250\n"
    "v_mul_f32 v233, v233, v250\n"
    "v_mul_f32 v234, v234, v250\n"
    "v_mul_f32 v235, v235, v250\n"
    "v_accvgpr_write_b32 a184, v232\n"
    "v_accvgpr_write_b32 a185, v233\n"
    "v_accvgpr_write_b32 a186, v234\n"
    "v_accvgpr_write_b32 a187, v235\n"
    "v_accvgpr_read_b32 v232, a200\n"
    "v_accvgpr_read_b32 v233, a201\n"
    "v_accvgpr_read_b32 v234, a202\n"
    "v_accvgpr_read_b32 v235, a203\n"
    "v_mul_f32 v232, v232, v250\n"
    "v_mul_f32 v233, v233, v250\n"
    "v_mul_f32 v234, v234, v250\n"
    "v_mul_f32 v235, v235, v250\n"
    "v_accvgpr_write_b32 a200, v232\n"
    "v_accvgpr_write_b32 a201, v233\n"
    "v_accvgpr_write_b32 a202, v234\n"
    "v_accvgpr_write_b32 a203, v235\n"
    "v_accvgpr_read_b32 v232, a216\n"
    "v_accvgpr_read_b32 v233, a217\n"
    "v_accvgpr_read_b32 v234, a218\n"
    "v_accvgpr_read_b32 v235, a219\n"
    "v_mul_f32 v232, v232, v250\n"
    "v_mul_f32 v233, v233, v250\n"
    "v_mul_f32 v234, v234, v250\n"
    "v_mul_f32 v235, v235, v250\n"
    "v_accvgpr_write_b32 a216, v232\n"
    "v_accvgpr_write_b32 a217, v233\n"
    "v_accvgpr_write_b32 a218, v234\n"
    "v_accvgpr_write_b32 a219, v235\n"
    "v_accvgpr_read_b32 v232, a232\n"
    "v_accvgpr_read_b32 v233, a233\n"
    "v_accvgpr_read_b32 v234, a234\n"
    "v_accvgpr_read_b32 v235, a235\n"
    "v_mul_f32 v232, v232, v250\n"
    "v_mul_f32 v233, v233, v250\n"
    "v_mul_f32 v234, v234, v250\n"
    "v_mul_f32 v235, v235, v250\n"
    "v_accvgpr_write_b32 a232, v232\n"
    "v_accvgpr_write_b32 a233, v233\n"
    "v_accvgpr_write_b32 a234, v234\n"
    "v_accvgpr_write_b32 a235, v235\n"
    "v_accvgpr_read_b32 v232, a248\n"
    "v_accvgpr_read_b32 v233, a249\n"
    "v_accvgpr_read_b32 v234, a250\n"
    "v_accvgpr_read_b32 v235, a251\n"
    "v_mul_f32 v232, v232, v250\n"
    "v_mul_f32 v233, v233, v250\n"
    "v_mul_f32 v234, v234, v250\n"
    "v_mul_f32 v235, v235, v250\n"
    "v_accvgpr_write_b32 a248, v232\n"
    "v_accvgpr_write_b32 a249, v233\n"
    "v_accvgpr_write_b32 a250, v234\n"
    "v_accvgpr_write_b32 a251, v235\n"
    "v_sub_f32 v232, v136, v249\n"
    "v_sub_f32 v233, v137, v249\n"
    "v_sub_f32 v234, v138, v249\n"
    "v_sub_f32 v235, v139, v249\n"
    "v_exp_f32 v232, v232\n"
    "v_exp_f32 v233, v233\n"
    "v_exp_f32 v234, v234\n"
    "v_exp_f32 v235, v235\n"
    "s_nop 0\n"
    "v_add_f32 v238, v232, v233\n"
    "v_add_f32 v238, v238, v234\n"
    "v_add_f32 v238, v238, v235\n"
    "v_cvt_pk_bf16_f32 v184, v232, v233\n"
    "v_cvt_pk_bf16_f32 v185, v234, v235\n"
    "v_sub_f32 v232, v152, v249\n"
    "v_sub_f32 v233, v153, v249\n"
    "v_sub_f32 v234, v154, v249\n"
    "v_sub_f32 v235, v155, v249\n"
    "v_exp_f32 v232, v232\n"
    "v_exp_f32 v233, v233\n"
    "v_exp_f32 v234, v234\n"
    "v_exp_f32 v235, v235\n"
    "s_nop 0\n"
    "v_add_f32 v238, v238, v232\n"
    "v_add_f32 v238, v238, v233\n"
    "v_add_f32 v238, v238, v234\n"
    "v_add_f32 v238, v238, v235\n"
    "v_cvt_pk_bf16_f32 v186, v232, v233\n"
    "v_cvt_pk_bf16_f32 v187, v234, v235\n"
    "v_max3_f32 v249, v140, v141, v142\n"
    "v_max3_f32 v249, v249, v143, v156\n"
    "v_max3_f32 v249, v249, v157, v158\n"
    "v_max_f32 v249, v249, v159\n"
    "v_mov_b32 v232, v249\n"
    "s_nop 1\n"
    "v_permlane32_swap_b32 v249, v232\n"
    "s_nop 1\n"
    "v_max_f32 v249, v249, v232\n"
    "v_mov_b32 v232, v249\n"
    "s_nop 1\n"
    "v_permlane16_swap_b32 v249, v232\n"
    "s_nop 1\n"
    "v_max_f32 v249, v249, v232\n"
    "v_max_f32 v249, 0, v249\n"
    "v_exp_f32 v250, -v249\n"
    "v_sub_f32 v204, v204, v249\n"
    "v_sub_f32 v205, v205, v249\n"
    "v_sub_f32 v206, v206, v249\n"
    "v_sub_f32 v207, v207, v249\n"
    "v_mul_f32 v243, v243, v250\n"
    "v_accvgpr_read_b32 v232, a12\n"
    "v_accvgpr_read_b32 v233, a13\n"
    "v_accvgpr_read_b32 v234, a14\n"
    "v_accvgpr_read_b32 v235, a15\n"
    "v_mul_f32 v232, v232, v250\n"
    "v_mul_f32 v233, v233, v250\n"
    "v_mul_f32 v234, v234, v250\n"
    "v_mul_f32 v235, v235, v250\n"
    "v_accvgpr_write_b32 a12, v232\n"
    "v_accvgpr_write_b32 a13, v233\n"
    "v_accvgpr_write_b32 a14, v234\n"
    "v_accvgpr_write_b32 a15, v235\n"
    "v_accvgpr_read_b32 v232, a28\n"
    "v_accvgpr_read_b32 v233, a29\n"
    "v_accvgpr_read_b32 v234, a30\n"
    "v_accvgpr_read_b32 v235, a31\n"
    "v_mul_f32 v232, v232, v250\n"
    "v_mul_f32 v233, v233, v250\n"
    "v_mul_f32 v234, v234, v250\n"
    "v_mul_f32 v235, v235, v250\n"
    "v_accvgpr_write_b32 a28, v232\n"
    "v_accvgpr_write_b32 a29, v233\n"
    "v_accvgpr_write_b32 a30, v234\n"
    "v_accvgpr_write_b32 a31, v235\n"
    "v_accvgpr_read_b32 v232, a44\n"
    "v_accvgpr_read_b32 v233, a45\n"
    "v_accvgpr_read_b32 v234, a46\n"
    "v_accvgpr_read_b32 v235, a47\n"
    "v_mul_f32 v232, v232, v250\n"
    "v_mul_f32 v233, v233, v250\n"
    "v_mul_f32 v234, v234, v250\n"
    "v_mul_f32 v235, v235, v250\n"
    "v_accvgpr_write_b32 a44, v232\n"
    "v_accvgpr_write_b32 a45, v233\n"
    "v_accvgpr_write_b32 a46, v234\n"
    "v_accvgpr_write_b32 a47, v235\n"
    "v_accvgpr_read_b32 v232, a60\n"
    "v_accvgpr_read_b32 v233, a61\n"
    "v_accvgpr_read_b32 v234, a62\n"
    "v_accvgpr_read_b32 v235, a63\n"
    "v_mul_f32 v232, v232, v250\n"
    "v_mul_f32 v233, v233, v250\n"
    "v_mul_f32 v234, v234, v250\n"
    "v_mul_f32 v235, v235, v250\n"
    "v_accvgpr_write_b32 a60, v232\n"
    "v_accvgpr_write_b32 a61, v233\n"
    "v_accvgpr_write_b32 a62, v234\n"
    "v_accvgpr_write_b32 a63, v235\n"
    "v_accvgpr_read_b32 v232, a76\n"
    "v_accvgpr_read_b32 v233, a77\n"
    "v_accvgpr_read_b32 v234, a78\n"
    "v_accvgpr_read_b32 v235, a79\n"
    "v_mul_f32 v232, v232, v250\n"
    "v_mul_f32 v233, v233, v250\n"
    "v_mul_f32 v234, v234, v250\n"
    "v_mul_f32 v235, v235, v250\n"
    "v_accvgpr_write_b32 a76, v232\n"
    "v_accvgpr_write_b32 a77, v233\n"
    "v_accvgpr_write_b32 a78, v234\n"
    "v_accvgpr_write_b32 a79, v235\n"
    "v_accvgpr_read_b32 v232, a92\n"
    "v_accvgpr_read_b32 v233, a93\n"
    "v_accvgpr_read_b32 v234, a94\n"
    "v_accvgpr_read_b32 v235, a95\n"
    "v_mul_f32 v232, v232, v250\n"
    "v_mul_f32 v233, v233, v250\n"
    "v_mul_f32 v234, v234, v250\n"
    "v_mul_f32 v235, v235, v250\n"
    "v_accvgpr_write_b32 a92, v232\n"
    "v_accvgpr_write_b32 a93, v233\n"
    "v_accvgpr_write_b32 a94, v234\n"
    "v_accvgpr_write_b32 a95, v235\n"
    "v_accvgpr_read_b32 v232, a108\n"
    "v_accvgpr_read_b32 v233, a109\n"
    "v_accvgpr_read_b32 v234, a110\n"
    "v_accvgpr_read_b32 v235, a111\n"
    "v_mul_f32 v232, v232, v250\n"
    "v_mul_f32 v233, v233, v250\n"
    "v_mul_f32 v234, v234, v250\n"
    "v_mul_f32 v235, v235, v250\n"
    "v_accvgpr_write_b32 a108, v232\n"
    "v_accvgpr_write_b32 a109, v233\n"
    "v_accvgpr_write_b32 a110, v234\n"
    "v_accvgpr_write_b32 a111, v235\n"
    "v_accvgpr_read_b32 v232, a124\n"
    "v_accvgpr_read_b32 v233, a125\n"
    "v_accvgpr_read_b32 v234, a126\n"
    "v_accvgpr_read_b32 v235, a127\n"
    "v_mul_f32 v232, v232, v250\n"
    "v_mul_f32 v233, v233, v250\n"
    "v_mul_f32 v234, v234, v250\n"
    "v_mul_f32 v235, v235, v250\n"
    "v_accvgpr_write_b32 a124, v232\n"
    "v_accvgpr_write_b32 a125, v233\n"
    "v_accvgpr_write_b32 a126, v234\n"
    "v_accvgpr_write_b32 a127, v235\n"
    "v_accvgpr_read_b32 v232, a140\n"
    "v_accvgpr_read_b32 v233, a141\n"
    "v_accvgpr_read_b32 v234, a142\n"
    "v_accvgpr_read_b32 v235, a143\n"
    "v_mul_f32 v232, v232, v250\n"
    "v_mul_f32 v233, v233, v250\n"
    "v_mul_f32 v234, v234, v250\n"
    "v_mul_f32 v235, v235, v250\n"
    "v_accvgpr_write_b32 a140, v232\n"
    "v_accvgpr_write_b32 a141, v233\n"
    "v_accvgpr_write_b32 a142, v234\n"
    "v_accvgpr_write_b32 a143, v235\n"
    "v_accvgpr_read_b32 v232, a156\n"
    "v_accvgpr_read_b32 v233, a157\n"
    "v_accvgpr_read_b32 v234, a158\n"
    "v_accvgpr_read_b32 v235, a159\n"
    "v_mul_f32 v232, v232, v250\n"
    "v_mul_f32 v233, v233, v250\n"
    "v_mul_f32 v234, v234, v250\n"
    "v_mul_f32 v235, v235, v250\n"
    "v_accvgpr_write_b32 a156, v232\n"
    "v_accvgpr_write_b32 a157, v233\n"
    "v_accvgpr_write_b32 a158, v234\n"
    "v_accvgpr_write_b32 a159, v235\n"
    "v_accvgpr_read_b32 v232, a172\n"
    "v_accvgpr_read_b32 v233, a173\n"
    "v_accvgpr_read_b32 v234, a174\n"
    "v_accvgpr_read_b32 v235, a175\n"
    "v_mul_f32 v232, v232, v250\n"
    "v_mul_f32 v233, v233, v250\n"
    "v_mul_f32 v234, v234, v250\n"
    "v_mul_f32 v235, v235, v250\n"
    "v_accvgpr_write_b32 a172, v232\n"
    "v_accvgpr_write_b32 a173, v233\n"
    "v_accvgpr_write_b32 a174, v234\n"
    "v_accvgpr_write_b32 a175, v235\n"
    "v_accvgpr_read_b32 v232, a188\n"
    "v_accvgpr_read_b32 v233, a189\n"
    "v_accvgpr_read_b32 v234, a190\n"
    "v_accvgpr_read_b32 v235, a191\n"
    "v_mul_f32 v232, v232, v250\n"
    "v_mul_f32 v233, v233, v250\n"
    "v_mul_f32 v234, v234, v250\n"
    "v_mul_f32 v235, v235, v250\n"
    "v_accvgpr_write_b32 a188, v232\n"
    "v_accvgpr_write_b32 a189, v233\n"
    "v_accvgpr_write_b32 a190, v234\n"
    "v_accvgpr_write_b32 a191, v235\n"
    "v_accvgpr_read_b32 v232, a204\n"
    "v_accvgpr_read_b32 v233, a205\n"
    "v_accvgpr_read_b32 v234, a206\n"
    "v_accvgpr_read_b32 v235, a207\n"
    "v_mul_f32 v232, v232, v250\n"
    "v_mul_f32 v233, v233, v250\n"
    "v_mul_f32 v234, v234, v250\n"
    "v_mul_f32 v235, v235, v250\n"
    "v_accvgpr_write_b32 a204, v232\n"
    "v_accvgpr_write_b32 a205, v233\n"
    "v_accvgpr_write_b32 a206, v234\n"
    "v_accvgpr_write_b32 a207, v235\n"
    "v_accvgpr_read_b32 v232, a220\n"
    "v_accvgpr_read_b32 v233, a221\n"
    "v_accvgpr_read_b32 v234, a222\n"
    "v_accvgpr_read_b32 v235, a223\n"
    "v_mul_f32 v232, v232, v250\n"
    "v_mul_f32 v233, v233, v250\n"
    "v_mul_f32 v234, v234, v250\n"
    "v_mul_f32 v235, v235, v250\n"
    "v_accvgpr_write_b32 a220, v232\n"
    "v_accvgpr_write_b32 a221, v233\n"
    "v_accvgpr_write_b32 a222, v234\n"
    "v_accvgpr_write_b32 a223, v235\n"
    "v_accvgpr_read_b32 v232, a236\n"
    "v_accvgpr_read_b32 v233, a237\n"
    "v_accvgpr_read_b32 v234, a238\n"
    "v_accvgpr_read_b32 v235, a239\n"
    "v_mul_f32 v232, v232, v250\n"
    "v_mul_f32 v233, v233, v250\n"
    "v_mul_f32 v234, v234, v250\n"
    "v_mul_f32 v235, v235, v250\n"
    "v_accvgpr_write_b32 a236, v232\n"
    "v_accvgpr_write_b32 a237, v233\n"
    "v_accvgpr_write_b32 a238, v234\n"
    "v_accvgpr_write_b32 a239, v235\n"
    "v_accvgpr_read_b32 v232, a252\n"
    "v_accvgpr_read_b32 v233, a253\n"
    "v_accvgpr_read_b32 v234, a254\n"
    "v_accvgpr_read_b32 v235, a255\n"
    "v_mul_f32 v232, v232, v250\n"
    "v_mul_f32 v233, v233, v250\n"
    "v_mul_f32 v234, v234, v250\n"
    "v_mul_f32 v235, v235, v250\n"
    "v_accvgpr_write_b32 a252, v232\n"
    "v_accvgpr_write_b32 a253, v233\n"
    "v_accvgpr_write_b32 a254, v234\n"
    "v_accvgpr_write_b32 a255, v235\n"
    "v_sub_f32 v232, v140, v249\n"
    "v_sub_f32 v233, v141, v249\n"
    "v_sub_f32 v234, v142, v249\n"
    "v_sub_f32 v235, v143, v249\n"
    "v_exp_f32 v232, v232\n"
    "v_exp_f32 v233, v233\n"
    "v_exp_f32 v234, v234\n"
    "v_exp_f32 v235, v235\n"
    "s_nop 0\n"
    "v_add_f32 v239, v232, v233\n"
    "v_add_f32 v239, v239, v234\n"
    "v_add_f32 v239, v239, v235\n"
    "v_cvt_pk_bf16_f32 v188, v232, v233\n"
    "v_cvt_pk_bf16_f32 v189, v234, v235\n"
    "v_sub_f32 v232, v156, v249\n"
    "v_sub_f32 v233, v157, v249\n"
    "v_sub_f32 v234, v158, v249\n"
    "v_sub_f32 v235, v159, v249\n"
    "v_exp_f32 v232, v232\n"
    "v_exp_f32 v233, v233\n"
    "v_exp_f32 v234, v234\n"
    "v_exp_f32 v235, v235\n"
    "s_nop 0\n"
    "v_add_f32 v239, v239, v232\n"
    "v_add_f32 v239, v239, v233\n"
    "v_add_f32 v239, v239, v234\n"
    "v_add_f32 v239, v239, v235\n"
    "v_cvt_pk_bf16_f32 v190, v232, v233\n"
    "v_cvt_pk_bf16_f32 v191, v234, v235\n"
    "s_nop 3\n"
    "s_cmp_eq_u32 s59, 0\n"
    "s_cbranch_scc1 RET1z_m\n"
    "s_cmp_eq_u32 s59, 1\n"
    "s_cbranch_scc1 RET1_m\n"
    "s_branch RET1l_m\n"
    "STUB0_m:\n"
    "s_nop 15\n"
    "v_max3_f32 v249, v128, v129, v130\n"
    "v_max3_f32 v249, v249, v131, v144\n"
    "v_max3_f32 v249, v249, v145, v146\n"
    "v_max_f32 v249, v249, v147\n"
    "v_mov_b32 v232, v249\n"
    "s_nop 1\n"
    "v_permlane32_swap_b32 v249, v232\n"
    "s_nop 1\n"
    "v_max_f32 v249, v249, v232\n"
    "v_mov_b32 v232, v249\n"
    "s_nop 1\n"
    "v_permlane16_swap_b32 v249, v232\n"
    "s_nop 1\n"
    "v_max_f32 v249, v249, v232\n"
    "v_max_f32 v249, 0, v249\n"
    "v_exp_f32 v250, -v249\n"
    "v_sub_f32 v192, v192, v249\n"
    "v_sub_f32 v193, v193, v249\n"
    "v_sub_f32 v194, v194, v249\n"
    "v_sub_f32 v195, v195, v249\n"
    "v_mul_f32 v240, v240, v250\n"
    "v_accvgpr_read_b32 v232, a0\n"
    "v_accvgpr_read_b32 v233, a1\n"
    "v_accvgpr_read_b32 v234, a2\n"
    "v_accvgpr_read_b32 v235, a3\n"
    "v_mul_f32 v232, v232, v250\n"
    "v_mul_f32 v233, v233, v250\n"
    "v_mul_f32 v234, v234, v250\n"
    "v_mul_f32 v235, v235, v250\n"
    "v_accvgpr_write_b32 a0, v232\n"
    "v_accvgpr_write_b32 a1, v233\n"
    "v_accvgpr_write_b32 a2, v234\n"
    "v_accvgpr_write_b32 a3, v235\n"
    "v_accvgpr_read_b32 v232, a16\n"
    "v_accvgpr_read_b32 v233, a17\n"
    "v_accvgpr_read_b32 v234, a18\n"
    "v_accvgpr_read_b32 v235, a19\n"
    "v_mul_f32 v232, v232, v250\n"
    "v_mul_f32 v233, v233, v250\n"
    "v_mul_f32 v234, v234, v250\n"
    "v_mul_f32 v235, v235, v250\n"
    "v_accvgpr_write_b32 a16, v232\n"
    "v_accvgpr_write_b32 a17, v233\n"
    "v_accvgpr_write_b32 a18, v234\n"
    "v_accvgpr_write_b32 a19, v235\n"
    "v_accvgpr_read_b32 v232, a32\n"
    "v_accvgpr_read_b32 v233, a33\n"
    "v_accvgpr_read_b32 v234, a34\n"
    "v_accvgpr_read_b32 v235, a35\n"
    "v_mul_f32 v232, v232, v250\n"
    "v_mul_f32 v233, v233, v250\n"
    "v_mul_f32 v234, v234, v250\n"
    "v_mul_f32 v235, v235, v250\n"
    "v_accvgpr_write_b32 a32, v232\n"
    "v_accvgpr_write_b32 a33, v233\n"
    "v_accvgpr_write_b32 a34, v234\n"
    "v_accvgpr_write_b32 a35, v235\n"
    "v_accvgpr_read_b32 v232, a48\n"
    "v_accvgpr_read_b32 v233, a49\n"
    "v_accvgpr_read_b32 v234, a50\n"
    "v_accvgpr_read_b32 v235, a51\n"
    "v_mul_f32 v232, v232, v250\n"
    "v_mul_f32 v233, v233, v250\n"
    "v_mul_f32 v234, v234, v250\n"
    "v_mul_f32 v235, v235, v250\n"
    "v_accvgpr_write_b32 a48, v232\n"
    "v_accvgpr_write_b32 a49, v233\n"
    "v_accvgpr_write_b32 a50, v234\n"
    "v_accvgpr_write_b32 a51, v235\n"
    "v_accvgpr_read_b32 v232, a64\n"
    "v_accvgpr_read_b32 v233, a65\n"
    "v_accvgpr_read_b32 v234, a66\n"
    "v_accvgpr_read_b32 v235, a67\n"
    "v_mul_f32 v232, v232, v250\n"
    "v_mul_f32 v233, v233, v250\n"
    "v_mul_f32 v234, v234, v250\n"
    "v_mul_f32 v235, v235, v250\n"
    "v_accvgpr_write_b32 a64, v232\n"
    "v_accvgpr_write_b32 a65, v233\n"
    "v_accvgpr_write_b32 a66, v234\n"
    "v_accvgpr_write_b32 a67, v235\n"
    "v_accvgpr_read_b32 v232, a80\n"
    "v_accvgpr_read_b32 v233, a81\n"
    "v_accvgpr_read_b32 v234, a82\n"
    "v_accvgpr_read_b32 v235, a83\n"
    "v_mul_f32 v232, v232, v250\n"
    "v_mul_f32 v233, v233, v250\n"
    "v_mul_f32 v234, v234, v250\n"
    "v_mul_f32 v235, v235, v250\n"
    "v_accvgpr_write_b32 a80, v232\n"
    "v_accvgpr_write_b32 a81, v233\n"
    "v_accvgpr_write_b32 a82, v234\n"
    "v_accvgpr_write_b32 a83, v235\n"
    "v_accvgpr_read_b32 v232, a96\n"
    "v_accvgpr_read_b32 v233, a97\n"
    "v_accvgpr_read_b32 v234, a98\n"
    "v_accvgpr_read_b32 v235, a99\n"
    "v_mul_f32 v232, v232, v250\n"
    "v_mul_f32 v233, v233, v250\n"
    "v_mul_f32 v234, v234, v250\n"
    "v_mul_f32 v235, v235, v250\n"
    "v_accvgpr_write_b32 a96, v232\n"
    "v_accvgpr_write_b32 a97, v233\n"
    "v_accvgpr_write_b32 a98, v234\n"
    "v_accvgpr_write_b32 a99, v235\n"
    "v_accvgpr_read_b32 v232, a112\n"
    "v_accvgpr_read_b32 v233, a113\n"
    "v_accvgpr_read_b32 v234, a114\n"
    "v_accvgpr_read_b32 v235, a115\n"
    "v_mul_f32 v232, v232, v250\n"
    "v_mul_f32 v233, v233, v250\n"
    "v_mul_f32 v234, v234, v250\n"
    "v_mul_f32 v235, v235, v250\n"
    "v_accvgpr_write_b32 a112, v232\n"
    "v_accvgpr_write_b32 a113, v233\n"
    "v_accvgpr_write_b32 a114, v234\n"
    "v_accvgpr_write_b32 a115, v235\n"
    "v_accvgpr_read_b32 v232, a128\n"
    "v_accvgpr_read_b32 v233, a129\n"
    "v_accvgpr_read_b32 v234, a130\n"
    "v_accvgpr_read_b32 v235, a131\n"
    "v_mul_f32 v232, v232, v250\n"
    "v_mul_f32 v233, v233, v250\n"
    "v_mul_f32 v234, v234, v250\n"
    "v_mul_f32 v235, v235, v250\n"
    "v_accvgpr_write_b32 a128, v232\n"
    "v_accvgpr_write_b32 a129, v233\n"
    "v_accvgpr_write_b32 a130, v234\n"
    "v_accvgpr_write_b32 a131, v235\n"
    "v_accvgpr_read_b32 v232, a144\n"
    "v_accvgpr_read_b32 v233, a145\n"
    "v_accvgpr_read_b32 v234, a146\n"
    "v_accvgpr_read_b32 v235, a147\n"
    "v_mul_f32 v232, v232, v250\n"
    "v_mul_f32 v233, v233, v250\n"
    "v_mul_f32 v234, v234, v250\n"
    "v_mul_f32 v235, v235, v250\n"
    "v_accvgpr_write_b32 a144, v232\n"
    "v_accvgpr_write_b32 a145, v233\n"
    "v_accvgpr_write_b32 a146, v234\n"
    "v_accvgpr_write_b32 a147, v235\n"
    "v_accvgpr_read_b32 v232, a160\n"
    "v_accvgpr_read_b32 v233, a161\n"
    "v_accvgpr_read_b32 v234, a162\n"
    "v_accvgpr_read_b32 v235, a163\n"
    "v_mul_f32 v232, v232, v250\n"
    "v_mul_f32 v233, v233, v250\n"
    "v_mul_f32 v234, v234, v250\n"
    "v_mul_f32 v235, v235, v250\n"
    "v_accvgpr_write_b32 a160, v232\n"
    "v_accvgpr_write_b32 a161, v233\n"
    "v_accvgpr_write_b32 a162, v234\n"
    "v_accvgpr_write_b32 a163, v235\n"
    "v_accvgpr_read_b32 v232, a176\n"
    "v_accvgpr_read_b32 v233, a177\n"
    "v_accvgpr_read_b32 v234, a178\n"
    "v_accvgpr_read_b32 v235, a179\n"
    "v_mul_f32 v232, v232, v250\n"
    "v_mul_f32 v233, v233, v250\n"
    "v_mul_f32 v234, v234, v250\n"
    "v_mul_f32 v235, v235, v250\n"
    "v_accvgpr_write_b32 a176, v232\n"
    "v_accvgpr_write_b32 a177, v233\n"
    "v_accvgpr_write_b32 a178, v234\n"
    "v_accvgpr_write_b32 a179, v235\n"
    "v_accvgpr_read_b32 v232, a192\n"
    "v_accvgpr_read_b32 v233, a193\n"
    "v_accvgpr_read_b32 v234, a194\n"
    "v_accvgpr_read_b32 v235, a195\n"
    "v_mul_f32 v232, v232, v250\n"
    "v_mul_f32 v233, v233, v250\n"
    "v_mul_f32 v234, v234, v250\n"
    "v_mul_f32 v235, v235, v250\n"
    "v_accvgpr_write_b32 a192, v232\n"
    "v_accvgpr_write_b32 a193, v233\n"
    "v_accvgpr_write_b32 a194, v234\n"
    "v_accvgpr_write_b32 a195, v235\n"
    "v_accvgpr_read_b32 v232, a208\n"
    "v_accvgpr_read_b32 v233, a209\n"
    "v_accvgpr_read_b32 v234, a210\n"
    "v_accvgpr_read_b32 v235, a211\n"
    "v_mul_f32 v232, v232, v250\n"
    "v_mul_f32 v233, v233, v250\n"
    "v_mul_f32 v234, v234, v250\n"
    "v_mul_f32 v235, v235, v250\n"
    "v_accvgpr_write_b32 a208, v232\n"
    "v_accvgpr_write_b32 a209, v233\n"
    "v_accvgpr_write_b32 a210, v234\n"
    "v_accvgpr_write_b32 a211, v235\n"
    "v_accvgpr_read_b32 v232, a224\n"
    "v_accvgpr_read_b32 v233, a225\n"
    "v_accvgpr_read_b32 v234, a226\n"
    "v_accvgpr_read_b32 v235, a227\n"
    "v_mul_f32 v232, v232, v250\n"
    "v_mul_f32 v233, v233, v250\n"
    "v_mul_f32 v234, v234, v250\n"
    "v_mul_f32 v235, v235, v250\n"
    "v_accvgpr_write_b32 a224, v232\n"
    "v_accvgpr_write_b32 a225, v233\n"
    "v_accvgpr_write_b32 a226, v234\n"
    "v_accvgpr_write_b32 a227, v235\n"
    "v_accvgpr_read_b32 v232, a240\n"
    "v_accvgpr_read_b32 v233, a241\n"
    "v_accvgpr_read_b32 v234, a242\n"
    "v_accvgpr_read_b32 v235, a243\n"
    "v_mul_f32 v232, v232, v250\n"
    "v_mul_f32 v233, v233, v250\n"
    "v_mul_f32 v234, v234, v250\n"
    "v_mul_f32 v235, v235, v250\n"
    "v_accvgpr_write_b32 a240, v232\n"
    "v_accvgpr_write_b32 a241, v233\n"
    "v_accvgpr_write_b32 a242, v234\n"
    "v_accvgpr_write_b32 a243, v235\n"
    "v_sub_f32 v232, v128, v249\n"
    "v_sub_f32 v233, v129, v249\n"
    "v_sub_f32 v234, v130, v249\n"
    "v_sub_f32 v235, v131, v249\n"
    "v_exp_f32 v232, v232\n"
    "v_exp_f32 v233, v233\n"
    "v_exp_f32 v234, v234\n"
    "v_exp_f32 v235, v235\n"
    "s_nop 0\n"
    "v_add_f32 v236, v232, v233\n"
    "v_add_f32 v236, v236, v234\n"
    "v_add_f32 v236, v236, v235\n"
    "v_cvt_pk_bf16_f32 v160, v232, v233\n"
    "v_cvt_pk_bf16_f32 v161, v234, v235\n"
    "v_sub_f32 v232, v144, v249\n"
    "v_sub_f32 v233, v145, v249\n"
    "v_sub_f32 v234, v146, v249\n"
    "v_sub_f32 v235, v147, v249\n"
    "v_exp_f32 v232, v232\n"
    "v_exp_f32 v233, v233\n"
    "v_exp_f32 v234, v234\n"
    "v_exp_f32 v235, v235\n"
    "s_nop 0\n"
    "v_add_f32 v236, v236, v232\n"
    "v_add_f32 v236, v236, v233\n"
    "v_add_f32 v236, v236, v234\n"
    "v_add_f32 v236, v236, v235\n"
    "v_cvt_pk_bf16_f32 v162, v232, v233\n"
    "v_cvt_pk_bf16_f32 v163, v234, v235\n"
    "v_max3_f32 v249, v132, v133, v134\n"
    "v_max3_f32 v249, v249, v135, v148\n"
    "v_max3_f32 v249, v249, v149, v150\n"
    "v_max_f32 v249, v249, v151\n"
    "v_mov_b32 v232, v249\n"
    "s_nop 1\n"
    "v_permlane32_swap_b32 v249, v232\n"
    "s_nop 1\n"
    "v_max_f32 v249, v249, v232\n"
    "v_mov_b32 v232, v249\n"
    "s_nop 1\n"
    "v_permlane16_swap_b32 v249, v232\n"
    "s_nop 1\n"
    "v_max_f32 v249, v249, v232\n"
    "v_max_f32 v249, 0, v249\n"
    "v_exp_f32 v250, -v249\n"
    "v_sub_f32 v196, v196, v249\n"
    "v_sub_f32 v197, v197, v249\n"
    "v_sub_f32 v198, v198, v249\n"
    "v_sub_f32 v199, v199, v249\n"
    "v_mul_f32 v241, v241, v250\n"
    "v_accvgpr_read_b32 v232, a4\n"
    "v_accvgpr_read_b32 v233, a5\n"
    "v_accvgpr_read_b32 v234, a6\n"
    "v_accvgpr_read_b32 v235, a7\n"
    "v_mul_f32 v232, v232, v250\n"
    "v_mul_f32 v233, v233, v250\n"
    "v_mul_f32 v234, v234, v250\n"
    "v_mul_f32 v235, v235, v250\n"
    "v_accvgpr_write_b32 a4, v232\n"
    "v_accvgpr_write_b32 a5, v233\n"
    "v_accvgpr_write_b32 a6, v234\n"
    "v_accvgpr_write_b32 a7, v235\n"
    "v_accvgpr_read_b32 v232, a20\n"
    "v_accvgpr_read_b32 v233, a21\n"
    "v_accvgpr_read_b32 v234, a22\n"
    "v_accvgpr_read_b32 v235, a23\n"
    "v_mul_f32 v232, v232, v250\n"
    "v_mul_f32 v233, v233, v250\n"
    "v_mul_f32 v234, v234, v250\n"
    "v_mul_f32 v235, v235, v250\n"
    "v_accvgpr_write_b32 a20, v232\n"
    "v_accvgpr_write_b32 a21, v233\n"
    "v_accvgpr_write_b32 a22, v234\n"
    "v_accvgpr_write_b32 a23, v235\n"
    "v_accvgpr_read_b32 v232, a36\n"
    "v_accvgpr_read_b32 v233, a37\n"
    "v_accvgpr_read_b32 v234, a38\n"
    "v_accvgpr_read_b32 v235, a39\n"
    "v_mul_f32 v232, v232, v250\n"
    "v_mul_f32 v233, v233, v250\n"
    "v_mul_f32 v234, v234, v250\n"
    "v_mul_f32 v235, v235, v250\n"
    "v_accvgpr_write_b32 a36, v232\n"
    "v_accvgpr_write_b32 a37, v233\n"
    "v_accvgpr_write_b32 a38, v234\n"
    "v_accvgpr_write_b32 a39, v235\n"
    "v_accvgpr_read_b32 v232, a52\n"
    "v_accvgpr_read_b32 v233, a53\n"
    "v_accvgpr_read_b32 v234, a54\n"
    "v_accvgpr_read_b32 v235, a55\n"
    "v_mul_f32 v232, v232, v250\n"
    "v_mul_f32 v233, v233, v250\n"
    "v_mul_f32 v234, v234, v250\n"
    "v_mul_f32 v235, v235, v250\n"
    "v_accvgpr_write_b32 a52, v232\n"
    "v_accvgpr_write_b32 a53, v233\n"
    "v_accvgpr_write_b32 a54, v234\n"
    "v_accvgpr_write_b32 a55, v235\n"
    "v_accvgpr_read_b32 v232, a68\n"
    "v_accvgpr_read_b32 v233, a69\n"
    "v_accvgpr_read_b32 v234, a70\n"
    "v_accvgpr_read_b32 v235, a71\n"
    "v_mul_f32 v232, v232, v250\n"
    "v_mul_f32 v233, v233, v250\n"
    "v_mul_f32 v234, v234, v250\n"
    "v_mul_f32 v235, v235, v250\n"
    "v_accvgpr_write_b32 a68, v232\n"
    "v_accvgpr_write_b32 a69, v233\n"
    "v_accvgpr_write_b32 a70, v234\n"
    "v_accvgpr_write_b32 a71, v235\n"
    "v_accvgpr_read_b32 v232, a84\n"
    "v_accvgpr_read_b32 v233, a85\n"
    "v_accvgpr_read_b32 v234, a86\n"
    "v_accvgpr_read_b32 v235, a87\n"
    "v_mul_f32 v232, v232, v250\n"
    "v_mul_f32 v233, v233, v250\n"
    "v_mul_f32 v234, v234, v250\n"
    "v_mul_f32 v235, v235, v250\n"
    "v_accvgpr_write_b32 a84, v232\n"
    "v_accvgpr_write_b32 a85, v233\n"
    "v_accvgpr_write_b32 a86, v234\n"
    "v_accvgpr_write_b32 a87, v235\n"
    "v_accvgpr_read_b32 v232, a100\n"
    "v_accvgpr_read_b32 v233, a101\n"
    "v_accvgpr_read_b32 v234, a102\n"
    "v_accvgpr_read_b32 v235, a103\n"
    "v_mul_f32 v232, v232, v250\n"
    "v_mul_f32 v233, v233, v250\n"
    "v_mul_f32 v234, v234, v250\n"
    "v_mul_f32 v235, v235, v250\n"
    "v_accvgpr_write_b32 a100, v232\n"
    "v_accvgpr_write_b32 a101, v233\n"
    "v_accvgpr_write_b32 a102, v234\n"
    "v_accvgpr_write_b32 a103, v235\n"
    "v_accvgpr_read_b32 v232, a116\n"
    "v_accvgpr_read_b32 v233, a117\n"
    "v_accvgpr_read_b32 v234, a118\n"
    "v_accvgpr_read_b32 v235, a119\n"
    "v_mul_f32 v232, v232, v250\n"
    "v_mul_f32 v233, v233, v250\n"
    "v_mul_f32 v234, v234, v250\n"
    "v_mul_f32 v235, v235, v250\n"
    "v_accvgpr_write_b32 a116, v232\n"
    "v_accvgpr_write_b32 a117, v233\n"
    "v_accvgpr_write_b32 a118, v234\n"
    "v_accvgpr_write_b32 a119, v235\n"
    "v_accvgpr_read_b32 v232, a132\n"
    "v_accvgpr_read_b32 v233, a133\n"
    "v_accvgpr_read_b32 v234, a134\n"
    "v_accvgpr_read_b32 v235, a135\n"
    "v_mul_f32 v232, v232, v250\n"
    "v_mul_f32 v233, v233, v250\n"
    "v_mul_f32 v234, v234, v250\n"
    "v_mul_f32 v235, v235, v250\n"
    "v_accvgpr_write_b32 a132, v232\n"
    "v_accvgpr_write_b32 a133, v233\n"
    "v_accvgpr_write_b32 a134, v234\n"
    "v_accvgpr_write_b32 a135, v235\n"
    "v_accvgpr_read_b32 v232, a148\n"
    "v_accvgpr_read_b32 v233, a149\n"
    "v_accvgpr_read_b32 v234, a150\n"
    "v_accvgpr_read_b32 v235, a151\n"
    "v_mul_f32 v232, v232, v250\n"
    "v_mul_f32 v233, v233, v250\n"
    "v_mul_f32 v234, v234, v250\n"
    "v_mul_f32 v235, v235, v250\n"
    "v_accvgpr_write_b32 a148, v232\n"
    "v_accvgpr_write_b32 a149, v233\n"
    "v_accvgpr_write_b32 a150, v234\n"
    "v_accvgpr_write_b32 a151, v235\n"
    "v_accvgpr_read_b32 v232, a164\n"
    "v_accvgpr_read_b32 v233, a165\n"
    "v_accvgpr_read_b32 v234, a166\n"
    "v_accvgpr_read_b32 v235, a167\n"
    "v_mul_f32 v232, v232, v250\n"
    "v_mul_f32 v233, v233, v250\n"
    "v_mul_f32 v234, v234, v250\n"
    "v_mul_f32 v235, v235, v250\n"
    "v_accvgpr_write_b32 a164, v232\n"
    "v_accvgpr_write_b32 a165, v233\n"
    "v_accvgpr_write_b32 a166, v234\n"
    "v_accvgpr_write_b32 a167, v235\n"
    "v_accvgpr_read_b32 v232, a180\n"
    "v_accvgpr_read_b32 v233, a181\n"
    "v_accvgpr_read_b32 v234, a182\n"
    "v_accvgpr_read_b32 v235, a183\n"
    "v_mul_f32 v232, v232, v250\n"
    "v_mul_f32 v233, v233, v250\n"
    "v_mul_f32 v234, v234, v250\n"
    "v_mul_f32 v235, v235, v250\n"
    "v_accvgpr_write_b32 a180, v232\n"
    "v_accvgpr_write_b32 a181, v233\n"
    "v_accvgpr_write_b32 a182, v234\n"
    "v_accvgpr_write_b32 a183, v235\n"
    "v_accvgpr_read_b32 v232, a196\n"
    "v_accvgpr_read_b32 v233, a197\n"
    "v_accvgpr_read_b32 v234, a198\n"
    "v_accvgpr_read_b32 v235, a199\n"
    "v_mul_f32 v232, v232, v250\n"
    "v_mul_f32 v233, v233, v250\n"
    "v_mul_f32 v234, v234, v250\n"
    "v_mul_f32 v235, v235, v250\n"
    "v_accvgpr_write_b32 a196, v232\n"
    "v_accvgpr_write_b32 a197, v233\n"
    "v_accvgpr_write_b32 a198, v234\n"
    "v_accvgpr_write_b32 a199, v235\n"
    "v_accvgpr_read_b32 v232, a212\n"
    "v_accvgpr_read_b32 v233, a213\n"
    "v_accvgpr_read_b32 v234, a214\n"
    "v_accvgpr_read_b32 v235, a215\n"
    "v_mul_f32 v232, v232, v250\n"
    "v_mul_f32 v233, v233, v250\n"
    "v_mul_f32 v234, v234, v250\n"
    "v_mul_f32 v235, v235, v250\n"
    "v_accvgpr_write_b32 a212, v232\n"
    "v_accvgpr_write_b32 a213, v233\n"
    "v_accvgpr_write_b32 a214, v234\n"
    "v_accvgpr_write_b32 a215, v235\n"
    "v_accvgpr_read_b32 v232, a228\n"
    "v_accvgpr_read_b32 v233, a229\n"
    "v_accvgpr_read_b32 v234, a230\n"
    "v_accvgpr_read_b32 v235, a231\n"
    "v_mul_f32 v232, v232, v250\n"
    "v_mul_f32 v233, v233, v250\n"
    "v_mul_f32 v234, v234, v250\n"
    "v_mul_f32 v235, v235, v250\n"
    "v_accvgpr_write_b32 a228, v232\n"
    "v_accvgpr_write_b32 a229, v233\n"
    "v_accvgpr_write_b32 a230, v234\n"
    "v_accvgpr_write_b32 a231, v235\n"
    "v_accvgpr_read_b32 v232, a244\n"
    "v_accvgpr_read_b32 v233, a245\n"
    "v_accvgpr_read_b32 v234, a246\n"
    "v_accvgpr_read_b32 v235, a247\n"
    "v_mul_f32 v232, v232, v250\n"
    "v_mul_f32 v233, v233, v250\n"
    "v_mul_f32 v234, v234, v250\n"
    "v_mul_f32 v235, v235, v250\n"
    "v_accvgpr_write_b32 a244, v232\n"
    "v_accvgpr_write_b32 a245, v233\n"
    "v_accvgpr_write_b32 a246, v234\n"
    "v_accvgpr_write_b32 a247, v235\n"
    "v_sub_f32 v232, v132, v249\n"
    "v_sub_f32 v233, v133, v249\n"
    "v_sub_f32 v234, v134, v249\n"
    "v_sub_f32 v235, v135, v249\n"
    "v_exp_f32 v232, v232\n"
    "v_exp_f32 v233, v233\n"
    "v_exp_f32 v234, v234\n"
    "v_exp_f32 v235, v235\n"
    "s_nop 0\n"
    "v_add_f32 v237, v232, v233\n"
    "v_add_f32 v237, v237, v234\n"
    "v_add_f32 v237, v237, v235\n"
    "v_cvt_pk_bf16_f32 v164, v232, v233\n"
    "v_cvt_pk_bf16_f32 v165, v234, v235\n"
    "v_sub_f32 v232, v148, v249\n"
    "v_sub_f32 v233, v149, v249\n"
    "v_sub_f32 v234, v150, v249\n"
    "v_sub_f32 v235, v151, v249\n"
    "v_exp_f32 v232, v232\n"
    "v_exp_f32 v233, v233\n"
    "v_exp_f32 v234, v234\n"
    "v_exp_f32 v235, v235\n"
    "s_nop 0\n"
    "v_add_f32 v237, v237, v232\n"
    "v_add_f32 v237, v237, v233\n"
    "v_add_f32 v237, v237, v234\n"
    "v_add_f32 v237, v237, v235\n"
    "v_cvt_pk_bf16_f32 v166, v232, v233\n"
    "v_cvt_pk_bf16_f32 v167, v234, v235\n"
    "v_max3_f32 v249, v136, v137, v138\n"
    "v_max3_f32 v249, v249, v139, v152\n"
    "v_max3_f32 v249, v249, v153, v154\n"
    "v_max_f32 v249, v249, v155\n"
    "v_mov_b32 v232, v249\n"
    "s_nop 1\n"
    "v_permlane32_swap_b32 v249, v232\n"
    "s_nop 1\n"
    "v_max_f32 v249, v249, v232\n"
    "v_mov_b32 v232, v249\n"
    "s_nop 1\n"
    "v_permlane16_swap_b32 v249, v232\n"
    "s_nop 1\n"
    "v_max_f32 v249, v249, v232\n"
    "v_max_f32 v249, 0, v249\n"
    "v_exp_f32 v250, -v249\n"
    "v_sub_f32 v200, v200, v249\n"
    "v_sub_f32 v201, v201, v249\n"
    "v_sub_f32 v202, v202, v249\n"
    "v_sub_f32 v203, v203, v249\n"
    "v_mul_f32 v242, v242, v250\n"
    "v_accvgpr_read_b32 v232, a8\n"
    "v_accvgpr_read_b32 v233, a9\n"
    "v_accvgpr_read_b32 v234, a10\n"
    "v_accvgpr_read_b32 v235, a11\n"
    "v_mul_f32 v232, v232, v250\n"
    "v_mul_f32 v233, v233, v250\n"
    "v_mul_f32 v234, v234, v250\n"
    "v_mul_f32 v235, v235, v250\n"
    "v_accvgpr_write_b32 a8, v232\n"
    "v_accvgpr_write_b32 a9, v233\n"
    "v_accvgpr_write_b32 a10, v234\n"
    "v_accvgpr_write_b32 a11, v235\n"
    "v_accvgpr_read_b32 v232, a24\n"
    "v_accvgpr_read_b32 v233, a25\n"
    "v_accvgpr_read_b32 v234, a26\n"
    "v_accvgpr_read_b32 v235, a27\n"
    "v_mul_f32 v232, v232, v250\n"
    "v_mul_f32 v233, v233, v250\n"
    "v_mul_f32 v234, v234, v250\n"
    "v_mul_f32 v235, v235, v250\n"
    "v_accvgpr_write_b32 a24, v232\n"
    "v_accvgpr_write_b32 a25, v233\n"
    "v_accvgpr_write_b32 a26, v234\n"
    "v_accvgpr_write_b32 a27, v235\n"
    "v_accvgpr_read_b32 v232, a40\n"
    "v_accvgpr_read_b32 v233, a41\n"
    "v_accvgpr_read_b32 v234, a42\n"
    "v_accvgpr_read_b32 v235, a43\n"
    "v_mul_f32 v232, v232, v250\n"
    "v_mul_f32 v233, v233, v250\n"
    "v_mul_f32 v234, v234, v250\n"
    "v_mul_f32 v235, v235, v250\n"
    "v_accvgpr_write_b32 a40, v232\n"
    "v_accvgpr_write_b32 a41, v233\n"
    "v_accvgpr_write_b32 a42, v234\n"
    "v_accvgpr_write_b32 a43, v235\n"
    "v_accvgpr_read_b32 v232, a56\n"
    "v_accvgpr_read_b32 v233, a57\n"
    "v_accvgpr_read_b32 v234, a58\n"
    "v_accvgpr_read_b32 v235, a59\n"
    "v_mul_f32 v232, v232, v250\n"
    "v_mul_f32 v233, v233, v250\n"
    "v_mul_f32 v234, v234, v250\n"
    "v_mul_f32 v235, v235, v250\n"
    "v_accvgpr_write_b32 a56, v232\n"
    "v_accvgpr_write_b32 a57, v233\n"
    "v_accvgpr_write_b32 a58, v234\n"
    "v_accvgpr_write_b32 a59, v235\n"
    "v_accvgpr_read_b32 v232, a72\n"
    "v_accvgpr_read_b32 v233, a73\n"
    "v_accvgpr_read_b32 v234, a74\n"
    "v_accvgpr_read_b32 v235, a75\n"
    "v_mul_f32 v232, v232, v250\n"
    "v_mul_f32 v233, v233, v250\n"
    "v_mul_f32 v234, v234, v250\n"
    "v_mul_f32 v235, v235, v250\n"
    "v_accvgpr_write_b32 a72, v232\n"
    "v_accvgpr_write_b32 a73, v233\n"
    "v_accvgpr_write_b32 a74, v234\n"
    "v_accvgpr_write_b32 a75, v235\n"
    "v_accvgpr_read_b32 v232, a88\n"
    "v_accvgpr_read_b32 v233, a89\n"
    "v_accvgpr_read_b32 v234, a90\n"
    "v_accvgpr_read_b32 v235, a91\n"
    "v_mul_f32 v232, v232, v250\n"
    "v_mul_f32 v233, v233, v250\n"
    "v_mul_f32 v234, v234, v250\n"
    "v_mul_f32 v235, v235, v250\n"
    "v_accvgpr_write_b32 a88, v232\n"
    "v_accvgpr_write_b32 a89, v233\n"
    "v_accvgpr_write_b32 a90, v234\n"
    "v_accvgpr_write_b32 a91, v235\n"
    "v_accvgpr_read_b32 v232, a104\n"
    "v_accvgpr_read_b32 v233, a105\n"
    "v_accvgpr_read_b32 v234, a106\n"
    "v_accvgpr_read_b32 v235, a107\n"
    "v_mul_f32 v232, v232, v250\n"
    "v_mul_f32 v233, v233, v250\n"
    "v_mul_f32 v234, v234, v250\n"
    "v_mul_f32 v235, v235, v250\n"
    "v_accvgpr_write_b32 a104, v232\n"
    "v_accvgpr_write_b32 a105, v233\n"
    "v_accvgpr_write_b32 a106, v234\n"
    "v_accvgpr_write_b32 a107, v235\n"
    "v_accvgpr_read_b32 v232, a120\n"
    "v_accvgpr_read_b32 v233, a121\n"
    "v_accvgpr_read_b32 v234, a122\n"
    "v_accvgpr_read_b32 v235, a123\n"
    "v_mul_f32 v232, v232, v250\n"
    "v_mul_f32 v233, v233, v250\n"
    "v_mul_f32 v234, v234, v250\n"
    "v_mul_f32 v235, v235, v250\n"
    "v_accvgpr_write_b32 a120, v232\n"
    "v_accvgpr_write_b32 a121, v233\n"
    "v_accvgpr_write_b32 a122, v234\n"
    "v_accvgpr_write_b32 a123, v235\n"
    "v_accvgpr_read_b32 v232, a136\n"
    "v_accvgpr_read_b32 v233, a137\n"
    "v_accvgpr_read_b32 v234, a138\n"
    "v_accvgpr_read_b32 v235, a139\n"
    "v_mul_f32 v232, v232, v250\n"
    "v_mul_f32 v233, v233, v250\n"
    "v_mul_f32 v234, v234, v250\n"
    "v_mul_f32 v235, v235, v250\n"
    "v_accvgpr_write_b32 a136, v232\n"
    "v_accvgpr_write_b32 a137, v233\n"
    "v_accvgpr_write_b32 a138, v234\n"
    "v_accvgpr_write_b32 a139, v235\n"
    "v_accvgpr_read_b32 v232, a152\n"
    "v_accvgpr_read_b32 v233, a153\n"
    "v_accvgpr_read_b32 v234, a154\n"
    "v_accvgpr_read_b32 v235, a155\n"
    "v_mul_f32 v232, v232, v250\n"
    "v_mul_f32 v233, v233, v250\n"
    "v_mul_f32 v234, v234, v250\n"
    "v_mul_f32 v235, v235, v250\n"
    "v_accvgpr_write_b32 a152, v232\n"
    "v_accvgpr_write_b32 a153, v233\n"
    "v_accvgpr_write_b32 a154, v234\n"
    "v_accvgpr_write_b32 a155, v235\n"
    "v_accvgpr_read_b32 v232, a168\n"
    "v_accvgpr_read_b32 v233, a169\n"
    "v_accvgpr_read_b32 v234, a170\n"
    "v_accvgpr_read_b32 v235, a171\n"
    "v_mul_f32 v232, v232, v250\n"
    "v_mul_f32 v233, v233, v250\n"
    "v_mul_f32 v234, v234, v250\n"
    "v_mul_f32 v235, v235, v250\n"
    "v_accvgpr_write_b32 a168, v232\n"
    "v_accvgpr_write_b32 a169, v233\n"
    "v_accvgpr_write_b32 a170, v234\n"
    "v_accvgpr_write_b32 a171, v235\n"
    "v_accvgpr_read_b32 v232, a184\n"
    "v_accvgpr_read_b32 v233, a185\n"
    "v_accvgpr_read_b32 v234, a186\n"
    "v_accvgpr_read_b32 v235, a187\n"
    "v_mul_f32 v232, v232, v250\n"
    "v_mul_f32 v233, v233, v250\n"
    "v_mul_f32 v234, v234, v250\n"
    "v_mul_f32 v235, v235, v250\n"
    "v_accvgpr_write_b32 a184, v232\n"
    "v_accvgpr_write_b32 a185, v233\n"
    "v_accvgpr_write_b32 a186, v234\n"
    "v_accvgpr_write_b32 a187, v235\n"
    "v_accvgpr_read_b32 v232, a200\n"
    "v_accvgpr_read_b32 v233, a201\n"
    "v_accvgpr_read_b32 v234, a202\n"
    "v_accvgpr_read_b32 v235, a203\n"
    "v_mul_f32 v232, v232, v250\n"
    "v_mul_f32 v233, v233, v250\n"
    "v_mul_f32 v234, v234, v250\n"
    "v_mul_f32 v235, v235, v250\n"
    "v_accvgpr_write_b32 a200, v232\n"
    "v_accvgpr_write_b32 a201, v233\n"
    "v_accvgpr_write_b32 a202, v234\n"
    "v_accvgpr_write_b32 a203, v235\n"
    "v_accvgpr_read_b32 v232, a216\n"
    "v_accvgpr_read_b32 v233, a217\n"
    "v_accvgpr_read_b32 v234, a218\n"
    "v_accvgpr_read_b32 v235, a219\n"
    "v_mul_f32 v232, v232, v250\n"
    "v_mul_f32 v233, v233, v250\n"
    "v_mul_f32 v234, v234, v250\n"
    "v_mul_f32 v235, v235, v250\n"
    "v_accvgpr_write_b32 a216, v232\n"
    "v_accvgpr_write_b32 a217, v233\n"
    "v_accvgpr_write_b32 a218, v234\n"
    "v_accvgpr_write_b32 a219, v235\n"
    "v_accvgpr_read_b32 v232, a232\n"
    "v_accvgpr_read_b32 v233, a233\n"
    "v_accvgpr_read_b32 v234, a234\n"
    "v_accvgpr_read_b32 v235, a235\n"
    "v_mul_f32 v232, v232, v250\n"
    "v_mul_f32 v233, v233, v250\n"
    "v_mul_f32 v234, v234, v250\n"
    "v_mul_f32 v235, v235, v250\n"
    "v_accvgpr_write_b32 a232, v232\n"
    "v_accvgpr_write_b32 a233, v233\n"
    "v_accvgpr_write_b32 a234, v234\n"
    "v_accvgpr_write_b32 a235, v235\n"
    "v_accvgpr_read_b32 v232, a248\n"
    "v_accvgpr_read_b32 v233, a249\n"
    "v_accvgpr_read_b32 v234, a250\n"
    "v_accvgpr_read_b32 v235, a251\n"
    "v_mul_f32 v232, v232, v250\n"
    "v_mul_f32 v233, v233, v250\n"
    "v_mul_f32 v234, v234, v250\n"
    "v_mul_f32 v235, v235, v250\n"
    "v_accvgpr_write_b32 a248, v232\n"
    "v_accvgpr_write_b32 a249, v233\n"
    "v_accvgpr_write_b32 a250, v234\n"
    "v_accvgpr_write_b32 a251, v235\n"
    "v_sub_f32 v232, v136, v249\n"
    "v_sub_f32 v233, v137, v249\n"
    "v_sub_f32 v234, v138, v249\n"
    "v_sub_f32 v235, v139, v249\n"
    "v_exp_f32 v232, v232\n"
    "v_exp_f32 v233, v233\n"
    "v_exp_f32 v234, v234\n"
    "v_exp_f32 v235, v235\n"
    "s_nop 0\n"
    "v_add_f32 v238, v232, v233\n"
    "v_add_f32 v238, v238, v234\n"
    "v_add_f32 v238, v238, v235\n"
    "v_cvt_pk_bf16_f32 v168, v232, v233\n"
    "v_cvt_pk_bf16_f32 v169, v234, v235\n"
    "v_sub_f32 v232, v152, v249\n"
    "v_sub_f32 v233, v153, v249\n"
    "v_sub_f32 v234, v154, v249\n"
    "v_sub_f32 v235, v155, v249\n"
    "v_exp_f32 v232, v232\n"
    "v_exp_f32 v233, v233\n"
    "v_exp_f32 v234, v234\n"
    "v_exp_f32 v235, v235\n"
    "s_nop 0\n"
    "v_add_f32 v238, v238, v232\n"
    "v_add_f32 v238, v238, v233\n"
    "v_add_f32 v238, v238, v234\n"
    "v_add_f32 v238, v238, v235\n"
    "v_cvt_pk_bf16_f32 v170, v232, v233\n"
    "v_cvt_pk_bf16_f32 v171, v234, v235\n"
    "v_max3_f32 v249, v140, v141, v142\n"
    "v_max3_f32 v249, v249, v143, v156\n"
    "v_max3_f32 v249, v249, v157, v158\n"
    "v_max_f32 v249, v249, v159\n"
    "v_mov_b32 v232, v249\n"
    "s_nop 1\n"
    "v_permlane32_swap_b32 v249, v232\n"
    "s_nop 1\n"
    "v_max_f32 v249, v249, v232\n"
    "v_mov_b32 v232, v249\n"
    "s_nop 1\n"
    "v_permlane16_swap_b32 v249, v232\n"
    "s_nop 1\n"
    "v_max_f32 v249, v249, v232\n"
    "v_max_f32 v249, 0, v249\n"
    "v_exp_f32 v250, -v249\n"
    "v_sub_f32 v204, v204, v249\n"
    "v_sub_f32 v205, v205, v249\n"
    "v_sub_f32 v206, v206, v249\n"
    "v_sub_f32 v207, v207, v249\n"
    "v_mul_f32 v243, v243, v250\n"
    "v_accvgpr_read_b32 v232, a12\n"
    "v_accvgpr_read_b32 v233, a13\n"
    "v_accvgpr_read_b32 v234, a14\n"
    "v_accvgpr_read_b32 v235, a15\n"
    "v_mul_f32 v232, v232, v250\n"
    "v_mul_f32 v233, v233, v250\n"
    "v_mul_f32 v234, v234, v250\n"
    "v_mul_f32 v235, v235, v250\n"
    "v_accvgpr_write_b32 a12, v232\n"
    "v_accvgpr_write_b32 a13, v233\n"
    "v_accvgpr_write_b32 a14, v234\n"
    "v_accvgpr_write_b32 a15, v235\n"
    "v_accvgpr_read_b32 v232, a28\n"
    "v_accvgpr_read_b32 v233, a29\n"
    "v_accvgpr_read_b32 v234, a30\n"
    "v_accvgpr_read_b32 v235, a31\n"
    "v_mul_f32 v232, v232, v250\n"
    "v_mul_f32 v233, v233, v250\n"
    "v_mul_f32 v234, v234, v250\n"
    "v_mul_f32 v235, v235, v250\n"
    "v_accvgpr_write_b32 a28, v232\n"
    "v_accvgpr_write_b32 a29, v233\n"
    "v_accvgpr_write_b32 a30, v234\n"
    "v_accvgpr_write_b32 a31, v235\n"
    "v_accvgpr_read_b32 v232, a44\n"
    "v_accvgpr_read_b32 v233, a45\n"
    "v_accvgpr_read_b32 v234, a46\n"
    "v_accvgpr_read_b32 v235, a47\n"
    "v_mul_f32 v232, v232, v250\n"
    "v_mul_f32 v233, v233, v250\n"
    "v_mul_f32 v234, v234, v250\n"
    "v_mul_f32 v235, v235, v250\n"
    "v_accvgpr_write_b32 a44, v232\n"
    "v_accvgpr_write_b32 a45, v233\n"
    "v_accvgpr_write_b32 a46, v234\n"
    "v_accvgpr_write_b32 a47, v235\n"
    "v_accvgpr_read_b32 v232, a60\n"
    "v_accvgpr_read_b32 v233, a61\n"
    "v_accvgpr_read_b32 v234, a62\n"
    "v_accvgpr_read_b32 v235, a63\n"
    "v_mul_f32 v232, v232, v250\n"
    "v_mul_f32 v233, v233, v250\n"
    "v_mul_f32 v234, v234, v250\n"
    "v_mul_f32 v235, v235, v250\n"
    "v_accvgpr_write_b32 a60, v232\n"
    "v_accvgpr_write_b32 a61, v233\n"
    "v_accvgpr_write_b32 a62, v234\n"
    "v_accvgpr_write_b32 a63, v235\n"
    "v_accvgpr_read_b32 v232, a76\n"
    "v_accvgpr_read_b32 v233, a77\n"
    "v_accvgpr_read_b32 v234, a78\n"
    "v_accvgpr_read_b32 v235, a79\n"
    "v_mul_f32 v232, v232, v250\n"
    "v_mul_f32 v233, v233, v250\n"
    "v_mul_f32 v234, v234, v250\n"
    "v_mul_f32 v235, v235, v250\n"
    "v_accvgpr_write_b32 a76, v232\n"
    "v_accvgpr_write_b32 a77, v233\n"
    "v_accvgpr_write_b32 a78, v234\n"
    "v_accvgpr_write_b32 a79, v235\n"
    "v_accvgpr_read_b32 v232, a92\n"
    "v_accvgpr_read_b32 v233, a93\n"
    "v_accvgpr_read_b32 v234, a94\n"
    "v_accvgpr_read_b32 v235, a95\n"
    "v_mul_f32 v232, v232, v250\n"
    "v_mul_f32 v233, v233, v250\n"
    "v_mul_f32 v234, v234, v250\n"
    "v_mul_f32 v235, v235, v250\n"
    "v_accvgpr_write_b32 a92, v232\n"
    "v_accvgpr_write_b32 a93, v233\n"
    "v_accvgpr_write_b32 a94, v234\n"
    "v_accvgpr_write_b32 a95, v235\n"
    "v_accvgpr_read_b32 v232, a108\n"
    "v_accvgpr_read_b32 v233, a109\n"
    "v_accvgpr_read_b32 v234, a110\n"
    "v_accvgpr_read_b32 v235, a111\n"
    "v_mul_f32 v232, v232, v250\n"
    "v_mul_f32 v233, v233, v250\n"
    "v_mul_f32 v234, v234, v250\n"
    "v_mul_f32 v235, v235, v250\n"
    "v_accvgpr_write_b32 a108, v232\n"
    "v_accvgpr_write_b32 a109, v233\n"
    "v_accvgpr_write_b32 a110, v234\n"
    "v_accvgpr_write_b32 a111, v235\n"
    "v_accvgpr_read_b32 v232, a124\n"
    "v_accvgpr_read_b32 v233, a125\n"
    "v_accvgpr_read_b32 v234, a126\n"
    "v_accvgpr_read_b32 v235, a127\n"
    "v_mul_f32 v232, v232, v250\n"
    "v_mul_f32 v233, v233, v250\n"
    "v_mul_f32 v234, v234, v250\n"
    "v_mul_f32 v235, v235, v250\n"
    "v_accvgpr_write_b32 a124, v232\n"
    "v_accvgpr_write_b32 a125, v233\n"
    "v_accvgpr_write_b32 a126, v234\n"
    "v_accvgpr_write_b32 a127, v235\n"
    "v_accvgpr_read_b32 v232, a140\n"
    "v_accvgpr_read_b32 v233, a141\n"
    "v_accvgpr_read_b32 v234, a142\n"
    "v_accvgpr_read_b32 v235, a143\n"
    "v_mul_f32 v232, v232, v250\n"
    "v_mul_f32 v233, v233, v250\n"
    "v_mul_f32 v234, v234, v250\n"
    "v_mul_f32 v235, v235, v250\n"
    "v_accvgpr_write_b32 a140, v232\n"
    "v_accvgpr_write_b32 a141, v233\n"
    "v_accvgpr_write_b32 a142, v234\n"
    "v_accvgpr_write_b32 a143, v235\n"
    "v_accvgpr_read_b32 v232, a156\n"
    "v_accvgpr_read_b32 v233, a157\n"
    "v_accvgpr_read_b32 v234, a158\n"
    "v_accvgpr_read_b32 v235, a159\n"
    "v_mul_f32 v232, v232, v250\n"
    "v_mul_f32 v233, v233, v250\n"
    "v_mul_f32 v234, v234, v250\n"
    "v_mul_f32 v235, v235, v250\n"
    "v_accvgpr_write_b32 a156, v232\n"
    "v_accvgpr_write_b32 a157, v233\n"
    "v_accvgpr_write_b32 a158, v234\n"
    "v_accvgpr_write_b32 a159, v235\n"
    "v_accvgpr_read_b32 v232, a172\n"
    "v_accvgpr_read_b32 v233, a173\n"
    "v_accvgpr_read_b32 v234, a174\n"
    "v_accvgpr_read_b32 v235, a175\n"
    "v_mul_f32 v232, v232, v250\n"
    "v_mul_f32 v233, v233, v250\n"
    "v_mul_f32 v234, v234, v250\n"
    "v_mul_f32 v235, v235, v250\n"
    "v_accvgpr_write_b32 a172, v232\n"
    "v_accvgpr_write_b32 a173, v233\n"
    "v_accvgpr_write_b32 a174, v234\n"
    "v_accvgpr_write_b32 a175, v235\n"
    "v_accvgpr_read_b32 v232, a188\n"
    "v_accvgpr_read_b32 v233, a189\n"
    "v_accvgpr_read_b32 v234, a190\n"
    "v_accvgpr_read_b32 v235, a191\n"
    "v_mul_f32 v232, v232, v250\n"
    "v_mul_f32 v233, v233, v250\n"
    "v_mul_f32 v234, v234, v250\n"
    "v_mul_f32 v235, v235, v250\n"
    "v_accvgpr_write_b32 a188, v232\n"
    "v_accvgpr_write_b32 a189, v233\n"
    "v_accvgpr_write_b32 a190, v234\n"
    "v_accvgpr_write_b32 a191, v235\n"
    "v_accvgpr_read_b32 v232, a204\n"
    "v_accvgpr_read_b32 v233, a205\n"
    "v_accvgpr_read_b32 v234, a206\n"
    "v_accvgpr_read_b32 v235, a207\n"
    "v_mul_f32 v232, v232, v250\n"
    "v_mul_f32 v233, v233, v250\n"
    "v_mul_f32 v234, v234, v250\n"
    "v_mul_f32 v235, v235, v250\n"
    "v_accvgpr_write_b32 a204, v232\n"
    "v_accvgpr_write_b32 a205, v233\n"
    "v_accvgpr_write_b32 a206, v234\n"
    "v_accvgpr_write_b32 a207, v235\n"
    "v_accvgpr_read_b32 v232, a220\n"
    "v_accvgpr_read_b32 v233, a221\n"
    "v_accvgpr_read_b32 v234, a222\n"
    "v_accvgpr_read_b32 v235, a223\n"
    "v_mul_f32 v232, v232, v250\n"
    "v_mul_f32 v233, v233, v250\n"
    "v_mul_f32 v234, v234, v250\n"
    "v_mul_f32 v235, v235, v250\n"
    "v_accvgpr_write_b32 a220, v232\n"
    "v_accvgpr_write_b32 a221, v233\n"
    "v_accvgpr_write_b32 a222, v234\n"
    "v_accvgpr_write_b32 a223, v235\n"
    "v_accvgpr_read_b32 v232, a236\n"
    "v_accvgpr_read_b32 v233, a237\n"
    "v_accvgpr_read_b32 v234, a238\n"
    "v_accvgpr_read_b32 v235, a239\n"
    "v_mul_f32 v232, v232, v250\n"
    "v_mul_f32 v233, v233, v250\n"
    "v_mul_f32 v234, v234, v250\n"
    "v_mul_f32 v235, v235, v250\n"
    "v_accvgpr_write_b32 a236, v232\n"
    "v_accvgpr_write_b32 a237, v233\n"
    "v_accvgpr_write_b32 a238, v234\n"
    "v_accvgpr_write_b32 a239, v235\n"
    "v_accvgpr_read_b32 v232, a252\n"
    "v_accvgpr_read_b32 v233, a253\n"
    "v_accvgpr_read_b32 v234, a254\n"
    "v_accvgpr_read_b32 v235, a255\n"
    "v_mul_f32 v232, v232, v250\n"
    "v_mul_f32 v233, v233, v250\n"
    "v_mul_f32 v234, v234, v250\n"
    "v_mul_f32 v235, v235, v250\n"
    "v_accvgpr_write_b32 a252, v232\n"
    "v_accvgpr_write_b32 a253, v233\n"
    "v_accvgpr_write_b32 a254, v234\n"
    "v_accvgpr_write_b32 a255, v235\n"
    "v_sub_f32 v232, v140, v249\n"
    "v_sub_f32 v233, v141, v249\n"
    "v_sub_f32 v234, v142, v249\n"
    "v_sub_f32 v235, v143, v249\n"
    "v_exp_f32 v232, v232\n"
    "v_exp_f32 v233, v233\n"
    "v_exp_f32 v234, v234\n"
    "v_exp_f32 v235, v235\n"
    "s_nop 0\n"
    "v_add_f32 v239, v232, v233\n"
    "v_add_f32 v239, v239, v234\n"
    "v_add_f32 v239, v239, v235\n"
    "v_cvt_pk_bf16_f32 v172, v232, v233\n"
    "v_cvt_pk_bf16_f32 v173, v234, v235\n"
    "v_sub_f32 v232, v156, v249\n"
    "v_sub_f32 v233, v157, v249\n"
    "v_sub_f32 v234, v158, v249\n"
    "v_sub_f32 v235, v159, v249\n"
    "v_exp_f32 v232, v232\n"
    "v_exp_f32 v233, v233\n"
    "v_exp_f32 v234, v234\n"
    "v_exp_f32 v235, v235\n"
    "s_nop 0\n"
    "v_add_f32 v239, v239, v232\n"
    "v_add_f32 v239, v239, v233\n"
    "v_add_f32 v239, v239, v234\n"
    "v_add_f32 v239, v239, v235\n"
    "v_cvt_pk_bf16_f32 v174, v232, v233\n"
    "v_cvt_pk_bf16_f32 v175, v234, v235\n"
    "s_nop 3\n"
    "s_branch RET0_m\n"
    "LATE_m:\n"
    "s_cmp_lg_u32 s54, 4\n"
    "s_cbranch_scc1 LT0_m\n"
    "s_mov_b32 s88, s22\n"
    "s_and_b32 s89, s23, 65535\n"
    "s_mov_b32 s99, s97\n"
    "s_add_u32 s90, s97, 8192\n"
    "s_branch LATERET_m\n"
    "LT0_m:\n"
    "s_cmp_lg_u32 s54, 3\n"
    "s_cbranch_scc1 LT1_m\n"
    "s_add_u32 s99, s99, 8192\n"
    "s_add_u32 s90, s99, 4352\n"
    "s_branch LATERET_m\n"
    "LT1_m:\n"
    "s_cmp_lg_u32 s54, 2\n"
    "s_cbranch_scc1 LT2_m\n"
    "s_mov_b32 s88, s72\n"
    "s_and_b32 s89, s73, 65535\n"
    "s_mov_b32 s99, s96\n"
    "s_add_u32 s90, s96, 3584\n"
    "s_branch LATERET_m\n"
    "LT2_m:\n"
    "s_cmp_lg_u32 s54, 1\n"
    "s_cbranch_scc1 LT3_m\n"
    "s_mov_b32 s88, s18\n"
    "s_mov_b32 s89, s19\n"
    "s_mov_b32 s99, s98\n"
    "s_mov_b32 s90, K1END-K1TAIL\n"
    "s_branch LATERET_m\n"
    "LT3_m:\n"
    "s_cmp_lg_u32 s54, 0\n"
    "s_cbranch_scc1 LT4_m\n"
    "s_mov_b32 s90, 0\n"
    "s_branch LATERET_m\n"
    "LT4_m:\n"
    "s_branch LATERET_m\n"
    "K1EXIT:\n"
    :: "s"(__builtin_amdgcn_kernarg_segment_ptr()), "s"(blockIdx.x), "s"(wid), "s"((unsigned)(size_t)lds)
    : "v0","v1","v2","v3","v4","v5","v6","v7","v8","v9","v10","v11","v12","v13","v14","v15","v16","v17","v18","v19","v20","v21","v22","v23","v24","v25","v26","v27","v28","v29","v30","v31","v32","v33","v34","v35","v36","v37","v38","v39","v40","v41","v42","v43","v44","v45","v46","v47","v48","v49","v50","v51","v52","v53","v54","v55","v56","v57","v58","v59","v60","v61","v62","v63","v64","v65","v66","v67","v68","v69","v70","v71","v72","v73","v74","v75","v76","v77","v78","v79","v80","v81","v82","v83","v84","v85","v86","v87","v88","v89","v90","v91","v92","v93","v94","v95","v96","v97","v98","v99","v100","v101","v102","v103","v104","v105","v106","v107","v108","v109","v110","v111","v112","v113","v114","v115","v116","v117","v118","v119","v120","v121","v122","v123","v124","v125","v126","v127","v128","v129","v130","v131","v132","v133","v134","v135","v136","v137","v138","v139","v140","v141","v142","v143","v144","v145","v146","v147","v148","v149","v150","v151","v152","v153","v154","v155","v156","v157","v158","v159","v160","v161","v162","v163","v164","v165","v166","v167","v168","v169","v170","v171","v172","v173","v174","v175","v176","v177","v178","v179","v180","v181","v182","v183","v184","v185","v186","v187","v188","v189","v190","v191","v192","v193","v194","v195","v196","v197","v198","v199","v200","v201","v202","v203","v204","v205","v206","v207","v208","v209","v210","v211","v212","v213","v214","v215","v216","v217","v218","v219","v220","v221","v222","v223","v224","v225","v226","v227","v228","v229","v230","v231","v232","v233","v234","v235","v236","v237","v238","v239","v240","v241","v242","v243","v244","v245","v246","v247","v248","v249","v250","v251","v252","v253","v254","v255","a0","a1","a2","a3","a4","a5","a6","a7","a8","a9","a10","a11","a12","a13","a14","a15","a16","a17","a18","a19","a20","a21","a22","a23","a24","a25","a26","a27","a28","a29","a30","a31","a32","a33","a34","a35","a36","a37","a38","a39","a40","a41","a42","a43","a44","a45","a46","a47","a48","a49","a50","a51","a52","a53","a54","a55","a56","a57","a58","a59","a60","a61","a62","a63","a64","a65","a66","a67","a68","a69","a70","a71","a72","a73","a74","a75","a76","a77","a78","a79","a80","a81","a82","a83","a84","a85","a86","a87","a88","a89","a90","a91","a92","a93","a94","a95","a96","a97","a98","a99","a100","a101","a102","a103","a104","a105","a106","a107","a108","a109","a110","a111","a112","a113","a114","a115","a116","a117","a118","a119","a120","a121","a122","a123","a124","a125","a126","a127","a128","a129","a130","a131","a132","a133","a134","a135","a136","a137","a138","a139","a140","a141","a142","a143","a144","a145","a146","a147","a148","a149","a150","a151","a152","a153","a154","a155","a156","a157","a158","a159","a160","a161","a162","a163","a164","a165","a166","a167","a168","a169","a170","a171","a172","a173","a174","a175","a176","a177","a178","a179","a180","a181","a182","a183","a184","a185","a186","a187","a188","a189","a190","a191","a192","a193","a194","a195","a196","a197","a198","a199","a200","a201","a202","a203","a204","a205","a206","a207","a208","a209","a210","a211","a212","a213","a214","a215","a216","a217","a218","a219","a220","a221","a222","a223","a224","a225","a226","a227","a228","a229","a230","a231","a232","a233","a234","a235","a236","a237","a238","a239","a240","a241","a242","a243","a244","a245","a246","a247","a248","a249","a250","a251","a252","a253","a254","a255","s8","s9","s10","s11","s12","s13","s14","s15","s16","s17","s18","s19","s20","s21","s22","s23","s24","s25","s26","s27","s28","s29","s30","s31","s36","s37","s38","s39","s40","s41","s42","s43","s44","s45","s46","s47","s48","s49","s50","s51","s52","s53","s54","s55","s56","s57","s58","s59","s60","s61","s62","s63","s64","s65","s66","s67","s68","s69","s70","s71","s72","s73","s74","s75","s76","s77","s78","s79","s80","s81","s82","s83","s84","s85","s86","s87","s88","s89","s90","s91","s92","s93","s94","s95","s96","s97","s98","s99", "vcc", "scc", "memory");
}

extern "C" void kernel_launch(void* const* d_in, const int* in_sizes, int n_in,
                              void* d_out, int out_size, void* d_ws, size_t ws_size,
                              hipStream_t stream) {
  hipLaunchKernelGGL(fused_block_kernel, dim3(256), dim3(256), 0, stream, d_in[0], d_in[1], d_in[2], d_in[3], d_in[4], d_in[6], d_in[7], d_in[8], d_out, d_ws);
}
```
